```python
import torch

def _softmax(x, axis=-1):
    return torch.softmax(x, dim=axis)

def _gelu(x):
    return torch.nn.functional.gelu(x, approximate='tanh')

def _silu(x):
    return torch.nn.functional.silu(x)

def _sigmoid(x):
    return torch.sigmoid(x)

def _layernorm(x, axis=-1, eps=1e-05):
    m = x.mean(dim=axis, keepdim=True)
    v = x.var(dim=axis, keepdim=True, unbiased=False)
    return (x - m) / torch.sqrt(v + eps)

def _scatter_add(tgt, idx, src, axis=0):
    return tgt.clone().index_add_(axis, idx, src)

def _segment_sum(src, seg, n):
    out = torch.zeros((n,) + src.shape[1:], dtype=src.dtype)
    return out.index_add_(0, seg, src)

def _pad(x, pw):
    flat = tuple((v for pair in reversed(pw) for v in pair))
    return torch.nn.functional.pad(x, flat)
kernel_name = 'standalone__conformer_family_zipformer__speech'

def setup_inputs():

    _w = lambda a: (lambda t: t.to(torch.bfloat16) if t.is_floating_point() else t)(torch.as_tensor(a))
    B, T, D, K, H = (2, 4000, 256, 5, 4)
    sc = 0.1
    return {'x': _w((lambda *s: torch.randn(*s, dtype=torch.bfloat16) * 0.8)(B, T, D)), 'w_ff1a': _w((lambda *s: torch.randn(*s, dtype=torch.bfloat16) * 0.8)(D, 4 * D) * sc), 'w_ff1b': _w((lambda *s: torch.randn(*s, dtype=torch.bfloat16) * 0.8)(4 * D, D) * sc), 'w_qkv': _w((lambda *s: torch.randn(*s, dtype=torch.bfloat16) * 0.8)(D, 3 * D) * sc), 'w_o': _w((lambda *s: torch.randn(*s, dtype=torch.bfloat16) * 1.6)(D, D) * sc), 'w_pw1': _w((lambda *s: torch.randn(*s, dtype=torch.bfloat16) * 1.6)(D, 2 * D) * sc), 'dw': _w((lambda *s: torch.randn(*s, dtype=torch.bfloat16) * 0.4)(K, D)), 'w_pw2': _w((lambda *s: torch.randn(*s, dtype=torch.bfloat16) * 0.4)(D, D) * sc), 'w_ff2a': _w((lambda *s: torch.randn(*s, dtype=torch.bfloat16) * 0.8)(D, 4 * D) * sc), 'w_ff2b': _w((lambda *s: torch.randn(*s, dtype=torch.bfloat16) * 0.8)(4 * D, D) * sc)}

def reference(x, w_ff1a, w_ff1b, w_qkv, w_o, w_pw1, dw, w_pw2, w_ff2a, w_ff2b):
    B, T, D = x.shape
    K = dw.shape[0]
    H = 4
    Dh = D // H
    h = _layernorm(x, axis=-1)
    h = _silu(h @ w_ff1a) @ w_ff1b
    x = x + 0.5 * h
    h = _layernorm(x, axis=-1)
    qkv = h @ w_qkv
    q = (lambda x, *d: x.permute(*d))(qkv[..., :D].reshape(B, T, H, Dh), 0, 2, 1, 3)
    k = (lambda x, *d: x.permute(*d))(qkv[..., D:2 * D].reshape(B, T, H, Dh), 0, 2, 1, 3)
    v = (lambda x, *d: x.permute(*d))(qkv[..., 2 * D:].reshape(B, T, H, Dh), 0, 2, 1, 3)
    scale = Dh ** (-0.5)
    scores = torch.einsum('bhqd,bhkd->bhqk', q, k) * scale
    p = _softmax(scores, axis=-1)
    att = torch.einsum('bhqk,bhkd->bhqd', p, v)
    att = (lambda x, *d: x.permute(*d))(att, 0, 2, 1, 3).reshape(B, T, D)
    x = x + att @ w_o
    h = _layernorm(x, axis=-1)
    h = h @ w_pw1
    a, b = (h[..., :D].clone(), h[..., D:].clone())
    h = a * _sigmoid(b)
    pad = K // 2
    hp = _pad(h, ((0, 0), (pad, pad), (0, 0)))
    c = torch.zeros_like(h)
    for i in range(K):
        c = c + hp[:, i:i + T, :] * dw[i][None, None, :]
    x = x + _silu(c) @ w_pw2
    h = _layernorm(x, axis=-1)
    h = _silu(h @ w_ff2a) @ w_ff2b
    x = x + 0.5 * h
    return _layernorm(x, axis=-1)
```

```cpp
#include <hip/hip_runtime.h>
#include <hip/hip_bf16.h>
#include <stdint.h>

typedef short bf16x8 __attribute__((ext_vector_type(8)));
typedef short s16x4 __attribute__((ext_vector_type(4)));
typedef float f32x16 __attribute__((ext_vector_type(16)));
typedef float f32x2 __attribute__((ext_vector_type(2)));
typedef __bf16 bf16x2_t __attribute__((ext_vector_type(2)));
typedef unsigned u32x4 __attribute__((ext_vector_type(4)));
typedef unsigned u32x2 __attribute__((ext_vector_type(2)));
#define LAS __attribute__((address_space(3)))
#define DI __device__ __forceinline__
#define MFMA(a, b, c) __builtin_amdgcn_mfma_f32_32x32x16_bf16((a), (b), (c), 0, 0, 0)

constexpr int TSEQ = 4000, NBLK = 125, NTB = 250;
constexpr size_t WS_X1F = 0;
constexpr size_t WS_QF = 4u << 20;
constexpr size_t WS_KVF = 8u << 20;
constexpr size_t WS_ATTF = 17u << 20;
constexpr float LN_EPS = 1e-5f;
constexpr float LOG2E = 1.4426950408889634f;
constexpr float QSCALE = 0.125f * LOG2E;

DI unsigned pk2(float a, float b) { f32x2 v = {a, b}; return __builtin_bit_cast(unsigned, __builtin_convertvector(v, bf16x2_t)); }
DI float bflo(unsigned u) { return __uint_as_float(u << 16); }
DI float bfhi(unsigned u) { return __uint_as_float(u & 0xffff0000u); }
DI float rbf(float a) { return bflo(pk2(a, a)); }
DI bf16x8 pack8(const f32x16& v, int s) {
  u32x4 p = {pk2(v[8 * s], v[8 * s + 1]), pk2(v[8 * s + 2], v[8 * s + 3]), pk2(v[8 * s + 4], v[8 * s + 5]), pk2(v[8 * s + 6], v[8 * s + 7])};
  return __builtin_bit_cast(bf16x8, p);
}
DI void unpack8(u32x4 p, f32x16& v, int s) {
#pragma unroll
  for (int i = 0; i < 4; ++i) { v[8 * s + 2 * i] = bflo(p[i]); v[8 * s + 2 * i + 1] = bfhi(p[i]); }
}
DI float halfsum(float x) {
  auto r = __builtin_amdgcn_permlane32_swap(__float_as_uint(x), __float_as_uint(x), false, false);
  return __uint_as_float(r[0]) + __uint_as_float(r[1]);
}
DI float halfmax(float x) {
  auto r = __builtin_amdgcn_permlane32_swap(__float_as_uint(x), __float_as_uint(x), false, false);
  return fmaxf(__uint_as_float(r[0]), __uint_as_float(r[1]));
}
DI float sigmoidf_(float x) { return __builtin_amdgcn_rcpf(1.0f + __builtin_amdgcn_exp2f(-LOG2E * x)); }
DI float siluf_(float x) { return x * sigmoidf_(x); }
DI void lds_barrier() { asm volatile("s_waitcnt lgkmcnt(0)\n\ts_barrier" ::: "memory"); }
DI void dma16(unsigned voff, const void* sbase, unsigned lds_addr) {
  unsigned sv;
  asm volatile("s_mov_b32 %0, m0\n\ts_mov_b32 m0, %3\n\ts_nop 0\n\tglobal_load_lds_dwordx4 %1, %2\n\ts_mov_b32 m0, %0"
               : "=&s"(sv) : "v"(voff), "s"(sbase), "s"(lds_addr));
}
typedef short v4i16_t __attribute__((ext_vector_type(4)));
DI s16x4 trread(LAS const char* p) { return __builtin_bit_cast(s16x4, __builtin_amdgcn_ds_read_tr16_b64_v4i16((LAS v4i16_t*)p)); }
DI bf16x8 ldsfrag(LAS const char* p) { return *(LAS const bf16x8*)p; }

constexpr int SLAB = 4096, RING = 6, RINGB = SLAB * RING;
template <class PROG> struct Stream {
  PROG prog;
  const char* sbase; unsigned voff, ld8; int left, panel; unsigned fslot, cslot, ring0; bool done; int lane;
  unsigned rd0, rd1;
  DI void init(unsigned ring_base, int lane_) {
    lane = lane_; ring0 = ring_base; left = 0; panel = 0; fslot = 0; cslot = 0; done = false; sbase = nullptr; voff = 0; ld8 = 0;
    const int h = lane >> 5, cb = (lane >> 4) & 1, q = (lane & 15) >> 2, p = lane & 3;
    rd0 = ring0 + (4 * h + q) * 128 + ((0 ^ (q >> 1)) * 64) + cb * 32 + p * 8;
    rd1 = ring0 + (4 * h + q) * 128 + ((1 ^ (q >> 1)) * 64) + cb * 32 + p * 8;
#pragma unroll
    for (int i = 0; i < RING - 1; ++i) { issue(0); issue(1); }
  }
  DI void issue(int half) {
    if (half == 0 && left == 0 && !done) {
      unsigned ld = 0; done = !prog.get(panel, sbase, ld, left); panel++; ld8 = ld * 8;
      voff = (unsigned)(lane >> 3) * ld + ((unsigned)((lane & 7) ^ (((lane >> 4) & 1) << 2)) << 4);
    }
    if (done) return;
    dma16(voff, sbase + (size_t)(2 * half) * ld8, ring0 + fslot + half * 2048);
    dma16(voff, sbase + (size_t)(2 * half + 1) * ld8, ring0 + fslot + half * 2048 + 1024);
    if (half == 1) { sbase += (size_t)4 * ld8; left--; fslot = (fslot == RINGB - SLAB) ? 0u : fslot + SLAB; }
  }
  DI void wait() {
    if (done) asm volatile("s_waitcnt vmcnt(0) lgkmcnt(0)" ::: "memory");
    else asm volatile("s_waitcnt vmcnt(16) lgkmcnt(0)" ::: "memory");
  }
  DI void next() { cslot = (cslot == RINGB - SLAB) ? 0u : cslot + SLAB; }
  DI bf16x8 afrag(int nt, int ks) const {
    LAS const char* p = (LAS const char*)(uintptr_t)((nt ? rd1 : rd0) + cslot);
    s16x4 lo = trread(p + ks * 2048), hi = trread(p + ks * 2048 + 1024);
    return __builtin_shufflevector(lo, hi, 0, 1, 2, 3, 4, 5, 6, 7);
  }
};

template <int NB, bool SWAP, class S>
DI void gemm_k256(S& st, f32x16 (&acc)[NB][2], const bf16x8 (&bf)[NB][16]) {
#pragma unroll
  for (int sl = 0; sl < 8; ++sl) {
    st.wait();
#pragma unroll
    for (int ks = 0; ks < 2; ++ks) {
      bf16x8 a0 = st.afrag(0, ks), a1 = st.afrag(1, ks);
#pragma unroll
      for (int nb = 0; nb < NB; ++nb) {
        if (SWAP) { acc[nb][0] = MFMA(bf[nb][2 * sl + ks], a0, acc[nb][0]); acc[nb][1] = MFMA(bf[nb][2 * sl + ks], a1, acc[nb][1]); }
        else { acc[nb][0] = MFMA(a0, bf[nb][2 * sl + ks], acc[nb][0]); acc[nb][1] = MFMA(a1, bf[nb][2 * sl + ks], acc[nb][1]); }
      }
      st.issue(ks);
    }
    st.next();
  }
}
template <class S>
DI void gemm_k512_lds(S& st, f32x16 (&acc)[2], LAS const char* actl) {
  for (int sl = 0; sl < 16; ++sl) {
    st.wait();
#pragma unroll
    for (int ks = 0; ks < 2; ++ks) {
      bf16x8 b = ldsfrag(actl + (2 * sl + ks) * 1024);
      bf16x8 a0 = st.afrag(0, ks), a1 = st.afrag(1, ks);
      acc[0] = MFMA(a0, b, acc[0]); acc[1] = MFMA(a1, b, acc[1]);
      st.issue(ks);
    }
    st.next();
  }
}

constexpr int L_RING = 0;
constexpr int L_ACT = 4 * RINGB;
constexpr int L_STAT = L_ACT + 32768;
constexpr int L_G = L_STAT + 2048;
constexpr int GROW = 136, GSZ = 36 * GROW;
constexpr int L_DW = L_G + 4 * GSZ;
constexpr int L_TOTAL = L_DW + 2560;

template <int NB>
DI void ln_stats(const f32x16 (&v)[NB][2], LAS char* lds, int w, int lane, float (&mean)[NB], float (&rstd)[NB]) {
  const int r = lane & 31;
#pragma unroll
  for (int nb = 0; nb < NB; ++nb) {
    float s = 0.f;
#pragma unroll
    for (int i = 0; i < 16; ++i) s += v[nb][0][i] + v[nb][1][i];
    const float mw = halfsum(s) * (1.0f / 64.0f);
    float m2 = 0.f;
#pragma unroll
    for (int i = 0; i < 16; ++i) { float d0 = v[nb][0][i] - mw, d1 = v[nb][1][i] - mw; m2 += d0 * d0 + d1 * d1; }
    m2 = halfsum(m2);
    if (lane < 32) *(LAS f32x2*)(lds + L_STAT + ((nb * 4 + w) * 32 + r) * 8) = (f32x2){mw, m2};
  }
  lds_barrier();
#pragma unroll
  for (int nb = 0; nb < NB; ++nb) {
    f32x2 s0 = *(LAS f32x2*)(lds + L_STAT + ((nb * 4 + 0) * 32 + r) * 8), s1 = *(LAS f32x2*)(lds + L_STAT + ((nb * 4 + 1) * 32 + r) * 8);
    f32x2 s2 = *(LAS f32x2*)(lds + L_STAT + ((nb * 4 + 2) * 32 + r) * 8), s3 = *(LAS f32x2*)(lds + L_STAT + ((nb * 4 + 3) * 32 + r) * 8);
    const float m = 0.25f * (s0[0] + s1[0] + s2[0] + s3[0]);
    const float e0 = s0[0] - m, e1 = s1[0] - m, e2 = s2[0] - m, e3 = s3[0] - m;
    const float M2 = s0[1] + s1[1] + s2[1] + s3[1] + 64.0f * (e0 * e0 + e1 * e1 + e2 * e2 + e3 * e3);
    mean[nb] = m; rstd[nb] = __builtin_amdgcn_rsqf(M2 * (1.0f / 256.0f) + LN_EPS);
  }
}
template <int NB>
DI void ln_to_frags(const f32x16 (&v)[NB][2], LAS char* lds, int w, int lane, bf16x8 (&out)[NB][16]) {
  float mean[NB], rstd[NB];
  ln_stats<NB>(v, lds, w, lane, mean, rstd);
#pragma unroll
  for (int nb = 0; nb < NB; ++nb)
#pragma unroll
    for (int nt = 0; nt < 2; ++nt) {
      f32x16 n;
#pragma unroll
      for (int i = 0; i < 16; ++i) n[i] = (v[nb][nt][i] - mean[nb]) * rstd[nb];
#pragma unroll
      for (int s = 0; s < 2; ++s) *(LAS bf16x8*)(lds + L_ACT + (nb * 16 + 4 * w + 2 * nt + s) * 1024 + lane * 16) = pack8(n, s);
    }
  lds_barrier();
#pragma unroll
  for (int nb = 0; nb < NB; ++nb)
#pragma unroll
    for (int f = 0; f < 16; ++f) out[nb][f] = ldsfrag(lds + L_ACT + (nb * 16 + f) * 1024 + lane * 16);
}

template <class S>
DI void ff_block(S& st, const bf16x8 (&xf)[1][16], LAS char* lds, int w, int lane, f32x16 (&out)[2]) {
  out[0] = f32x16{}; out[1] = f32x16{};
  for (int half = 0; half < 2; ++half) {
    f32x16 hacc[2][1][2];
#pragma unroll
    for (int c = 0; c < 2; ++c) {
      hacc[c][0][0] = f32x16{}; hacc[c][0][1] = f32x16{};
      gemm_k256<1, false>(st, hacc[c], xf);
    }
    lds_barrier();
#pragma unroll
    for (int c = 0; c < 2; ++c)
#pragma unroll
      for (int nt = 0; nt < 2; ++nt) {
        f32x16 hv;
#pragma unroll
        for (int i = 0; i < 16; ++i) hv[i] = siluf_(hacc[c][0][nt][i]);
#pragma unroll
        for (int s = 0; s < 2; ++s) *(LAS bf16x8*)(lds + L_ACT + (4 * (w + 4 * c) + 2 * nt + s) * 1024 + lane * 16) = pack8(hv, s);
      }
    lds_barrier();
    gemm_k512_lds(st, out, (LAS const char*)(lds + L_ACT + lane * 16));
  }
}

struct FFProg {
  const char *wa, *wb; int w;
  DI void get(int p, const char*& base, unsigned& ld, int& n) const {
    if (p == 2 || p == 5) { base = wb + (size_t)(p == 5 ? 512 : 0) * 512 + w * 128; ld = 512; n = 16; }
    else { const int c = (p < 2) ? p : p - 1; base = wa + (size_t)(w + 4 * c) * 128; ld = 2048; n = 8; }
  }
};
struct Prog1 {
  FFProg ff; const char* wqkv;
  DI bool get(int p, const char*& base, unsigned& ld, int& n) const {
    if (p < 6) { ff.get(p, base, ld, n); return true; }
    if (p < 9) { base = wqkv + (size_t)(p - 6) * 512 + ff.w * 128; ld = 1536; n = 8; return true; }
    return false;
  }
};
struct Prog3 {
  FFProg ff; const char *wo, *pw1, *pw2;
  DI bool get(int p, const char*& base, unsigned& ld, int& n) const {
    if (p == 0) { base = wo + ff.w * 128; ld = 512; n = 8; return true; }
    if (p < 3) { base = pw1 + (size_t)(p - 1) * 512 + ff.w * 128; ld = 1024; n = 8; return true; }
    if (p == 3) { base = pw2 + ff.w * 128; ld = 512; n = 8; return true; }
    if (p < 10) { ff.get(p - 4, base, ld, n); return true; }
    return false;
  }
};

__global__ __launch_bounds__(256, 1) void k1_kernel(const char* __restrict__ x, const char* __restrict__ w1a, const char* __restrict__ w1b,
                                                    const char* __restrict__ wqkv, char* __restrict__ ws) {
  extern __shared__ __attribute__((aligned(16))) char smem[];
  LAS char* lds = (LAS char*)smem;
  const int lane = threadIdx.x & 63, w = __builtin_amdgcn_readfirstlane(threadIdx.x >> 6), r = lane & 31, h = lane >> 5;
  const int tb = blockIdx.x;
  f32x16 xv[1][2];
  {
    const char* xrow = x + (size_t)(tb * 32 + r) * 512 + (w * 64 + 4 * h) * 2;
#pragma unroll
    for (int nt = 0; nt < 2; ++nt)
#pragma unroll
      for (int g = 0; g < 4; ++g) {
        u32x2 u = *(const u32x2*)(xrow + (nt * 32 + 8 * g) * 2);
        xv[0][nt][4 * g] = bflo(u[0]); xv[0][nt][4 * g + 1] = bfhi(u[0]); xv[0][nt][4 * g + 2] = bflo(u[1]); xv[0][nt][4 * g + 3] = bfhi(u[1]);
      }
  }
  Stream<Prog1> st;
  st.prog.ff.wa = w1a; st.prog.ff.wb = w1b; st.prog.ff.w = w; st.prog.wqkv = wqkv;
  st.init((unsigned)(uintptr_t)(lds + L_RING + w * RINGB), lane);

  bf16x8 xf[1][16];
  ln_to_frags<1>(xv, lds, w, lane, xf);
  f32x16 acc[2];
  ff_block(st, xf, lds, w, lane, acc);
#pragma unroll
  for (int nt = 0; nt < 2; ++nt) {
#pragma unroll
    for (int i = 0; i < 16; ++i) xv[0][nt][i] = rbf(xv[0][nt][i] + 0.5f * rbf(acc[nt][i]));
#pragma unroll
    for (int s = 0; s < 2; ++s) *(bf16x8*)(ws + WS_X1F + ((size_t)tb * 16 + 4 * w + 2 * nt + s) * 1024 + lane * 16) = pack8(xv[0][nt], s);
  }
  ln_to_frags<1>(xv, lds, w, lane, xf);
  const int b = tb / NBLK, blk = tb - b * NBLK, bh = b * 4 + w;
  {
    f32x16 a[1][2]; a[0][0] = f32x16{}; a[0][1] = f32x16{};
    gemm_k256<1, false>(st, a, xf);
#pragma unroll
    for (int nt = 0; nt < 2; ++nt) {
#pragma unroll
      for (int i = 0; i < 16; ++i) a[0][nt][i] *= QSCALE;
#pragma unroll
      for (int s = 0; s < 2; ++s) *(bf16x8*)(ws + WS_QF + ((size_t)(bh * NBLK + blk) * 4 + 2 * nt + s) * 1024 + lane * 16) = pack8(a[0][nt], s);
    }
  }
  {
    f32x16 a[1][2]; a[0][0] = f32x16{}; a[0][1] = f32x16{};
    gemm_k256<1, false>(st, a, xf);
#pragma unroll
    for (int nt = 0; nt < 2; ++nt)
#pragma unroll
      for (int s = 0; s < 2; ++s) *(bf16x8*)(ws + WS_KVF + ((size_t)(bh * NBLK + blk) * 8 + 2 * nt + s) * 1024 + lane * 16) = pack8(a[0][nt], s);
  }
  {
    f32x16 a[1][2]; a[0][0] = f32x16{}; a[0][1] = f32x16{};
    gemm_k256<1, true>(st, a, xf);
#pragma unroll
    for (int nt = 0; nt < 2; ++nt)
#pragma unroll
      for (int s = 0; s < 2; ++s) *(bf16x8*)(ws + WS_KVF + ((size_t)(bh * NBLK + blk) * 8 + 4 + 2 * nt + s) * 1024 + lane * 16) = pack8(a[0][nt], s);
  }
}

constexpr int A_STAGE = 32768, A_NST = 32, A_LDS = 3 * A_STAGE;
__global__ __launch_bounds__(512, 2) void attn_kernel(char* __restrict__ ws) {
  extern __shared__ __attribute__((aligned(16))) char smem[];
  LAS char* lds = (LAS char*)smem;
  const int lane = threadIdx.x & 63, w = __builtin_amdgcn_readfirstlane(threadIdx.x >> 6);
  const int bh = blockIdx.x & 7, qg = blockIdx.x >> 3, qi = w & 3, kg = w >> 2;
  const int qb_raw = qg * 4 + qi, qb = qb_raw < NBLK ? qb_raw : NBLK - 1;
  const char* kv = ws + WS_KVF + (size_t)bh * NBLK * 8192;
  const unsigned lds0 = (unsigned)(uintptr_t)lds;
  const unsigned voff = w * 4096 + lane * 16;
  auto stage_dma = [&](int st, int buf) {
#pragma unroll
    for (int p = 0; p < 4; ++p) dma16(voff, kv + (size_t)st * A_STAGE + p * 1024, lds0 + buf * A_STAGE + w * 4096 + p * 1024);
  };
  stage_dma(0, 0); stage_dma(1, 1);
  bf16x8 qf[4];
#pragma unroll
  for (int f = 0; f < 4; ++f) qf[f] = *(const bf16x8*)(ws + WS_QF + ((size_t)(bh * NBLK + qb) * 4 + f) * 1024 + lane * 16);
  f32x16 o0 = f32x16{}, o1 = f32x16{}, negr;
  float ref = 0.f, lsum = 0.f;
#pragma unroll
  for (int i = 0; i < 16; ++i) negr[i] = 0.f;
  bool first = true;
  int buf = 0;
  for (int st = 0; st < A_NST; ++st) {
    if (st + 1 < A_NST) asm volatile("s_waitcnt vmcnt(4) lgkmcnt(0)\n\ts_barrier" ::: "memory");
    else asm volatile("s_waitcnt vmcnt(0) lgkmcnt(0)\n\ts_barrier" ::: "memory");
    if (st + 2 < A_NST) stage_dma(st + 2, buf == 0 ? 2 : buf - 1);
    const int nblk = (st < A_NST - 1) ? 2 : (kg == 0 ? 1 : 0);
    for (int bi = 0; bi < nblk; ++bi) {
      LAS const char* kp = lds + buf * A_STAGE + (kg * 2 + bi) * 8192 + lane * 16;
      f32x16 s = MFMA(ldsfrag(kp), qf[0], negr);
      s = MFMA(ldsfrag(kp + 1024), qf[1], s);
      s = MFMA(ldsfrag(kp + 2048), qf[2], s);
      s = MFMA(ldsfrag(kp + 3072), qf[3], s);
      f32x16 p;
#pragma unroll
      for (int i = 0; i < 16; ++i) p[i] = __builtin_amdgcn_exp2f(s[i]);
      u32x4 pa = __builtin_bit_cast(u32x4, pack8(p, 0)), pb = __builtin_bit_cast(u32x4, pack8(p, 1));
      const unsigned orall = (pa[0] | pa[1] | pa[2]) | (pa[3] | pb[0] | pb[1]) | (pb[2] | pb[3]);
      if (__builtin_expect(first || __any((orall & 0x40004000u) != 0u), 0)) {
        f32x16 z = f32x16{};
        f32x16 sr = MFMA(ldsfrag(kp), qf[0], z);
        sr = MFMA(ldsfrag(kp + 1024), qf[1], sr);
        sr = MFMA(ldsfrag(kp + 2048), qf[2], sr);
        sr = MFMA(ldsfrag(kp + 3072), qf[3], sr);
        float mx = sr[0];
#pragma unroll
        for (int i = 1; i < 16; ++i) mx = fmaxf(mx, sr[i]);
        mx = halfmax(mx);
        float nref = ref, alpha = 1.0f;
        if (first) { nref = mx + 7.0f; alpha = 0.f; }
        else if (mx - ref >= 0.5f) { nref = mx + 7.0f; alpha = __builtin_amdgcn_exp2f(ref - nref); }
        ref = nref; lsum *= alpha;
#pragma unroll
        for (int i = 0; i < 16; ++i) { o0[i] *= alpha; o1[i] *= alpha; negr[i] = -nref; p[i] = __builtin_amdgcn_exp2f(sr[i] - nref); }
        pa = __builtin_bit_cast(u32x4, pack8(p, 0)); pb = __builtin_bit_cast(u32x4, pack8(p, 1));
        first = false;
      }
      float ls = 0.f;
#pragma unroll
      for (int i = 0; i < 16; ++i) ls += p[i];
      lsum += ls;
      const bf16x8 p0 = __builtin_bit_cast(bf16x8, pa), p1 = __builtin_bit_cast(bf16x8, pb);
      o0 = MFMA(ldsfrag(kp + 4096), p0, o0);
      o1 = MFMA(ldsfrag(kp + 6144), p0, o1);
      o0 = MFMA(ldsfrag(kp + 5120), p1, o0);
      o1 = MFMA(ldsfrag(kp + 7168), p1, o1);
    }
    buf = (buf == 2) ? 0 : buf + 1;
  }
  lsum = halfsum(lsum);
  lds_barrier();
  typedef float f32x4 __attribute__((ext_vector_type(4)));
  LAS char* cb = lds + qi * 9216 + lane * 16;
  if (kg == 1) {
#pragma unroll
    for (int g = 0; g < 4; ++g) {
      *(LAS f32x4*)(cb + g * 1024) = (f32x4){o0[4 * g], o0[4 * g + 1], o0[4 * g + 2], o0[4 * g + 3]};
      *(LAS f32x4*)(cb + 4096 + g * 1024) = (f32x4){o1[4 * g], o1[4 * g + 1], o1[4 * g + 2], o1[4 * g + 3]};
    }
    *(LAS f32x2*)(lds + qi * 9216 + 8192 + lane * 8) = (f32x2){ref, lsum};
  }
  lds_barrier();
  if (kg == 0 && qb_raw < NBLK) {
    const f32x2 rl = *(LAS f32x2*)(lds + qi * 9216 + 8192 + lane * 8);
    const float m = fmaxf(ref, rl[0]);
    const float a0 = __builtin_amdgcn_exp2f(ref - m), a1 = __builtin_amdgcn_exp2f(rl[0] - m);
    const float inv = 1.0f / (lsum * a0 + rl[1] * a1);
    const float c0 = a0 * inv, c1 = a1 * inv;
#pragma unroll
    for (int g = 0; g < 4; ++g) {
      const f32x4 u0 = *(LAS f32x4*)(cb + g * 1024), u1 = *(LAS f32x4*)(cb + 4096 + g * 1024);
#pragma unroll
      for (int j = 0; j < 4; ++j) { o0[4 * g + j] = o0[4 * g + j] * c0 + u0[j] * c1; o1[4 * g + j] = o1[4 * g + j] * c0 + u1[j] * c1; }
    }
    char* dst = ws + WS_ATTF + ((size_t)((bh >> 2) * NBLK + qb) * 16 + (bh & 3) * 4) * 1024 + lane * 16;
    *(bf16x8*)(dst) = pack8(o0, 0); *(bf16x8*)(dst + 1024) = pack8(o0, 1);
    *(bf16x8*)(dst + 2048) = pack8(o1, 0); *(bf16x8*)(dst + 3072) = pack8(o1, 1);
  }
}

__global__ __launch_bounds__(256, 1) void k3_kernel(const char* __restrict__ wo, const char* __restrict__ pw1, const char* __restrict__ dw,
                                                    const char* __restrict__ pw2, const char* __restrict__ w2a, const char* __restrict__ w2b,
                                                    const char* __restrict__ ws, char* __restrict__ out) {
  extern __shared__ __attribute__((aligned(16))) char smem[];
  LAS char* lds = (LAS char*)smem;
  const int lane = threadIdx.x & 63, w = __builtin_amdgcn_readfirstlane(threadIdx.x >> 6), r = lane & 31, h = lane >> 5;
  const int tb = blockIdx.x, b = tb / NBLK, t0 = (tb - b * NBLK) * 32;
  const int hc = r & 3, ht = (hc < 2) ? t0 - 2 + hc : t0 + 30 + hc;
  const bool hvalid = ht >= 0 && ht < TSEQ;
  const int hg = b * TSEQ + (ht < 0 ? 0 : (ht >= TSEQ ? TSEQ - 1 : ht));
  const size_t hoff = (size_t)(hg >> 5) * 16384 + ((hg & 31) + 32 * h) * 16;
  const size_t moff = (size_t)tb * 16384 + lane * 16;
  bf16x8 bf[2][16];
#pragma unroll
  for (int f = 0; f < 16; ++f) {
    bf[0][f] = *(const bf16x8*)(ws + WS_ATTF + moff + f * 1024);
    bf[1][f] = *(const bf16x8*)(ws + WS_ATTF + hoff + f * 1024);
  }
  f32x16 xv[2][2];
#pragma unroll
  for (int nt = 0; nt < 2; ++nt)
#pragma unroll
    for (int s = 0; s < 2; ++s) {
      unpack8(*(const u32x4*)(ws + WS_X1F + moff + (4 * w + 2 * nt + s) * 1024), xv[0][nt], s);
      unpack8(*(const u32x4*)(ws + WS_X1F + hoff + (4 * w + 2 * nt + s) * 1024), xv[1][nt], s);
    }
  if (threadIdx.x < 160) *(LAS u32x4*)(lds + L_DW + threadIdx.x * 16) = *(const u32x4*)(dw + threadIdx.x * 16);
  Stream<Prog3> st;
  st.prog.ff.wa = w2a; st.prog.ff.wb = w2b; st.prog.ff.w = w; st.prog.wo = wo; st.prog.pw1 = pw1; st.prog.pw2 = pw2;
  st.init((unsigned)(uintptr_t)(lds + L_RING + w * RINGB), lane);
  {
    f32x16 a[2][2];
#pragma unroll
    for (int nb = 0; nb < 2; ++nb) { a[nb][0] = f32x16{}; a[nb][1] = f32x16{}; }
    gemm_k256<2, false>(st, a, bf);
#pragma unroll
    for (int nb = 0; nb < 2; ++nb)
#pragma unroll
      for (int nt = 0; nt < 2; ++nt)
#pragma unroll
        for (int i = 0; i < 16; ++i) xv[nb][nt][i] = rbf(xv[nb][nt][i] + rbf(a[nb][nt][i]));
  }
  ln_to_frags<2>(xv, lds, w, lane, bf);
  {
    f32x16 a[2][2], g[2][2];
#pragma unroll
    for (int nb = 0; nb < 2; ++nb) { a[nb][0] = f32x16{}; a[nb][1] = f32x16{}; g[nb][0] = f32x16{}; g[nb][1] = f32x16{}; }
    gemm_k256<2, false>(st, a, bf);
    gemm_k256<2, false>(st, g, bf);
    LAS char* G = lds + L_G + w * GSZ;
#pragma unroll
    for (int nb = 0; nb < 2; ++nb) {
      const int row = nb == 0 ? r + 2 : (hc < 2 ? hc : 32 + hc);
      const bool wr = nb == 0 || r < 4;
      const float keep = (nb == 0 || hvalid) ? 1.0f : 0.0f;
#pragma unroll
      for (int nt = 0; nt < 2; ++nt)
#pragma unroll
        for (int q = 0; q < 4; ++q) {
          float e[4];
#pragma unroll
          for (int j = 0; j < 4; ++j) e[j] = keep * rbf(a[nb][nt][4 * q + j]) * rbf(sigmoidf_(rbf(g[nb][nt][4 * q + j])));
          if (wr) *(LAS u32x2*)(G + row * GROW + (nt * 32 + 8 * q + 4 * h) * 2) = (u32x2){pk2(e[0], e[1]), pk2(e[2], e[3])};
        }
    }
    lds_barrier();
#pragma unroll
    for (int nt = 0; nt < 2; ++nt) {
      f32x16 c;
#pragma unroll
      for (int q = 0; q < 4; ++q) {
        float e[4] = {0.f, 0.f, 0.f, 0.f};
#pragma unroll
        for (int i = 0; i < 5; ++i) {
          const u32x2 gv = *(LAS u32x2*)(G + (r + i) * GROW + (nt * 32 + 8 * q + 4 * h) * 2);
          const u32x2 dv = *(LAS u32x2*)(lds + L_DW + i * 512 + (w * 64 + nt * 32 + 8 * q + 4 * h) * 2);
          e[0] += bflo(gv[0]) * bflo(dv[0]); e[1] += bfhi(gv[0]) * bfhi(dv[0]);
          e[2] += bflo(gv[1]) * bflo(dv[1]); e[3] += bfhi(gv[1]) * bfhi(dv[1]);
        }
#pragma unroll
        for (int j = 0; j < 4; ++j) c[4 * q + j] = siluf_(e[j]);
      }
#pragma unroll
      for (int s = 0; s < 2; ++s) *(LAS bf16x8*)(lds + L_ACT + (4 * w + 2 * nt + s) * 1024 + lane * 16) = pack8(c, s);
    }
    lds_barrier();
  }
  bf16x8 xf[1][16];
#pragma unroll
  for (int f = 0; f < 16; ++f) xf[0][f] = ldsfrag(lds + L_ACT + f * 1024 + lane * 16);
  f32x16 x3[1][2];
  {
    f32x16 a[1][2]; a[0][0] = f32x16{}; a[0][1] = f32x16{};
    gemm_k256<1, false>(st, a, xf);
#pragma unroll
    for (int nt = 0; nt < 2; ++nt)
#pragma unroll
      for (int i = 0; i < 16; ++i) x3[0][nt][i] = rbf(xv[0][nt][i] + rbf(a[0][nt][i]));
  }
  ln_to_frags<1>(x3, lds, w, lane, xf);
  f32x16 acc[2];
  ff_block(st, xf, lds, w, lane, acc);
#pragma unroll
  for (int nt = 0; nt < 2; ++nt)
#pragma unroll
    for (int i = 0; i < 16; ++i) x3[0][nt][i] = rbf(x3[0][nt][i] + 0.5f * rbf(acc[nt][i]));
  float mean[1], rstd[1];
  ln_stats<1>(x3, lds, w, lane, mean, rstd);
  char* orow = out + (size_t)(tb * 32 + r) * 512 + (w * 64 + 4 * h) * 2;
#pragma unroll
  for (int nt = 0; nt < 2; ++nt)
#pragma unroll
    for (int q = 0; q < 4; ++q) {
      float e[4];
#pragma unroll
      for (int j = 0; j < 4; ++j) e[j] = (x3[0][nt][4 * q + j] - mean[0]) * rstd[0];
      *(u32x2*)(orow + (nt * 32 + 8 * q) * 2) = (u32x2){pk2(e[0], e[1]), pk2(e[2], e[3])};
    }
}

extern "C" void kernel_launch(void* const* d_in, const int* in_sizes, int n_in, void* d_out, int out_size, void* d_ws, size_t ws_size,
                              hipStream_t stream) {
  const char* x = (const char*)d_in[0];
  const char* w1a = (const char*)d_in[1];
  const char* w1b = (const char*)d_in[2];
  const char* wqkv = (const char*)d_in[3];
  const char* wo = (const char*)d_in[4];
  const char* pw1 = (const char*)d_in[5];
  const char* dw = (const char*)d_in[6];
  const char* pw2 = (const char*)d_in[7];
  const char* w2a = (const char*)d_in[8];
  const char* w2b = (const char*)d_in[9];
  char* ws = (char*)d_ws;
  (void)hipFuncSetAttribute((const void*)k1_kernel, hipFuncAttributeMaxDynamicSharedMemorySize, L_TOTAL);
  (void)hipFuncSetAttribute((const void*)attn_kernel, hipFuncAttributeMaxDynamicSharedMemorySize, A_LDS);
  (void)hipFuncSetAttribute((const void*)k3_kernel, hipFuncAttributeMaxDynamicSharedMemorySize, L_TOTAL);
  k1_kernel<<<NTB, 256, L_TOTAL, stream>>>(x, w1a, w1b, wqkv, ws);
  attn_kernel<<<256, 512, A_LDS, stream>>>(ws);
  k3_kernel<<<NTB, 256, L_TOTAL, stream>>>(wo, pw1, dw, pw2, w2a, w2b, ws, (char*)d_out);
}
```

```cpp
#include <hip/hip_runtime.h>
#include <hip/hip_bf16.h>
#include <stdint.h>

typedef short bf16x8 __attribute__((ext_vector_type(8)));
typedef short s16x4 __attribute__((ext_vector_type(4)));
typedef float f32x16 __attribute__((ext_vector_type(16)));
typedef float f32x2 __attribute__((ext_vector_type(2)));
typedef __bf16 bf16x2_t __attribute__((ext_vector_type(2)));
typedef unsigned u32x4 __attribute__((ext_vector_type(4)));
typedef unsigned u32x2 __attribute__((ext_vector_type(2)));
#define LAS __attribute__((address_space(3)))
#define DI __device__ __forceinline__
#define MFMA(a, b, c) __builtin_amdgcn_mfma_f32_32x32x16_bf16((a), (b), (c), 0, 0, 0)

constexpr int TSEQ = 4000, NBLK = 125, NTB = 250;
constexpr size_t WS_X1F = 0;
constexpr size_t WS_QF = 4u << 20;
constexpr size_t WS_KVF = 8u << 20;
constexpr size_t WS_ATTF = 17u << 20;
constexpr float LN_EPS = 1e-5f;
constexpr float LOG2E = 1.4426950408889634f;
constexpr float QSCALE = 0.125f * LOG2E;

DI unsigned pk2(float a, float b) { f32x2 v = {a, b}; return __builtin_bit_cast(unsigned, __builtin_convertvector(v, bf16x2_t)); }
DI float bflo(unsigned u) { return __uint_as_float(u << 16); }
DI float bfhi(unsigned u) { return __uint_as_float(u & 0xffff0000u); }
DI float rbf(float a) { return bflo(pk2(a, a)); }
DI bf16x8 pack8(const f32x16& v, int s) {
  u32x4 p = {pk2(v[8 * s], v[8 * s + 1]), pk2(v[8 * s + 2], v[8 * s + 3]), pk2(v[8 * s + 4], v[8 * s + 5]), pk2(v[8 * s + 6], v[8 * s + 7])};
  return __builtin_bit_cast(bf16x8, p);
}
DI void unpack8(u32x4 p, f32x16& v, int s) {
#pragma unroll
  for (int i = 0; i < 4; ++i) { v[8 * s + 2 * i] = bflo(p[i]); v[8 * s + 2 * i + 1] = bfhi(p[i]); }
}
DI float halfsum(float x) {
  auto r = __builtin_amdgcn_permlane32_swap(__float_as_uint(x), __float_as_uint(x), false, false);
  return __uint_as_float(r[0]) + __uint_as_float(r[1]);
}
DI float halfmax(float x) {
  auto r = __builtin_amdgcn_permlane32_swap(__float_as_uint(x), __float_as_uint(x), false, false);
  return fmaxf(__uint_as_float(r[0]), __uint_as_float(r[1]));
}
DI float sigmoidf_(float x) { return __builtin_amdgcn_rcpf(1.0f + __builtin_amdgcn_exp2f(-LOG2E * x)); }
DI float siluf_(float x) { return x * sigmoidf_(x); }
DI void lds_barrier() { asm volatile("s_waitcnt lgkmcnt(0)\n\ts_barrier" ::: "memory"); }
DI void dma16(unsigned voff, const void* sbase, unsigned lds_addr) {
  unsigned sv;
  asm volatile("s_mov_b32 %0, m0\n\ts_mov_b32 m0, %3\n\ts_nop 0\n\tglobal_load_lds_dwordx4 %1, %2\n\ts_mov_b32 m0, %0"
               : "=&s"(sv) : "v"(voff), "s"(sbase), "s"(lds_addr));
}
typedef short v4i16_t __attribute__((ext_vector_type(4)));
DI s16x4 trread(LAS const char* p) { return __builtin_bit_cast(s16x4, __builtin_amdgcn_ds_read_tr16_b64_v4i16((LAS v4i16_t*)p)); }
DI bf16x8 ldsfrag(LAS const char* p) { return *(LAS const bf16x8*)p; }

constexpr int SLAB = 4096, RING = 6, RINGB = SLAB * RING;
struct Panel { const char* sbase; unsigned ld32; unsigned v0, v1, v2, v3; };
DI Panel make_panel(const char* base, unsigned ld, int lane) {
  Panel p; p.sbase = base; p.ld32 = ld * 32;
  p.v0 = (unsigned)(lane >> 3) * ld + ((unsigned)((lane & 7) ^ (((lane >> 4) & 1) << 2)) << 4);
  p.v1 = p.v0 + (ld * 8 - 1024); p.v2 = p.v0 + (ld * 16 - 2048); p.v3 = p.v0 + (ld * 24 - 3072);
  return p;
}
struct Stream {
  unsigned fslot, cslot, ring0, rd0, rd1;
  bf16x8 cur[4];
  DI void init(unsigned ring_base, int lane) {
    ring0 = ring_base; fslot = 0; cslot = 0;
    const int h = lane >> 5, cb = (lane >> 4) & 1, q = (lane & 15) >> 2, p = lane & 3;
    rd0 = ring0 + (4 * h + q) * 128 + ((0 ^ (q >> 1)) * 64) + cb * 32 + p * 8;
    rd1 = ring0 + (4 * h + q) * 128 + ((1 ^ (q >> 1)) * 64) + cb * 32 + p * 8;
  }
  DI void issue(Panel& p) {
    asm volatile("s_mov_b32 m0, %5\n\ts_nop 0\n\t"
                 "global_load_lds_dwordx4 %0, %4\n\tglobal_load_lds_dwordx4 %1, %4 offset:1024\n\t"
                 "global_load_lds_dwordx4 %2, %4 offset:2048\n\tglobal_load_lds_dwordx4 %3, %4 offset:3072"
                 :: "v"(p.v0), "v"(p.v1), "v"(p.v2), "v"(p.v3), "s"(p.sbase), "s"(ring0 + fslot));
    p.sbase += p.ld32; fslot = (fslot == RINGB - SLAB) ? 0u : fslot + SLAB;
  }
  template <int N> DI void wait() { asm volatile("s_waitcnt vmcnt(%0) lgkmcnt(0)" :: "n"(N) : "memory"); }
  DI void read(bf16x8 (&f)[4]) {
    LAS const char* p0 = (LAS const char*)(uintptr_t)(rd0 + cslot);
    LAS const char* p1 = (LAS const char*)(uintptr_t)(rd1 + cslot);
#pragma unroll
    for (int ks = 0; ks < 2; ++ks) {
      s16x4 l0 = trread(p0 + ks * 2048), h0 = trread(p0 + ks * 2048 + 1024);
      s16x4 l1 = trread(p1 + ks * 2048), h1 = trread(p1 + ks * 2048 + 1024);
      f[2 * ks] = __builtin_shufflevector(l0, h0, 0, 1, 2, 3, 4, 5, 6, 7);
      f[2 * ks + 1] = __builtin_shufflevector(l1, h1, 0, 1, 2, 3, 4, 5, 6, 7);
    }
    cslot = (cslot == RINGB - SLAB) ? 0u : cslot + SLAB;
  }
  DI void start(Panel& p) {
#pragma unroll
    for (int i = 0; i < RING; ++i) issue(p);
    wait<4 * (RING - 1)>(); read(cur);
  }
};
template <int NB, bool SWAP, bool LAST>
DI void gemm_k256(Stream& st, Panel& pc, Panel& pn, f32x16 (&acc)[NB][2], const bf16x8 (&bf)[NB][16]) {
#pragma unroll
  for (int sl = 0; sl < 8; ++sl) {
    bf16x8 nxt[4];
    if (!(LAST && sl == 7)) {
      if (!LAST || sl < 3) st.template wait<4 * (RING - 2)>();
      else if (sl == 3) st.template wait<12>();
      else if (sl == 4) st.template wait<8>();
      else if (sl == 5) st.template wait<4>();
      else st.template wait<0>();
      st.read(nxt);
      if (sl + RING < 8) st.issue(pc); else if (!LAST) st.issue(pn);
    }
#pragma unroll
    for (int ks = 0; ks < 2; ++ks)
#pragma unroll
      for (int nb = 0; nb < NB; ++nb)
#pragma unroll
        for (int nt = 0; nt < 2; ++nt)
          acc[nb][nt] = SWAP ? MFMA(bf[nb][2 * sl + ks], st.cur[2 * ks + nt], acc[nb][nt]) : MFMA(st.cur[2 * ks + nt], bf[nb][2 * sl + ks], acc[nb][nt]);
    if (!(LAST && sl == 7)) {
#pragma unroll
      for (int i = 0; i < 4; ++i) st.cur[i] = nxt[i];
    }
  }
}
template <bool LAST>
DI void gemm_k512_lds(Stream& st, Panel& pc, Panel& pn, f32x16 (&acc)[2], LAS const char* actl) {
  auto step = [&](int sl, auto waitfn, int mode) {
    bf16x8 nxt[4];
    bf16x8 b0 = ldsfrag(actl + (2 * sl) * 1024), b1 = ldsfrag(actl + (2 * sl + 1) * 1024);
    if (mode != 3) { waitfn(); st.read(nxt); if (mode == 0) st.issue(pc); else if (mode == 1) st.issue(pn); }
    acc[0] = MFMA(st.cur[0], b0, acc[0]); acc[1] = MFMA(st.cur[1], b0, acc[1]);
    acc[0] = MFMA(st.cur[2], b1, acc[0]); acc[1] = MFMA(st.cur[3], b1, acc[1]);
    if (mode != 3) {
#pragma unroll
      for (int i = 0; i < 4; ++i) st.cur[i] = nxt[i];
    }
  };
  auto w16 = [&]() { st.template wait<4 * (RING - 2)>(); };
#pragma unroll 2
  for (int sl = 0; sl < 16 - RING; ++sl) step(sl, w16, 0);
  if (!LAST) {
#pragma unroll 2
    for (int sl = 16 - RING; sl < 16; ++sl) step(sl, w16, 1);
  } else {
    step(10, w16, 2);
    step(11, [&]() { st.template wait<12>(); }, 2);
    step(12, [&]() { st.template wait<8>(); }, 2);
    step(13, [&]() { st.template wait<4>(); }, 2);
    step(14, [&]() { st.template wait<0>(); }, 2);
    step(15, w16, 3);
  }
}

constexpr int L_RING = 0;
constexpr int L_ACT = 4 * RINGB;
constexpr int L_STAT = L_ACT + 32768;
constexpr int L_G = L_STAT + 2048;
constexpr int GROW = 136, GSZ = 36 * GROW;
constexpr int L_DW = L_G + 4 * GSZ;
constexpr int L_TOTAL = L_DW + 2560;

template <int NB>
DI void ln_stats(const f32x16 (&v)[NB][2], LAS char* lds, int w, int lane, float (&mean)[NB], float (&rstd)[NB]) {
  const int r = lane & 31;
#pragma unroll
  for (int nb = 0; nb < NB; ++nb) {
    float s = 0.f;
#pragma unroll
    for (int i = 0; i < 16; ++i) s += v[nb][0][i] + v[nb][1][i];
    const float mw = halfsum(s) * (1.0f / 64.0f);
    float m2 = 0.f;
#pragma unroll
    for (int i = 0; i < 16; ++i) { float d0 = v[nb][0][i] - mw, d1 = v[nb][1][i] - mw; m2 += d0 * d0 + d1 * d1; }
    m2 = halfsum(m2);
    if (lane < 32) *(LAS f32x2*)(lds + L_STAT + ((nb * 4 + w) * 32 + r) * 8) = (f32x2){mw, m2};
  }
  lds_barrier();
#pragma unroll
  for (int nb = 0; nb < NB; ++nb) {
    f32x2 s0 = *(LAS f32x2*)(lds + L_STAT + ((nb * 4 + 0) * 32 + r) * 8), s1 = *(LAS f32x2*)(lds + L_STAT + ((nb * 4 + 1) * 32 + r) * 8);
    f32x2 s2 = *(LAS f32x2*)(lds + L_STAT + ((nb * 4 + 2) * 32 + r) * 8), s3 = *(LAS f32x2*)(lds + L_STAT + ((nb * 4 + 3) * 32 + r) * 8);
    const float m = 0.25f * (s0[0] + s1[0] + s2[0] + s3[0]);
    const float e0 = s0[0] - m, e1 = s1[0] - m, e2 = s2[0] - m, e3 = s3[0] - m;
    const float M2 = s0[1] + s1[1] + s2[1] + s3[1] + 64.0f * (e0 * e0 + e1 * e1 + e2 * e2 + e3 * e3);
    mean[nb] = m; rstd[nb] = __builtin_amdgcn_rsqf(M2 * (1.0f / 256.0f) + LN_EPS);
  }
}
template <int NB>
DI void ln_to_frags(const f32x16 (&v)[NB][2], LAS char* lds, int w, int lane, bf16x8 (&out)[NB][16]) {
  float mean[NB], rstd[NB];
  ln_stats<NB>(v, lds, w, lane, mean, rstd);
#pragma unroll
  for (int nb = 0; nb < NB; ++nb)
#pragma unroll
    for (int nt = 0; nt < 2; ++nt) {
      f32x16 n;
#pragma unroll
      for (int i = 0; i < 16; ++i) n[i] = (v[nb][nt][i] - mean[nb]) * rstd[nb];
#pragma unroll
      for (int s = 0; s < 2; ++s) *(LAS bf16x8*)(lds + L_ACT + (nb * 16 + 4 * w + 2 * nt + s) * 1024 + lane * 16) = pack8(n, s);
    }
  lds_barrier();
#pragma unroll
  for (int nb = 0; nb < NB; ++nb)
#pragma unroll
    for (int f = 0; f < 16; ++f) out[nb][f] = ldsfrag(lds + L_ACT + (nb * 16 + f) * 1024 + lane * 16);
}

template <bool LAST>
DI void ff_block(Stream& st, Panel& pa0, const char* wa, const char* wb, Panel& pnext, const bf16x8 (&xf)[1][16], LAS char* lds, int w, int lane, f32x16 (&out)[2]) {
  out[0] = f32x16{}; out[1] = f32x16{};
  Panel pa = pa0;
#pragma unroll
  for (int half = 0; half < 2; ++half) {
    f32x16 hacc[2][1][2];
    Panel pa1 = make_panel(wa + (size_t)(w + 8 * half + 4) * 128, 2048, lane);
    Panel pb = make_panel(wb + (size_t)half * 512 * 512 + w * 128, 512, lane);
    hacc[0][0][0] = f32x16{}; hacc[0][0][1] = f32x16{}; hacc[1][0][0] = f32x16{}; hacc[1][0][1] = f32x16{};
    gemm_k256<1, false, false>(st, pa, pa1, hacc[0], xf);
    gemm_k256<1, false, false>(st, pa1, pb, hacc[1], xf);
    lds_barrier();
#pragma unroll
    for (int c = 0; c < 2; ++c)
#pragma unroll
      for (int nt = 0; nt < 2; ++nt) {
        f32x16 hv;
#pragma unroll
        for (int i = 0; i < 16; ++i) hv[i] = siluf_(hacc[c][0][nt][i]);
#pragma unroll
        for (int s = 0; s < 2; ++s) *(LAS bf16x8*)(lds + L_ACT + (4 * (w + 4 * c) + 2 * nt + s) * 1024 + lane * 16) = pack8(hv, s);
      }
    lds_barrier();
    if (half == 0) {
      pa = make_panel(wa + (size_t)(w + 8) * 128, 2048, lane);
      gemm_k512_lds<false>(st, pb, pa, out, (LAS const char*)(lds + L_ACT + lane * 16));
    } else {
      gemm_k512_lds<LAST>(st, pb, pnext, out, (LAS const char*)(lds + L_ACT + lane * 16));
    }
  }
}

__global__ __launch_bounds__(256, 1) void k1_kernel(const char* __restrict__ x, const char* __restrict__ w1a, const char* __restrict__ w1b,
                                                    const char* __restrict__ wqkv, char* __restrict__ ws) {
  extern __shared__ __attribute__((aligned(16))) char smem[];
  LAS char* lds = (LAS char*)smem;
  const int lane = threadIdx.x & 63, w = __builtin_amdgcn_readfirstlane(threadIdx.x >> 6), r = lane & 31, h = lane >> 5;
  const int tb = blockIdx.x;
  f32x16 xv[1][2];
  {
    const char* xrow = x + (size_t)(tb * 32 + r) * 512 + (w * 64 + 4 * h) * 2;
#pragma unroll
    for (int nt = 0; nt < 2; ++nt)
#pragma unroll
      for (int g = 0; g < 4; ++g) {
        u32x2 u = *(const u32x2*)(xrow + (nt * 32 + 8 * g) * 2);
        xv[0][nt][4 * g] = bflo(u[0]); xv[0][nt][4 * g + 1] = bfhi(u[0]); xv[0][nt][4 * g + 2] = bflo(u[1]); xv[0][nt][4 * g + 3] = bfhi(u[1]);
      }
  }
  Stream st;
  st.init((unsigned)(uintptr_t)(lds + L_RING + w * RINGB), lane);
  Panel pa0 = make_panel(w1a + (size_t)w * 128, 2048, lane);
  st.start(pa0);

  bf16x8 xf[1][16];
  ln_to_frags<1>(xv, lds, w, lane, xf);
  f32x16 acc[2];
  Panel pq = make_panel(wqkv + w * 128, 1536, lane);
  ff_block<false>(st, pa0, w1a, w1b, pq, xf, lds, w, lane, acc);
  Panel pk = make_panel(wqkv + 512 + w * 128, 1536, lane);
  Panel pv = make_panel(wqkv + 1024 + w * 128, 1536, lane);
#pragma unroll
  for (int nt = 0; nt < 2; ++nt) {
#pragma unroll
    for (int i = 0; i < 16; ++i) xv[0][nt][i] = rbf(xv[0][nt][i] + 0.5f * rbf(acc[nt][i]));
#pragma unroll
    for (int s = 0; s < 2; ++s) *(bf16x8*)(ws + WS_X1F + ((size_t)tb * 16 + 4 * w + 2 * nt + s) * 1024 + lane * 16) = pack8(xv[0][nt], s);
  }
  ln_to_frags<1>(xv, lds, w, lane, xf);
  const int b = tb / NBLK, blk = tb - b * NBLK, bh = b * 4 + w;
  {
    f32x16 a[1][2]; a[0][0] = f32x16{}; a[0][1] = f32x16{};
    gemm_k256<1, false, false>(st, pq, pk, a, xf);
#pragma unroll
    for (int nt = 0; nt < 2; ++nt) {
#pragma unroll
      for (int i = 0; i < 16; ++i) a[0][nt][i] *= QSCALE;
#pragma unroll
      for (int s = 0; s < 2; ++s) *(bf16x8*)(ws + WS_QF + ((size_t)(bh * NBLK + blk) * 4 + 2 * nt + s) * 1024 + lane * 16) = pack8(a[0][nt], s);
    }
  }
  {
    f32x16 a[1][2]; a[0][0] = f32x16{}; a[0][1] = f32x16{};
    gemm_k256<1, false, false>(st, pk, pv, a, xf);
#pragma unroll
    for (int nt = 0; nt < 2; ++nt)
#pragma unroll
      for (int s = 0; s < 2; ++s) *(bf16x8*)(ws + WS_KVF + ((size_t)(bh * NBLK + blk) * 8 + 2 * nt + s) * 1024 + lane * 16) = pack8(a[0][nt], s);
  }
  {
    f32x16 a[1][2]; a[0][0] = f32x16{}; a[0][1] = f32x16{};
    gemm_k256<1, true, true>(st, pv, pv, a, xf);
#pragma unroll
    for (int nt = 0; nt < 2; ++nt)
#pragma unroll
      for (int s = 0; s < 2; ++s) *(bf16x8*)(ws + WS_KVF + ((size_t)(bh * NBLK + blk) * 8 + 4 + 2 * nt + s) * 1024 + lane * 16) = pack8(a[0][nt], s);
  }
}

constexpr int A_STAGE = 32768, A_NST = 32, A_LDS = 3 * A_STAGE;
__global__ __launch_bounds__(512, 2) void attn_kernel(char* __restrict__ ws) {
  extern __shared__ __attribute__((aligned(16))) char smem[];
  LAS char* lds = (LAS char*)smem;
  const int lane = threadIdx.x & 63, w = __builtin_amdgcn_readfirstlane(threadIdx.x >> 6);
  const int bh = blockIdx.x & 7, qg = blockIdx.x >> 3, qi = w & 3, kg = w >> 2;
  const int qb_raw = qg * 4 + qi, qb = qb_raw < NBLK ? qb_raw : NBLK - 1;
  const char* kv = ws + WS_KVF + (size_t)bh * NBLK * 8192;
  const unsigned lds0 = (unsigned)(uintptr_t)lds;
  const unsigned voff = w * 4096 + lane * 16;
  auto stage_dma = [&](int st, int buf) {
#pragma unroll
    for (int p = 0; p < 4; ++p) dma16(voff, kv + (size_t)st * A_STAGE + p * 1024, lds0 + buf * A_STAGE + w * 4096 + p * 1024);
  };
  stage_dma(0, 0); stage_dma(1, 1);
  bf16x8 qf[4];
#pragma unroll
  for (int f = 0; f < 4; ++f) qf[f] = *(const bf16x8*)(ws + WS_QF + ((size_t)(bh * NBLK + qb) * 4 + f) * 1024 + lane * 16);
#pragma unroll
  for (int f = 0; f < 4; ++f) asm volatile("" : "+v"(qf[f]));
  f32x16 o0 = f32x16{}, o1 = f32x16{}, negr;
  float ref = 0.f, lsum = 0.f;
#pragma unroll
  for (int i = 0; i < 16; ++i) negr[i] = 0.f;
  bool first = true;
  int buf = 0;
  for (int st = 0; st < A_NST; ++st) {
    if (st + 1 < A_NST) asm volatile("s_waitcnt vmcnt(4) lgkmcnt(0)\n\ts_barrier" ::: "memory");
    else asm volatile("s_waitcnt vmcnt(0) lgkmcnt(0)\n\ts_barrier" ::: "memory");
    if (st + 2 < A_NST) stage_dma(st + 2, buf == 0 ? 2 : buf - 1);
    const int nblk = (st < A_NST - 1) ? 2 : (kg == 0 ? 1 : 0);
    for (int bi = 0; bi < nblk; ++bi) {
      LAS const char* kp = lds + buf * A_STAGE + (kg * 2 + bi) * 8192 + lane * 16;
      f32x16 s = MFMA(ldsfrag(kp), qf[0], negr);
      s = MFMA(ldsfrag(kp + 1024), qf[1], s);
      s = MFMA(ldsfrag(kp + 2048), qf[2], s);
      s = MFMA(ldsfrag(kp + 3072), qf[3], s);
      f32x16 p;
#pragma unroll
      for (int i = 0; i < 16; ++i) p[i] = __builtin_amdgcn_exp2f(s[i]);
      u32x4 pa = __builtin_bit_cast(u32x4, pack8(p, 0)), pb = __builtin_bit_cast(u32x4, pack8(p, 1));
      const unsigned orall = (pa[0] | pa[1] | pa[2]) | (pa[3] | pb[0] | pb[1]) | (pb[2] | pb[3]);
      if (__builtin_expect(first || __any((orall & 0x40004000u) != 0u), 0)) {
        f32x16 z = f32x16{};
        f32x16 sr = MFMA(ldsfrag(kp), qf[0], z);
        sr = MFMA(ldsfrag(kp + 1024), qf[1], sr);
        sr = MFMA(ldsfrag(kp + 2048), qf[2], sr);
        sr = MFMA(ldsfrag(kp + 3072), qf[3], sr);
        float mx = sr[0];
#pragma unroll
        for (int i = 1; i < 16; ++i) mx = fmaxf(mx, sr[i]);
        mx = halfmax(mx);
        float nref = ref, alpha = 1.0f;
        if (first) { nref = mx + 7.0f; alpha = 0.f; }
        else if (mx - ref >= 0.5f) { nref = mx + 7.0f; alpha = __builtin_amdgcn_exp2f(ref - nref); }
        ref = nref; lsum *= alpha;
#pragma unroll
        for (int i = 0; i < 16; ++i) { o0[i] *= alpha; o1[i] *= alpha; negr[i] = -nref; p[i] = __builtin_amdgcn_exp2f(sr[i] - nref); }
        pa = __builtin_bit_cast(u32x4, pack8(p, 0)); pb = __builtin_bit_cast(u32x4, pack8(p, 1));
        first = false;
      }
      float ls = 0.f;
#pragma unroll
      for (int i = 0; i < 16; ++i) ls += p[i];
      lsum += ls;
      const bf16x8 p0 = __builtin_bit_cast(bf16x8, pa), p1 = __builtin_bit_cast(bf16x8, pb);
      o0 = MFMA(ldsfrag(kp + 4096), p0, o0);
      o1 = MFMA(ldsfrag(kp + 6144), p0, o1);
      o0 = MFMA(ldsfrag(kp + 5120), p1, o0);
      o1 = MFMA(ldsfrag(kp + 7168), p1, o1);
    }
    buf = (buf == 2) ? 0 : buf + 1;
  }
  lsum = halfsum(lsum);
  lds_barrier();
  typedef float f32x4 __attribute__((ext_vector_type(4)));
  LAS char* cb = lds + qi * 9216 + lane * 16;
  if (kg == 1) {
#pragma unroll
    for (int g = 0; g < 4; ++g) {
      *(LAS f32x4*)(cb + g * 1024) = (f32x4){o0[4 * g], o0[4 * g + 1], o0[4 * g + 2], o0[4 * g + 3]};
      *(LAS f32x4*)(cb + 4096 + g * 1024) = (f32x4){o1[4 * g], o1[4 * g + 1], o1[4 * g + 2], o1[4 * g + 3]};
    }
    *(LAS f32x2*)(lds + qi * 9216 + 8192 + lane * 8) = (f32x2){ref, lsum};
  }
  lds_barrier();
  if (kg == 0 && qb_raw < NBLK) {
    const f32x2 rl = *(LAS f32x2*)(lds + qi * 9216 + 8192 + lane * 8);
    const float m = fmaxf(ref, rl[0]);
    const float a0 = __builtin_amdgcn_exp2f(ref - m), a1 = __builtin_amdgcn_exp2f(rl[0] - m);
    const float inv = 1.0f / (lsum * a0 + rl[1] * a1);
    const float c0 = a0 * inv, c1 = a1 * inv;
#pragma unroll
    for (int g = 0; g < 4; ++g) {
      const f32x4 u0 = *(LAS f32x4*)(cb + g * 1024), u1 = *(LAS f32x4*)(cb + 4096 + g * 1024);
#pragma unroll
      for (int j = 0; j < 4; ++j) { o0[4 * g + j] = o0[4 * g + j] * c0 + u0[j] * c1; o1[4 * g + j] = o1[4 * g + j] * c0 + u1[j] * c1; }
    }
    char* dst = ws + WS_ATTF + ((size_t)((bh >> 2) * NBLK + qb) * 16 + (bh & 3) * 4) * 1024 + lane * 16;
    *(bf16x8*)(dst) = pack8(o0, 0); *(bf16x8*)(dst + 1024) = pack8(o0, 1);
    *(bf16x8*)(dst + 2048) = pack8(o1, 0); *(bf16x8*)(dst + 3072) = pack8(o1, 1);
  }
}

__global__ __launch_bounds__(256, 1) void k3_kernel(const char* __restrict__ wo, const char* __restrict__ pw1, const char* __restrict__ dw,
                                                    const char* __restrict__ pw2, const char* __restrict__ w2a, const char* __restrict__ w2b,
                                                    const char* __restrict__ ws, char* __restrict__ out) {
  extern __shared__ __attribute__((aligned(16))) char smem[];
  LAS char* lds = (LAS char*)smem;
  const int lane = threadIdx.x & 63, w = __builtin_amdgcn_readfirstlane(threadIdx.x >> 6), r = lane & 31, h = lane >> 5;
  const int tb = blockIdx.x, b = tb / NBLK, t0 = (tb - b * NBLK) * 32;
  const int hc = r & 3, ht = (hc < 2) ? t0 - 2 + hc : t0 + 30 + hc;
  const bool hvalid = ht >= 0 && ht < TSEQ;
  const int hg = b * TSEQ + (ht < 0 ? 0 : (ht >= TSEQ ? TSEQ - 1 : ht));
  const size_t hoff = (size_t)(hg >> 5) * 16384 + ((hg & 31) + 32 * h) * 16;
  const size_t moff = (size_t)tb * 16384 + lane * 16;
  bf16x8 bf[2][16];
#pragma unroll
  for (int f = 0; f < 16; ++f) {
    bf[0][f] = *(const bf16x8*)(ws + WS_ATTF + moff + f * 1024);
    bf[1][f] = *(const bf16x8*)(ws + WS_ATTF + hoff + f * 1024);
  }
  f32x16 xv[2][2];
#pragma unroll
  for (int nt = 0; nt < 2; ++nt)
#pragma unroll
    for (int s = 0; s < 2; ++s) {
      unpack8(*(const u32x4*)(ws + WS_X1F + moff + (4 * w + 2 * nt + s) * 1024), xv[0][nt], s);
      unpack8(*(const u32x4*)(ws + WS_X1F + hoff + (4 * w + 2 * nt + s) * 1024), xv[1][nt], s);
    }
  if (threadIdx.x < 160) *(LAS u32x4*)(lds + L_DW + threadIdx.x * 16) = *(const u32x4*)(dw + threadIdx.x * 16);
  Stream st;
  st.init((unsigned)(uintptr_t)(lds + L_RING + w * RINGB), lane);
  Panel pwo = make_panel(wo + w * 128, 512, lane);
  st.start(pwo);
  Panel p1a = make_panel(pw1 + w * 128, 1024, lane);
  Panel p1b = make_panel(pw1 + 512 + w * 128, 1024, lane);
  Panel pp2 = make_panel(pw2 + w * 128, 512, lane);
  {
    f32x16 a[2][2];
#pragma unroll
    for (int nb = 0; nb < 2; ++nb) { a[nb][0] = f32x16{}; a[nb][1] = f32x16{}; }
    gemm_k256<2, false, false>(st, pwo, p1a, a, bf);
#pragma unroll
    for (int nb = 0; nb < 2; ++nb)
#pragma unroll
      for (int nt = 0; nt < 2; ++nt)
#pragma unroll
        for (int i = 0; i < 16; ++i) xv[nb][nt][i] = rbf(xv[nb][nt][i] + rbf(a[nb][nt][i]));
  }
  ln_to_frags<2>(xv, lds, w, lane, bf);
  {
    f32x16 a[2][2], g[2][2];
#pragma unroll
    for (int nb = 0; nb < 2; ++nb) { a[nb][0] = f32x16{}; a[nb][1] = f32x16{}; g[nb][0] = f32x16{}; g[nb][1] = f32x16{}; }
    gemm_k256<2, false, false>(st, p1a, p1b, a, bf);
    gemm_k256<2, false, false>(st, p1b, pp2, g, bf);
    LAS char* G = lds + L_G + w * GSZ;
#pragma unroll
    for (int nb = 0; nb < 2; ++nb) {
      const int row = nb == 0 ? r + 2 : (hc < 2 ? hc : 32 + hc);
      const bool wr = nb == 0 || r < 4;
      const float keep = (nb == 0 || hvalid) ? 1.0f : 0.0f;
#pragma unroll
      for (int nt = 0; nt < 2; ++nt)
#pragma unroll
        for (int q = 0; q < 4; ++q) {
          float e[4];
#pragma unroll
          for (int j = 0; j < 4; ++j) e[j] = keep * rbf(a[nb][nt][4 * q + j]) * rbf(sigmoidf_(rbf(g[nb][nt][4 * q + j])));
          if (wr) *(LAS u32x2*)(G + row * GROW + (nt * 32 + 8 * q + 4 * h) * 2) = (u32x2){pk2(e[0], e[1]), pk2(e[2], e[3])};
        }
    }
    lds_barrier();
#pragma unroll
    for (int nt = 0; nt < 2; ++nt) {
      f32x16 c;
#pragma unroll
      for (int q = 0; q < 4; ++q) {
        float e[4] = {0.f, 0.f, 0.f, 0.f};
#pragma unroll
        for (int i = 0; i < 5; ++i) {
          const u32x2 gv = *(LAS u32x2*)(G + (r + i) * GROW + (nt * 32 + 8 * q + 4 * h) * 2);
          const u32x2 dv = *(LAS u32x2*)(lds + L_DW + i * 512 + (w * 64 + nt * 32 + 8 * q + 4 * h) * 2);
          e[0] += bflo(gv[0]) * bflo(dv[0]); e[1] += bfhi(gv[0]) * bfhi(dv[0]);
          e[2] += bflo(gv[1]) * bflo(dv[1]); e[3] += bfhi(gv[1]) * bfhi(dv[1]);
        }
#pragma unroll
        for (int j = 0; j < 4; ++j) c[4 * q + j] = siluf_(e[j]);
      }
#pragma unroll
      for (int s = 0; s < 2; ++s) *(LAS bf16x8*)(lds + L_ACT + (4 * w + 2 * nt + s) * 1024 + lane * 16) = pack8(c, s);
    }
    lds_barrier();
  }
  bf16x8 xf[1][16];
#pragma unroll
  for (int f = 0; f < 16; ++f) xf[0][f] = ldsfrag(lds + L_ACT + f * 1024 + lane * 16);
  f32x16 x3[1][2];
  Panel pa0 = make_panel(w2a + (size_t)w * 128, 2048, lane);
  {
    f32x16 a[1][2]; a[0][0] = f32x16{}; a[0][1] = f32x16{};
    gemm_k256<1, false, false>(st, pp2, pa0, a, xf);
#pragma unroll
    for (int nt = 0; nt < 2; ++nt)
#pragma unroll
      for (int i = 0; i < 16; ++i) x3[0][nt][i] = rbf(xv[0][nt][i] + rbf(a[0][nt][i]));
  }
  ln_to_frags<1>(x3, lds, w, lane, xf);
  f32x16 acc[2];
  ff_block<true>(st, pa0, w2a, w2b, pa0, xf, lds, w, lane, acc);
#pragma unroll
  for (int nt = 0; nt < 2; ++nt)
#pragma unroll
    for (int i = 0; i < 16; ++i) x3[0][nt][i] = rbf(x3[0][nt][i] + 0.5f * rbf(acc[nt][i]));
  float mean[1], rstd[1];
  ln_stats<1>(x3, lds, w, lane, mean, rstd);
  char* orow = out + (size_t)(tb * 32 + r) * 512 + (w * 64 + 4 * h) * 2;
#pragma unroll
  for (int nt = 0; nt < 2; ++nt)
#pragma unroll
    for (int q = 0; q < 4; ++q) {
      float e[4];
#pragma unroll
      for (int j = 0; j < 4; ++j) e[j] = (x3[0][nt][4 * q + j] - mean[0]) * rstd[0];
      *(u32x2*)(orow + (nt * 32 + 8 * q) * 2) = (u32x2){pk2(e[0], e[1]), pk2(e[2], e[3])};
    }
}

extern "C" void kernel_launch(void* const* d_in, const int* in_sizes, int n_in, void* d_out, int out_size, void* d_ws, size_t ws_size,
                              hipStream_t stream) {
  const char* x = (const char*)d_in[0];
  const char* w1a = (const char*)d_in[1];
  const char* w1b = (const char*)d_in[2];
  const char* wqkv = (const char*)d_in[3];
  const char* wo = (const char*)d_in[4];
  const char* pw1 = (const char*)d_in[5];
  const char* dw = (const char*)d_in[6];
  const char* pw2 = (const char*)d_in[7];
  const char* w2a = (const char*)d_in[8];
  const char* w2b = (const char*)d_in[9];
  char* ws = (char*)d_ws;
  (void)hipFuncSetAttribute((const void*)k1_kernel, hipFuncAttributeMaxDynamicSharedMemorySize, L_TOTAL);
  (void)hipFuncSetAttribute((const void*)attn_kernel, hipFuncAttributeMaxDynamicSharedMemorySize, A_LDS);
  (void)hipFuncSetAttribute((const void*)k3_kernel, hipFuncAttributeMaxDynamicSharedMemorySize, L_TOTAL);
  k1_kernel<<<NTB, 256, L_TOTAL, stream>>>(x, w1a, w1b, wqkv, ws);
  attn_kernel<<<256, 512, A_LDS, stream>>>(ws);
  k3_kernel<<<NTB, 256, L_TOTAL, stream>>>(wo, pw1, dw, pw2, w2a, w2b, ws, (char*)d_out);
}
```

```cpp
#include <hip/hip_runtime.h>
#include <hip/hip_bf16.h>
#include <stdint.h>

typedef short bf16x8 __attribute__((ext_vector_type(8)));
typedef short s16x4 __attribute__((ext_vector_type(4)));
typedef float f32x16 __attribute__((ext_vector_type(16)));
typedef float f32x2 __attribute__((ext_vector_type(2)));
typedef __bf16 bf16x2_t __attribute__((ext_vector_type(2)));
typedef unsigned u32x4 __attribute__((ext_vector_type(4)));
typedef unsigned u32x2 __attribute__((ext_vector_type(2)));
#define LAS __attribute__((address_space(3)))
#define DI __device__ __forceinline__
#define MFMA(a, b, c) __builtin_amdgcn_mfma_f32_32x32x16_bf16((a), (b), (c), 0, 0, 0)
#define MFMA16(a, b, c) __builtin_amdgcn_mfma_f32_16x16x32_bf16((a), (b), (c), 0, 0, 0)
#define SB() __builtin_amdgcn_sched_barrier(0)
typedef float f32x4 __attribute__((ext_vector_type(4)));

constexpr int TSEQ = 4000, NBLK = 125, NTB = 250;
constexpr size_t WS_X1F = 0;
constexpr size_t WS_QF = 4u << 20;
constexpr size_t WS_KVF = 8u << 20;
constexpr size_t WS_ATTF = 17u << 20;
constexpr float LN_EPS = 1e-5f;
constexpr float LOG2E = 1.4426950408889634f;
constexpr float QSCALE = 0.125f * LOG2E;

DI unsigned pk2(float a, float b) { f32x2 v = {a, b}; return __builtin_bit_cast(unsigned, __builtin_convertvector(v, bf16x2_t)); }
DI float bflo(unsigned u) { return __uint_as_float(u << 16); }
DI float bfhi(unsigned u) { return __uint_as_float(u & 0xffff0000u); }
DI float rbf(float a) { return bflo(pk2(a, a)); }
DI bf16x8 pack8(const f32x16& v, int s) {
  u32x4 p = {pk2(v[8 * s], v[8 * s + 1]), pk2(v[8 * s + 2], v[8 * s + 3]), pk2(v[8 * s + 4], v[8 * s + 5]), pk2(v[8 * s + 6], v[8 * s + 7])};
  return __builtin_bit_cast(bf16x8, p);
}
DI void unpack8(u32x4 p, f32x16& v, int s) {
#pragma unroll
  for (int i = 0; i < 4; ++i) { v[8 * s + 2 * i] = bflo(p[i]); v[8 * s + 2 * i + 1] = bfhi(p[i]); }
}
DI float halfsum(float x) {
  auto r = __builtin_amdgcn_permlane32_swap(__float_as_uint(x), __float_as_uint(x), false, false);
  return __uint_as_float(r[0]) + __uint_as_float(r[1]);
}
DI float halfmax(float x) {
  auto r = __builtin_amdgcn_permlane32_swap(__float_as_uint(x), __float_as_uint(x), false, false);
  return fmaxf(__uint_as_float(r[0]), __uint_as_float(r[1]));
}
DI float sigmoidf_(float x) { return __builtin_amdgcn_rcpf(1.0f + __builtin_amdgcn_exp2f(-LOG2E * x)); }
DI float siluf_(float x) { return x * sigmoidf_(x); }
DI void lds_barrier() { asm volatile("s_waitcnt lgkmcnt(0)\n\ts_barrier" ::: "memory"); }
typedef short v4i16_t __attribute__((ext_vector_type(4)));
DI s16x4 trread(LAS const char* p) { return __builtin_bit_cast(s16x4, __builtin_amdgcn_ds_read_tr16_b64_v4i16((LAS v4i16_t*)p)); }
DI bf16x8 ldsfrag(LAS const char* p) { return *(LAS const bf16x8*)p; }

constexpr int SLAB = 4096, RING = 7, RINGB = SLAB * RING;
struct Panel { const char* sbase; unsigned ld32; unsigned v0, v1, v2, v3; };
DI Panel make_panel(const char* base, unsigned ld, int lane) {
  Panel p; p.sbase = base; p.ld32 = ld * 32;
  p.v0 = (unsigned)(lane >> 3) * ld + ((unsigned)((lane & 7) ^ (((lane >> 4) & 1) << 2)) << 4);
  p.v1 = p.v0 + (ld * 8 - 1024); p.v2 = p.v0 + (ld * 16 - 2048); p.v3 = p.v0 + (ld * 24 - 3072);
  return p;
}
struct Stream {
  unsigned fslot, cslot, ring0, rd0, rd1;
  bf16x8 cur[4];
  DI void init(unsigned ring_base, int lane) {
    ring0 = ring_base; fslot = 0; cslot = 0;
    const int h = lane >> 5, cb = (lane >> 4) & 1, q = (lane & 15) >> 2, p = lane & 3;
    rd0 = ring0 + (4 * h + q) * 128 + ((0 ^ (q >> 1)) * 64) + cb * 32 + p * 8;
    rd1 = ring0 + (4 * h + q) * 128 + ((1 ^ (q >> 1)) * 64) + cb * 32 + p * 8;
  }
  DI void issue(Panel& p) {
    asm volatile("s_mov_b32 m0, %5\n\ts_nop 0\n\t"
                 "global_load_lds_dwordx4 %0, %4\n\tglobal_load_lds_dwordx4 %1, %4 offset:1024\n\t"
                 "global_load_lds_dwordx4 %2, %4 offset:2048\n\tglobal_load_lds_dwordx4 %3, %4 offset:3072"
                 :: "v"(p.v0), "v"(p.v1), "v"(p.v2), "v"(p.v3), "s"(p.sbase), "s"(ring0 + fslot));
    p.sbase += p.ld32; fslot = (fslot == RINGB - SLAB) ? 0u : fslot + SLAB;
  }
  template <int N> DI void wait() { asm volatile("s_waitcnt vmcnt(%0) lgkmcnt(0)" :: "n"(N) : "memory"); }
  DI void read(bf16x8 (&f)[4]) {
    LAS const char* p0 = (LAS const char*)(uintptr_t)(rd0 + cslot);
    LAS const char* p1 = (LAS const char*)(uintptr_t)(rd1 + cslot);
#pragma unroll
    for (int ks = 0; ks < 2; ++ks) {
      s16x4 l0 = trread(p0 + ks * 2048), h0 = trread(p0 + ks * 2048 + 1024);
      s16x4 l1 = trread(p1 + ks * 2048), h1 = trread(p1 + ks * 2048 + 1024);
      f[2 * ks] = __builtin_shufflevector(l0, h0, 0, 1, 2, 3, 4, 5, 6, 7);
      f[2 * ks + 1] = __builtin_shufflevector(l1, h1, 0, 1, 2, 3, 4, 5, 6, 7);
    }
    cslot = (cslot == RINGB - SLAB) ? 0u : cslot + SLAB;
  }
  DI void start(Panel& p) {
#pragma unroll
    for (int i = 0; i < RING; ++i) issue(p);
  }
  DI void first() { wait<4 * (RING - 1)>(); read(cur); }
};
struct NoWork { DI void operator()(int) const {} };
template <int NB, bool SWAP, bool LAST, bool BLDS, class W>
DI void gemm_k256(Stream& st, Panel& pc, Panel& pn, f32x16 (&acc)[NB][2], const bf16x8 (&bf)[NB][16], LAS const char* bl, const W& work) {
#pragma unroll
  for (int sl = 0; sl < 8; ++sl) {
    bf16x8 nxt[4], b0, b1;
    if (BLDS) { b0 = ldsfrag(bl + (2 * sl) * 1024); b1 = ldsfrag(bl + (2 * sl + 1) * 1024); }
    if (!(LAST && sl == 7)) {
      constexpr int Y0 = RING - 2;
      if (!LAST || 6 - sl >= Y0) st.template wait<4 * Y0>();
      else if (sl == 2) st.template wait<16>();
      else if (sl == 3) st.template wait<12>();
      else if (sl == 4) st.template wait<8>();
      else if (sl == 5) st.template wait<4>();
      else st.template wait<0>();
      st.read(nxt);
      if (sl + RING < 8) st.issue(pc); else if (!LAST) st.issue(pn);
    }
#pragma unroll
    for (int ks = 0; ks < 2; ++ks)
#pragma unroll
      for (int nb = 0; nb < NB; ++nb)
#pragma unroll
        for (int nt = 0; nt < 2; ++nt) {
          const bf16x8 b = BLDS ? (ks ? b1 : b0) : bf[nb][2 * sl + ks];
          acc[nb][nt] = SWAP ? MFMA(b, st.cur[2 * ks + nt], acc[nb][nt]) : MFMA(st.cur[2 * ks + nt], b, acc[nb][nt]);
        }
    work(sl);
    if (!(LAST && sl == 7)) {
#pragma unroll
      for (int i = 0; i < 4; ++i) st.cur[i] = nxt[i];
    }
  }
}

constexpr int L_RING = 0;
constexpr int L_ACT = 4 * RINGB;
constexpr int L_STAT = L_ACT + 32768;
constexpr int GROW = 136, GSZ = 36 * GROW;
constexpr int L_DW = L_STAT + 2048;
constexpr int L_TOTAL = L_DW + 2560;

template <int NB>
DI void ln_stats(const f32x16 (&v)[NB][2], LAS char* lds, int w, int lane, float (&mean)[NB], float (&rstd)[NB]) {
  const int r = lane & 31;
#pragma unroll
  for (int nb = 0; nb < NB; ++nb) {
    float s = 0.f;
#pragma unroll
    for (int i = 0; i < 16; ++i) s += v[nb][0][i] + v[nb][1][i];
    const float mw = halfsum(s) * (1.0f / 64.0f);
    float m2 = 0.f;
#pragma unroll
    for (int i = 0; i < 16; ++i) { float d0 = v[nb][0][i] - mw, d1 = v[nb][1][i] - mw; m2 += d0 * d0 + d1 * d1; }
    m2 = halfsum(m2);
    if (lane < 32) *(LAS f32x2*)(lds + L_STAT + ((nb * 4 + w) * 32 + r) * 8) = (f32x2){mw, m2};
  }
  lds_barrier();
#pragma unroll
  for (int nb = 0; nb < NB; ++nb) {
    f32x2 s0 = *(LAS f32x2*)(lds + L_STAT + ((nb * 4 + 0) * 32 + r) * 8), s1 = *(LAS f32x2*)(lds + L_STAT + ((nb * 4 + 1) * 32 + r) * 8);
    f32x2 s2 = *(LAS f32x2*)(lds + L_STAT + ((nb * 4 + 2) * 32 + r) * 8), s3 = *(LAS f32x2*)(lds + L_STAT + ((nb * 4 + 3) * 32 + r) * 8);
    const float m = 0.25f * (s0[0] + s1[0] + s2[0] + s3[0]);
    const float e0 = s0[0] - m, e1 = s1[0] - m, e2 = s2[0] - m, e3 = s3[0] - m;
    const float M2 = s0[1] + s1[1] + s2[1] + s3[1] + 64.0f * (e0 * e0 + e1 * e1 + e2 * e2 + e3 * e3);
    mean[nb] = m; rstd[nb] = __builtin_amdgcn_rsqf(M2 * (1.0f / 256.0f) + LN_EPS);
  }
}
template <int NB>
DI void ln_to_frags(const f32x16 (&v)[NB][2], LAS char* lds, int w, int lane, bf16x8 (&out)[NB][16]) {
  float mean[NB], rstd[NB];
  ln_stats<NB>(v, lds, w, lane, mean, rstd);
  LAS char* fb = lds + L_ACT + (NB == 1 ? 16384 : 0) + lane * 16;
#pragma unroll
  for (int nb = 0; nb < NB; ++nb)
#pragma unroll
    for (int nt = 0; nt < 2; ++nt) {
      f32x16 n;
#pragma unroll
      for (int i = 0; i < 16; ++i) n[i] = (v[nb][nt][i] - mean[nb]) * rstd[nb];
#pragma unroll
      for (int s = 0; s < 2; ++s) *(LAS bf16x8*)(fb + (nb * 16 + 4 * w + 2 * nt + s) * 1024) = pack8(n, s);
    }
  lds_barrier();
#pragma unroll
  for (int nb = 0; nb < NB; ++nb)
#pragma unroll
    for (int f = 0; f < 16; ++f) out[nb][f] = ldsfrag(fb + (nb * 16 + f) * 1024);
}

struct SiluWork {
  const f32x16 (&h)[1][2]; LAS char* dst;
  DI void operator()(int sl) const {
    if (sl & 1) {
      const int f = sl >> 1, nt = f >> 1, s = f & 1;
      f32x16 hv;
#pragma unroll
      for (int j = 0; j < 8; ++j) hv[8 * s + j] = siluf_(h[0][nt][8 * s + j]);
      *(LAS bf16x8*)(dst + f * 1024) = pack8(hv, s);
    }
  }
};
template <bool LAST>
DI void ff_block(Stream& st, Panel& pa0, const char* wa, const char* wb, Panel& pnext, const bf16x8 (&xf)[1][16], LAS char* lds, int w, int lane, f32x16 (&out)[1][2]) {
  out[0][0] = f32x16{}; out[0][1] = f32x16{};
  LAS char* act = lds + L_ACT + lane * 16;
  f32x16 hc[1][2]; hc[0][0] = f32x16{}; hc[0][1] = f32x16{};
  Panel pn = make_panel(wa + (size_t)(w + 4) * 128, 2048, lane);
  gemm_k256<1, false, false, false>(st, pa0, pn, hc, xf, act, NoWork{});
#pragma unroll 1
  for (int c = 0; c < 3; ++c) {
    Panel pc = pn;
    pn = make_panel(wb + (size_t)c * 256 * 512 + w * 128, 512, lane);
    f32x16 hn[1][2]; hn[0][0] = f32x16{}; hn[0][1] = f32x16{};
    gemm_k256<1, false, false, false>(st, pc, pn, hn, xf, act, SiluWork{hc, act + (c & 1) * 16384 + w * 4096});
    lds_barrier();
    pc = pn;
    pn = (c < 2) ? make_panel(wa + (size_t)(w + 4 * c + 8) * 128, 2048, lane) : make_panel(wb + (size_t)3 * 256 * 512 + w * 128, 512, lane);
    gemm_k256<1, false, false, true>(st, pc, pn, out, xf, act + (c & 1) * 16384, NoWork{});
    hc[0][0] = hn[0][0]; hc[0][1] = hn[0][1];
  }
  {
    SiluWork sw{hc, act + 16384 + w * 4096};
#pragma unroll
    for (int sl = 1; sl < 8; sl += 2) sw(sl);
  }
  lds_barrier();
  gemm_k256<1, false, LAST, true>(st, pn, pnext, out, xf, act + 16384, NoWork{});
}

DI void l2_warm(const char* p, int kb_total, int idx, int lane, u32x4& t) {
  const int per = (kb_total + 30) / 31;
  int b = idx * per, e = b + per; if (e > kb_total) e = kb_total;
  for (int i = b; i < e; ++i)
    asm volatile("global_load_dwordx4 %0, %1, %2" : "+v"(t) : "v"(lane * 16 + i * 1024), "s"(p));
}

__global__ __launch_bounds__(320, 1) void k1_kernel(const char* __restrict__ x, const char* __restrict__ w1a, const char* __restrict__ w1b,
                                                    const char* __restrict__ wqkv, char* __restrict__ ws) {
  extern __shared__ __attribute__((aligned(16))) char smem[];
  LAS char* lds = (LAS char*)smem;
  const int lane = threadIdx.x & 63, w = __builtin_amdgcn_readfirstlane(threadIdx.x >> 6), r = lane & 31, h = lane >> 5;
  const int tb = blockIdx.x;
  if (w == 4) {
    const int idx = tb >> 3;
    u32x4 t = {0u, 0u, 0u, 0u};
    if (idx < 31) { l2_warm(w1a, 512, idx, lane, t); l2_warm(w1b, 512, idx, lane, t); l2_warm(wqkv, 384, idx, lane, t); }
    asm volatile("s_waitcnt vmcnt(0)" : "+v"(t) :: "memory");
    return;
  }
  Stream st;
  st.init((unsigned)(uintptr_t)(lds + L_RING + w * RINGB), lane);
  Panel pa0 = make_panel(w1a + (size_t)w * 128, 2048, lane);
  f32x16 xv[1][2];
  {
    const char* xrow = x + (size_t)(tb * 32 + r) * 512 + (w * 64 + 4 * h) * 2;
    u32x2 u[8];
#pragma unroll
    for (int g = 0; g < 8; ++g) u[g] = *(const u32x2*)(xrow + (g * 8) * 2);
    st.start(pa0);
#pragma unroll
    for (int g = 0; g < 8; ++g) {
      xv[0][g >> 2][4 * (g & 3)] = bflo(u[g][0]); xv[0][g >> 2][4 * (g & 3) + 1] = bfhi(u[g][0]);
      xv[0][g >> 2][4 * (g & 3) + 2] = bflo(u[g][1]); xv[0][g >> 2][4 * (g & 3) + 3] = bfhi(u[g][1]);
    }
  }
  bf16x8 xf[1][16];
  ln_to_frags<1>(xv, lds, w, lane, xf);
  st.first();
  f32x16 acc[1][2];
  Panel pq = make_panel(wqkv + w * 128, 1536, lane);
  ff_block<false>(st, pa0, w1a, w1b, pq, xf, lds, w, lane, acc);
#pragma unroll
  for (int nt = 0; nt < 2; ++nt) {
#pragma unroll
    for (int i = 0; i < 16; ++i) xv[0][nt][i] = rbf(xv[0][nt][i] + 0.5f * rbf(acc[0][nt][i]));
#pragma unroll
    for (int s = 0; s < 2; ++s) *(bf16x8*)(ws + WS_X1F + ((size_t)tb * 16 + 4 * w + 2 * nt + s) * 1024 + lane * 16) = pack8(xv[0][nt], s);
  }
  ln_to_frags<1>(xv, lds, w, lane, xf);
  const int b = tb / NBLK, blk = tb - b * NBLK, bh = b * 4 + w;
  char* kvdst = ws + WS_KVF + (size_t)(bh * NBLK + blk) * 8192 + lane * 16;
#pragma unroll 1
  for (int c = 0; c < 2; ++c) {
    Panel pc = pq;
    pq = make_panel(wqkv + (c + 1) * 512 + w * 128, 1536, lane);
    f32x16 a[1][2]; a[0][0] = f32x16{}; a[0][1] = f32x16{};
    gemm_k256<1, false, false, false>(st, pc, pq, a, xf, lds, NoWork{});
    const float sc = c == 0 ? QSCALE : 1.0f;
    char* dst = c == 0 ? ws + WS_QF + (size_t)(bh * NBLK + blk) * 4096 + lane * 16 : kvdst;
#pragma unroll
    for (int nt = 0; nt < 2; ++nt) {
#pragma unroll
      for (int i = 0; i < 16; ++i) a[0][nt][i] *= sc;
#pragma unroll
      for (int s = 0; s < 2; ++s) *(bf16x8*)(dst + (2 * nt + s) * 1024) = pack8(a[0][nt], s);
    }
  }
  {
    f32x16 a[1][2]; a[0][0] = f32x16{}; a[0][1] = f32x16{};
    gemm_k256<1, true, true, false>(st, pq, pq, a, xf, lds, NoWork{});
#pragma unroll
    for (int nt = 0; nt < 2; ++nt)
#pragma unroll
      for (int s = 0; s < 2; ++s) *(bf16x8*)(kvdst + (4 + 2 * nt + s) * 1024) = pack8(a[0][nt], s);
  }
}

constexpr int A_KG = 2, A_SBLK = A_KG * 2, A_HST = A_SBLK * 4096, A_NST = (NBLK + A_SBLK - 1) / A_SBLK, A_NFULL = NBLK / A_SBLK;
constexpr int A_VOFF = 3 * A_HST, A_LDS = 6 * A_HST;
__global__ __launch_bounds__(256 * A_KG, A_KG) void attn_kernel(char* __restrict__ ws, const char* __restrict__ wo, const char* __restrict__ pw1,
                                                                const char* __restrict__ pw2, const char* __restrict__ w2a, const char* __restrict__ w2b) {
  extern __shared__ __attribute__((aligned(16))) char smem[];
  LAS char* lds = (LAS char*)smem;
  const int lane = threadIdx.x & 63, w = __builtin_amdgcn_readfirstlane(threadIdx.x >> 6);
  const int bh = blockIdx.x & 7, qg = blockIdx.x >> 3, qi = w & 3, kg = w >> 2;
  const int qb_raw = qg * 4 + qi, qb = qb_raw < NBLK ? qb_raw : NBLK - 1;
  const char* kv = ws + WS_KVF + (size_t)bh * NBLK * 8192;
  const unsigned lds0 = (unsigned)(uintptr_t)lds;
  const unsigned voff = (w >> 1) * 8192 + (w & 1) * 2048 + lane * 16;
  auto dma2 = [&](int st, int isv, unsigned ldsoff) {
    asm volatile("s_mov_b32 m0, %2\n\ts_nop 0\n\tglobal_load_lds_dwordx4 %0, %1\n\tglobal_load_lds_dwordx4 %0, %1 offset:1024"
                 :: "v"(voff), "s"(kv + (size_t)st * (2 * A_HST) + isv * 4096), "s"(lds0 + ldsoff + w * 2048));
  };
  dma2(0, 0, 0); dma2(1, 0, A_HST); dma2(0, 1, A_VOFF); dma2(2, 0, 2 * A_HST); dma2(1, 1, A_VOFF + A_HST);
  bf16x8 qf[4];
#pragma unroll
  for (int f = 0; f < 4; ++f) qf[f] = *(const bf16x8*)(ws + WS_QF + ((size_t)(bh * NBLK + qb) * 4 + f) * 1024 + lane * 16);
#pragma unroll
  for (int f = 0; f < 4; ++f) asm volatile("" : "+v"(qf[f]));
  u32x4 wt = {0u, 0u, 0u, 0u};
  {
    const int i0 = (qg * 8 + w) * 6;
#pragma unroll
    for (int i = 0; i < 6; ++i) {
      const int kb = i0 + i;
      const char* p = kb < 128 ? wo + kb * 1024 : kb < 384 ? pw1 + (kb - 128) * 1024 : kb < 512 ? pw2 + (kb - 384) * 1024 : kb < 1024 ? w2a + (kb - 512) * 1024 : w2b + (kb - 1024) * 1024;
      asm volatile("global_load_dwordx4 %0, %1, %2" : "+v"(wt) : "v"(lane * 16), "s"(p));
    }
  }
  const int rem = NBLK - A_NFULL * A_SBLK - kg * 2;
  const int ntail = rem < 0 ? 0 : (rem > 2 ? 2 : rem);
  LAS const char* base = lds + kg * 8192 + lane * 16;
  f32x16 o0 = f32x16{}, o1 = f32x16{}, negr, sa, sb;
  f32x4 lacc = {0.f, 0.f, 0.f, 0.f};
  bf16x8 sel;
  { const short one = (((lane >> 4) ^ lane) & 1) ? (short)0 : (short)0x3f80;
#pragma unroll
    for (int j = 0; j < 8; ++j) sel[j] = one; }
  bf16x8 kf[4], vf[4]; unsigned pa[8], pb[8];
#pragma unroll
  for (int i = 0; i < 8; ++i) pa[i] = 0u;
  float ref;
  asm volatile("s_waitcnt vmcnt(0)\n\ts_barrier" : "+v"(wt) :: "memory");
  {
#pragma unroll
    for (int f = 0; f < 4; ++f) kf[f] = ldsfrag(base + f * 1024);
    f32x16 z = f32x16{};
    sa = MFMA(kf[0], qf[0], z); sa = MFMA(kf[1], qf[1], sa); sa = MFMA(kf[2], qf[2], sa); sa = MFMA(kf[3], qf[3], sa);
    float mx = sa[0];
#pragma unroll
    for (int i = 1; i < 16; ++i) mx = fmaxf(mx, sa[i]);
    ref = halfmax(mx) + 7.0f;
#pragma unroll
    for (int i = 0; i < 16; ++i) { sa[i] -= ref; negr[i] = -ref; }
#pragma unroll
    for (int f = 0; f < 4; ++f) { kf[f] = ldsfrag(base + 4096 + f * 1024); vf[f] = bf16x8{}; }
  }
#define SLOT3(MF, RF, i) do { MF; RF; e[2*(i)] = __builtin_amdgcn_exp2f(SC_[2*(i)]); e[2*(i)+1] = __builtin_amdgcn_exp2f(SC_[2*(i)+1]); \
    if ((i) > 0) PN_[(i)-1] = pk2(e[2*(i)-2], e[2*(i)-1]); SB(); } while (0)
  auto iter = [&](f32x16& SC_, f32x16& SN_, unsigned (&PP_)[8], unsigned (&PN_)[8], LAS const char* kn, LAS const char* vc) {
    float e[16];
    const bf16x8 p0 = __builtin_bit_cast(bf16x8, (u32x4){PP_[0], PP_[1], PP_[2], PP_[3]}), p1 = __builtin_bit_cast(bf16x8, (u32x4){PP_[4], PP_[5], PP_[6], PP_[7]});
    SB();
    SLOT3(SN_ = MFMA(kf[0], qf[0], negr), kf[0] = ldsfrag(kn), 0);
    SLOT3(o0 = MFMA(vf[0], p0, o0), vf[0] = ldsfrag(vc), 1);
    SLOT3(SN_ = MFMA(kf[1], qf[1], SN_), kf[1] = ldsfrag(kn + 1024), 2);
    SLOT3(o1 = MFMA(vf[2], p0, o1); lacc = MFMA16(sel, p0, lacc), vf[2] = ldsfrag(vc + 2048), 3);
    SLOT3(SN_ = MFMA(kf[2], qf[2], SN_), kf[2] = ldsfrag(kn + 2048), 4);
    SLOT3(o0 = MFMA(vf[1], p1, o0), vf[1] = ldsfrag(vc + 1024), 5);
    SLOT3(SN_ = MFMA(kf[3], qf[3], SN_), kf[3] = ldsfrag(kn + 3072), 6);
    SLOT3(o1 = MFMA(vf[3], p1, o1); lacc = MFMA16(sel, p1, lacc), vf[3] = ldsfrag(vc + 3072), 7);
    PN_[7] = pk2(e[14], e[15]);
    const unsigned orall = (PN_[0] | PN_[1] | PN_[2]) | (PN_[3] | PN_[4] | PN_[5]) | (PN_[6] | PN_[7]);
    if (__builtin_expect(__any((orall & 0x40004000u) != 0u), 0)) {
      float mx = SC_[0];
#pragma unroll
      for (int i = 1; i < 16; ++i) mx = fmaxf(mx, SC_[i]);
      mx = halfmax(mx);
      const float delta = (mx >= 0.5f) ? mx + 7.0f : 0.0f;
      const float alpha = __builtin_amdgcn_exp2f(-delta);
      const float alo = __shfl_xor(alpha, 16);
      const float aA = (lane & 16) ? alo : alpha, aB = (lane & 16) ? alpha : alo;
      lacc = lacc * (f32x4){aA, aB, aA, aB};
      o0 = o0 * alpha; o1 = o1 * alpha; negr = negr - delta; SN_ = SN_ - delta;
      asm volatile("" : "+v"(o0), "+v"(o1), "+v"(negr), "+v"(SN_), "+v"(lacc));
      ref = -negr[0];
#pragma unroll
      for (int i = 0; i < 8; ++i) PN_[i] = pk2(__builtin_amdgcn_exp2f(SC_[2*i] - delta), __builtin_amdgcn_exp2f(SC_[2*i+1] - delta));
    }
  };
  int r0 = 0, r1 = A_HST, r2 = 2 * A_HST;
  for (int st = 0; st < A_NFULL; ++st) {
    asm volatile("s_waitcnt vmcnt(4) lgkmcnt(0)\n\ts_barrier" ::: "memory");
    dma2(st + 3 < A_NST ? st + 3 : A_NST - 1, 0, r0); dma2(st + 2 < A_NST ? st + 2 : A_NST - 1, 1, A_VOFF + r2);
    iter(sa, sb, pa, pb, base + r1, base + A_VOFF + r0);
    iter(sb, sa, pb, pa, base + r1 + 4096, base + A_VOFF + r0 + 4096);
    const int t = r0; r0 = r1; r1 = r2; r2 = t;
  }
  asm volatile("s_waitcnt vmcnt(0) lgkmcnt(0)\n\ts_barrier" ::: "memory");
  if (ntail > 0) {
    iter(sa, sb, pa, pb, base + r1, base + A_VOFF + r0);
    if (ntail > 1) iter(sb, sa, pb, pa, base + r1 + 4096, base + A_VOFF + r0 + 4096);
    else {
#pragma unroll
      for (int i = 0; i < 8; ++i) pa[i] = pb[i];
    }
  }
  {
    const bf16x8 p0 = __builtin_bit_cast(bf16x8, (u32x4){pa[0], pa[1], pa[2], pa[3]}), p1 = __builtin_bit_cast(bf16x8, (u32x4){pa[4], pa[5], pa[6], pa[7]});
    o0 = MFMA(vf[0], p0, o0); o1 = MFMA(vf[2], p0, o1); o0 = MFMA(vf[1], p1, o0); o1 = MFMA(vf[3], p1, o1);
    lacc = MFMA16(sel, p0, lacc); lacc = MFMA16(sel, p1, lacc);
  }
  float lsum = (lane & 16) ? lacc[1] : lacc[0];
  lds_barrier();
  if (kg > 0) {
    LAS char* cb = lds + ((kg - 1) * 4 + qi) * 9216 + lane * 16;
#pragma unroll
    for (int g = 0; g < 4; ++g) {
      *(LAS f32x4*)(cb + g * 1024) = (f32x4){o0[4 * g], o0[4 * g + 1], o0[4 * g + 2], o0[4 * g + 3]};
      *(LAS f32x4*)(cb + 4096 + g * 1024) = (f32x4){o1[4 * g], o1[4 * g + 1], o1[4 * g + 2], o1[4 * g + 3]};
    }
    *(LAS f32x2*)(lds + ((kg - 1) * 4 + qi) * 9216 + 8192 + lane * 8) = (f32x2){ref, lsum};
  }
  lds_barrier();
  if (kg == 0 && qb_raw < NBLK) {
#pragma unroll
    for (int k = 1; k < A_KG; ++k) {
      LAS char* cb = lds + ((k - 1) * 4 + qi) * 9216 + lane * 16;
      const f32x2 rl = *(LAS f32x2*)(lds + ((k - 1) * 4 + qi) * 9216 + 8192 + lane * 8);
      const float m = fmaxf(ref, rl[0]);
      const float c0 = __builtin_amdgcn_exp2f(ref - m), c1 = __builtin_amdgcn_exp2f(rl[0] - m);
      lsum = lsum * c0 + rl[1] * c1; ref = m;
#pragma unroll
      for (int g = 0; g < 4; ++g) {
        const f32x4 u0 = *(LAS f32x4*)(cb + g * 1024), u1 = *(LAS f32x4*)(cb + 4096 + g * 1024);
#pragma unroll
        for (int j = 0; j < 4; ++j) { o0[4 * g + j] = o0[4 * g + j] * c0 + u0[j] * c1; o1[4 * g + j] = o1[4 * g + j] * c0 + u1[j] * c1; }
      }
    }
    const float inv = 1.0f / lsum;
#pragma unroll
    for (int i = 0; i < 16; ++i) { o0[i] *= inv; o1[i] *= inv; }
    char* dst = ws + WS_ATTF + ((size_t)((bh >> 2) * NBLK + qb) * 16 + (bh & 3) * 4) * 1024 + lane * 16;
    *(bf16x8*)(dst) = pack8(o0, 0); *(bf16x8*)(dst + 1024) = pack8(o0, 1);
    *(bf16x8*)(dst + 2048) = pack8(o1, 0); *(bf16x8*)(dst + 3072) = pack8(o1, 1);
  }
}

__global__ __launch_bounds__(256, 1) void k3_kernel(const char* __restrict__ wo, const char* __restrict__ pw1, const char* __restrict__ dw,
                                                    const char* __restrict__ pw2, const char* __restrict__ w2a, const char* __restrict__ w2b,
                                                    const char* __restrict__ ws, char* __restrict__ out) {
  extern __shared__ __attribute__((aligned(16))) char smem[];
  LAS char* lds = (LAS char*)smem;
  const int lane = threadIdx.x & 63, w = __builtin_amdgcn_readfirstlane(threadIdx.x >> 6), r = lane & 31, h = lane >> 5;
  const int tb = blockIdx.x, b = tb / NBLK, t0 = (tb - b * NBLK) * 32;
  Stream st;
  st.init((unsigned)(uintptr_t)(lds + L_RING + w * RINGB), lane);
  Panel pwo = make_panel(wo + w * 128, 512, lane);
  const int hc = r & 3, ht = (hc < 2) ? t0 - 2 + hc : t0 + 30 + hc;
  const bool hvalid = ht >= 0 && ht < TSEQ;
  const int hg = b * TSEQ + (ht < 0 ? 0 : (ht >= TSEQ ? TSEQ - 1 : ht));
  const size_t hoff = (size_t)(hg >> 5) * 16384 + ((hg & 31) + 32 * h) * 16;
  const size_t moff = (size_t)tb * 16384 + lane * 16;
  bf16x8 bf[2][16];
#pragma unroll
  for (int f = 0; f < 16; ++f) {
    bf[0][f] = *(const bf16x8*)(ws + WS_ATTF + moff + f * 1024);
    bf[1][f] = *(const bf16x8*)(ws + WS_ATTF + hoff + f * 1024);
  }
  u32x4 xr[2][4];
#pragma unroll
  for (int f = 0; f < 4; ++f) {
    xr[0][f] = *(const u32x4*)(ws + WS_X1F + moff + (4 * w + f) * 1024);
    xr[1][f] = *(const u32x4*)(ws + WS_X1F + hoff + (4 * w + f) * 1024);
  }
  u32x4 dwv = {0u, 0u, 0u, 0u};
  if (threadIdx.x < 160) dwv = *(const u32x4*)(dw + threadIdx.x * 16);
  st.start(pwo);
  if (threadIdx.x < 160) *(LAS u32x4*)(lds + L_DW + threadIdx.x * 16) = dwv;
  st.first();
  Panel p1a = make_panel(pw1 + w * 128, 1024, lane);
  f32x16 xv[2][2];
  {
    f32x16 a[2][2];
#pragma unroll
    for (int nb = 0; nb < 2; ++nb) { a[nb][0] = f32x16{}; a[nb][1] = f32x16{}; }
    gemm_k256<2, false, false, false>(st, pwo, p1a, a, bf, lds, NoWork{});
#pragma unroll
    for (int nb = 0; nb < 2; ++nb)
#pragma unroll
      for (int nt = 0; nt < 2; ++nt) {
        unpack8(xr[nb][2 * nt], xv[nb][nt], 0); unpack8(xr[nb][2 * nt + 1], xv[nb][nt], 1);
#pragma unroll
        for (int i = 0; i < 16; ++i) xv[nb][nt][i] = rbf(xv[nb][nt][i] + rbf(a[nb][nt][i]));
      }
  }
  ln_to_frags<2>(xv, lds, w, lane, bf);
  Panel pp2 = make_panel(pw2 + w * 128, 512, lane);
  {
    f32x16 a[2][2], g[2][2];
#pragma unroll
    for (int nb = 0; nb < 2; ++nb) { a[nb][0] = f32x16{}; a[nb][1] = f32x16{}; g[nb][0] = f32x16{}; g[nb][1] = f32x16{}; }
    Panel p1b = make_panel(pw1 + 512 + w * 128, 1024, lane);
    gemm_k256<2, false, false, false>(st, p1a, p1b, a, bf, lds, NoWork{});
    gemm_k256<2, false, false, false>(st, p1b, pp2, g, bf, lds, NoWork{});
    LAS char* G = lds + L_ACT + w * GSZ;
    lds_barrier();
#pragma unroll
    for (int nb = 0; nb < 2; ++nb) {
      const int row = nb == 0 ? r + 2 : (hc < 2 ? hc : 32 + hc);
      const bool wr = nb == 0 || r < 4;
      const float keep = (nb == 0 || hvalid) ? 1.0f : 0.0f;
#pragma unroll
      for (int nt = 0; nt < 2; ++nt)
#pragma unroll
        for (int q = 0; q < 4; ++q) {
          float e[4];
#pragma unroll
          for (int j = 0; j < 4; ++j) e[j] = keep * rbf(a[nb][nt][4 * q + j]) * rbf(sigmoidf_(rbf(g[nb][nt][4 * q + j])));
          if (wr) *(LAS u32x2*)(G + row * GROW + (nt * 32 + 8 * q + 4 * h) * 2) = (u32x2){pk2(e[0], e[1]), pk2(e[2], e[3])};
        }
    }
    asm volatile("s_waitcnt lgkmcnt(0)" ::: "memory");
    f32x16 c[2];
#pragma unroll
    for (int nt = 0; nt < 2; ++nt)
#pragma unroll
      for (int q = 0; q < 4; ++q) {
        float e[4] = {0.f, 0.f, 0.f, 0.f};
#pragma unroll
        for (int i = 0; i < 5; ++i) {
          const u32x2 gv = *(LAS u32x2*)(G + (r + i) * GROW + (nt * 32 + 8 * q + 4 * h) * 2);
          const u32x2 dv = *(LAS u32x2*)(lds + L_DW + i * 512 + (w * 64 + nt * 32 + 8 * q + 4 * h) * 2);
          e[0] += bflo(gv[0]) * bflo(dv[0]); e[1] += bfhi(gv[0]) * bfhi(dv[0]);
          e[2] += bflo(gv[1]) * bflo(dv[1]); e[3] += bfhi(gv[1]) * bfhi(dv[1]);
        }
#pragma unroll
        for (int j = 0; j < 4; ++j) c[nt][4 * q + j] = siluf_(e[j]);
      }
    lds_barrier();
#pragma unroll
    for (int nt = 0; nt < 2; ++nt)
#pragma unroll
      for (int s = 0; s < 2; ++s) *(LAS bf16x8*)(lds + L_ACT + (4 * w + 2 * nt + s) * 1024 + lane * 16) = pack8(c[nt], s);
    lds_barrier();
  }
  bf16x8 xf[1][16];
  f32x16 x3[1][2];
  Panel pa0 = make_panel(w2a + (size_t)w * 128, 2048, lane);
  {
    f32x16 a[1][2]; a[0][0] = f32x16{}; a[0][1] = f32x16{};
    gemm_k256<1, false, false, true>(st, pp2, pa0, a, xf, lds + L_ACT + lane * 16, NoWork{});
#pragma unroll
    for (int nt = 0; nt < 2; ++nt)
#pragma unroll
      for (int i = 0; i < 16; ++i) x3[0][nt][i] = rbf(xv[0][nt][i] + rbf(a[0][nt][i]));
  }
  ln_to_frags<1>(x3, lds, w, lane, xf);
  f32x16 acc[1][2];
  ff_block<true>(st, pa0, w2a, w2b, pa0, xf, lds, w, lane, acc);
#pragma unroll
  for (int nt = 0; nt < 2; ++nt)
#pragma unroll
    for (int i = 0; i < 16; ++i) x3[0][nt][i] = rbf(x3[0][nt][i] + 0.5f * rbf(acc[0][nt][i]));
  float mean[1], rstd[1];
  ln_stats<1>(x3, lds, w, lane, mean, rstd);
  char* orow = out + (size_t)(tb * 32 + r) * 512 + (w * 64 + 4 * h) * 2;
#pragma unroll
  for (int nt = 0; nt < 2; ++nt)
#pragma unroll
    for (int q = 0; q < 4; ++q) {
      float e[4];
#pragma unroll
      for (int j = 0; j < 4; ++j) e[j] = (x3[0][nt][4 * q + j] - mean[0]) * rstd[0];
      *(u32x2*)(orow + (nt * 32 + 8 * q) * 2) = (u32x2){pk2(e[0], e[1]), pk2(e[2], e[3])};
    }
}

extern "C" void kernel_launch(void* const* d_in, const int* in_sizes, int n_in, void* d_out, int out_size, void* d_ws, size_t ws_size,
                              hipStream_t stream) {
  const char* x = (const char*)d_in[0];
  const char* w1a = (const char*)d_in[1];
  const char* w1b = (const char*)d_in[2];
  const char* wqkv = (const char*)d_in[3];
  const char* wo = (const char*)d_in[4];
  const char* pw1 = (const char*)d_in[5];
  const char* dw = (const char*)d_in[6];
  const char* pw2 = (const char*)d_in[7];
  const char* w2a = (const char*)d_in[8];
  const char* w2b = (const char*)d_in[9];
  char* ws = (char*)d_ws;
  (void)hipFuncSetAttribute((const void*)k1_kernel, hipFuncAttributeMaxDynamicSharedMemorySize, L_TOTAL);
  (void)hipFuncSetAttribute((const void*)attn_kernel, hipFuncAttributeMaxDynamicSharedMemorySize, A_LDS);
  (void)hipFuncSetAttribute((const void*)k3_kernel, hipFuncAttributeMaxDynamicSharedMemorySize, L_TOTAL);
  k1_kernel<<<NTB, 320, L_TOTAL, stream>>>(x, w1a, w1b, wqkv, ws);
  attn_kernel<<<256, 256 * A_KG, A_LDS, stream>>>(ws, wo, pw1, pw2, w2a, w2b);
  k3_kernel<<<NTB, 256, L_TOTAL, stream>>>(wo, pw1, dw, pw2, w2a, w2b, ws, (char*)d_out);
}
```

```cpp
#include <hip/hip_runtime.h>
#include <hip/hip_bf16.h>
#include <stdint.h>

typedef short bf16x8 __attribute__((ext_vector_type(8)));
typedef short s16x4 __attribute__((ext_vector_type(4)));
typedef float f32x16 __attribute__((ext_vector_type(16)));
typedef float f32x2 __attribute__((ext_vector_type(2)));
typedef __bf16 bf16x2_t __attribute__((ext_vector_type(2)));
typedef unsigned u32x4 __attribute__((ext_vector_type(4)));
typedef unsigned u32x2 __attribute__((ext_vector_type(2)));
#define LAS __attribute__((address_space(3)))
#define DI __device__ __forceinline__
#define MFMA(a, b, c) __builtin_amdgcn_mfma_f32_32x32x16_bf16((a), (b), (c), 0, 0, 0)
#define MFMA16(a, b, c) __builtin_amdgcn_mfma_f32_16x16x32_bf16((a), (b), (c), 0, 0, 0)
#define SB() __builtin_amdgcn_sched_barrier(0)
#define STAMP(k, i) do {} while (0)
typedef float f32x4 __attribute__((ext_vector_type(4)));

constexpr int TSEQ = 4000, NBLK = 125, NTB = 250;
constexpr size_t WS_X1F = 0;
constexpr size_t WS_QF = 4u << 20;
constexpr size_t WS_KVF = 8u << 20;
constexpr size_t WS_ATTF = 17u << 20;
constexpr float LN_EPS = 1e-5f;
constexpr float LOG2E = 1.4426950408889634f;
constexpr float QSCALE = 0.125f * LOG2E;

DI unsigned pk2(float a, float b) { f32x2 v = {a, b}; return __builtin_bit_cast(unsigned, __builtin_convertvector(v, bf16x2_t)); }
DI float bflo(unsigned u) { return __uint_as_float(u << 16); }
DI float bfhi(unsigned u) { return __uint_as_float(u & 0xffff0000u); }
DI float rbf(float a) { return bflo(pk2(a, a)); }
DI bf16x8 pack8(const f32x16& v, int s) {
  u32x4 p = {pk2(v[8 * s], v[8 * s + 1]), pk2(v[8 * s + 2], v[8 * s + 3]), pk2(v[8 * s + 4], v[8 * s + 5]), pk2(v[8 * s + 6], v[8 * s + 7])};
  return __builtin_bit_cast(bf16x8, p);
}
DI void unpack8(u32x4 p, f32x16& v, int s) {
#pragma unroll
  for (int i = 0; i < 4; ++i) { v[8 * s + 2 * i] = bflo(p[i]); v[8 * s + 2 * i + 1] = bfhi(p[i]); }
}
DI float halfsum(float x) {
  auto r = __builtin_amdgcn_permlane32_swap(__float_as_uint(x), __float_as_uint(x), false, false);
  return __uint_as_float(r[0]) + __uint_as_float(r[1]);
}
DI float halfmax(float x) {
  auto r = __builtin_amdgcn_permlane32_swap(__float_as_uint(x), __float_as_uint(x), false, false);
  return fmaxf(__uint_as_float(r[0]), __uint_as_float(r[1]));
}
DI float sigmoidf_(float x) { return __builtin_amdgcn_rcpf(1.0f + __builtin_amdgcn_exp2f(-LOG2E * x)); }
DI float siluf_(float x) { return x * sigmoidf_(x); }
DI void lds_barrier() { asm volatile("s_waitcnt lgkmcnt(0)\n\ts_barrier" ::: "memory"); }
typedef short v4i16_t __attribute__((ext_vector_type(4)));
DI s16x4 trread(LAS const char* p) { return __builtin_bit_cast(s16x4, __builtin_amdgcn_ds_read_tr16_b64_v4i16((LAS v4i16_t*)p)); }
DI bf16x8 ldsfrag(LAS const char* p) { return *(LAS const bf16x8*)p; }

constexpr int SLAB = 4096, RING = 6, RINGB = SLAB * RING;
struct Panel { const char* sbase; unsigned ld32; unsigned v0, v1, v2, v3; };
DI Panel make_panel(const char* base, unsigned ld, int lane) {
  Panel p; p.sbase = base; p.ld32 = ld * 32;
  p.v0 = (unsigned)(lane >> 3) * ld + ((unsigned)((lane & 7) ^ (((lane >> 4) & 1) << 2)) << 4);
  p.v1 = p.v0 + (ld * 8 - 1024); p.v2 = p.v0 + (ld * 16 - 2048); p.v3 = p.v0 + (ld * 24 - 3072);
  return p;
}
struct Stream {
  unsigned fslot, cslot, ring0, rd0, rd1;
  bf16x8 cur[4];
  DI void init(unsigned ring_base, int lane) {
    ring0 = ring_base; fslot = 0; cslot = 0;
    const int h = lane >> 5, cb = (lane >> 4) & 1, q = (lane & 15) >> 2, p = lane & 3;
    rd0 = ring0 + (4 * h + q) * 128 + ((0 ^ (q >> 1)) * 64) + cb * 32 + p * 8;
    rd1 = ring0 + (4 * h + q) * 128 + ((1 ^ (q >> 1)) * 64) + cb * 32 + p * 8;
  }
  DI void issue_piece(Panel& p, int i) {
    if (i == 0) asm volatile("s_mov_b32 m0, %2\n\ts_nop 0\n\tglobal_load_lds_dwordx4 %0, %1" :: "v"(p.v0), "s"(p.sbase), "s"(ring0 + fslot));
    else if (i == 1) asm volatile("s_mov_b32 m0, %2\n\ts_nop 0\n\tglobal_load_lds_dwordx4 %0, %1 offset:1024" :: "v"(p.v1), "s"(p.sbase), "s"(ring0 + fslot));
    else if (i == 2) asm volatile("s_mov_b32 m0, %2\n\ts_nop 0\n\tglobal_load_lds_dwordx4 %0, %1 offset:2048" :: "v"(p.v2), "s"(p.sbase), "s"(ring0 + fslot));
    else {
      asm volatile("s_mov_b32 m0, %2\n\ts_nop 0\n\tglobal_load_lds_dwordx4 %0, %1 offset:3072" :: "v"(p.v3), "s"(p.sbase), "s"(ring0 + fslot));
      p.sbase += p.ld32; fslot = (fslot == RINGB - SLAB) ? 0u : fslot + SLAB;
    }
  }
  template <int N> DI void wait() { asm volatile("s_waitcnt vmcnt(%0) lgkmcnt(0)" :: "n"(N) : "memory"); }
  DI bf16x8 read1(int i) const {
    LAS const char* p = (LAS const char*)(uintptr_t)(((i & 1) ? rd1 : rd0) + cslot) + (i >> 1) * 2048;
    s16x4 l = trread(p), h = trread(p + 1024);
    return __builtin_shufflevector(l, h, 0, 1, 2, 3, 4, 5, 6, 7);
  }
  DI void next() { cslot = (cslot == RINGB - SLAB) ? 0u : cslot + SLAB; }
  DI void start(Panel& p) {
#pragma unroll 1
    for (int i = 0; i < RING; ++i) { issue_piece(p, 0); issue_piece(p, 1); issue_piece(p, 2); issue_piece(p, 3); }
  }
  DI void first() {
    wait<4 * (RING - 1)>();
#pragma unroll
    for (int i = 0; i < 4; ++i) cur[i] = read1(i);
    next();
  }
};
struct NoWork { DI void init() {} DI void step(int, int) {} DI void fini() {} };
struct SiluWork {
  LAS char* p;
  u32x2 raw; float x[4], e[4];
  DI void init() { raw = *(LAS u32x2*)p; }
  DI void step(int sl, int i) {
    if (i == 0) {
      x[0] = bflo(raw[0]); x[1] = bfhi(raw[0]); x[2] = bflo(raw[1]); x[3] = bfhi(raw[1]);
#pragma unroll
      for (int j = 0; j < 4; ++j) e[j] = -LOG2E * x[j];
      const int n = sl + 1;
      raw = *(LAS u32x2*)(p + ((n >> 1) & 3) * 1024 + (n & 1) * 8);
    } else if (i == 1) {
#pragma unroll
      for (int j = 0; j < 4; ++j) e[j] = __builtin_amdgcn_exp2f(e[j]);
    } else if (i == 2) {
#pragma unroll
      for (int j = 0; j < 4; ++j) e[j] = __builtin_amdgcn_rcpf(1.0f + e[j]);
    } else {
      *(LAS u32x2*)(p + (sl >> 1) * 1024 + (sl & 1) * 8) = (u32x2){pk2(x[0] * e[0], x[1] * e[1]), pk2(x[2] * e[2], x[3] * e[3])};
    }
  }
  DI void fini() {}
};
template <int NB, bool SWAP, bool LAST, class W>
DI void gemm_k256(Stream& st, Panel& pc, Panel& pn, f32x16 (&acc)[NB][2], LAS const char* bl, W work) {
  bf16x8 bq[NB][2];
#pragma unroll
  for (int nb = 0; nb < NB; ++nb) { bq[nb][0] = ldsfrag(bl + nb * 16384); bq[nb][1] = ldsfrag(bl + nb * 16384 + 1024); }
  work.init();
  Panel pi = pc;
#pragma unroll 1
  for (int sl = 0; sl < 8; ++sl) {
    bf16x8 nxt[4], bn[NB][2];
    if (sl == 8 - RING) pi = pn;
    if (!LAST || sl < 3) st.template wait<4 * (RING - 2)>();
    else if (sl == 3) st.template wait<12>();
    else if (sl == 4) st.template wait<8>();
    else if (sl == 5) st.template wait<4>();
    else st.template wait<0>();
    const int bnext = ((sl + 1) & 7) * 2048;
    SB();
#pragma unroll
    for (int i = 0; i < 4; ++i) {
      const int ks = i >> 1, nt = i & 1;
#pragma unroll
      for (int nb = 0; nb < NB; ++nb)
        acc[nb][nt] = SWAP ? MFMA(bq[nb][ks], st.cur[i], acc[nb][nt]) : MFMA(st.cur[i], bq[nb][ks], acc[nb][nt]);
      nxt[i] = st.read1(i);
      if (i & 1) {
#pragma unroll
        for (int nb = 0; nb < NB; ++nb) bn[nb][ks] = ldsfrag(bl + nb * 16384 + bnext + ks * 1024);
      }
      if (!LAST || sl < 8 - RING) st.issue_piece(pi, i);
      work.step(sl, i);
      SB();
    }
    st.next();
#pragma unroll
    for (int i = 0; i < 4; ++i) st.cur[i] = nxt[i];
#pragma unroll
    for (int nb = 0; nb < NB; ++nb) { bq[nb][0] = bn[nb][0]; bq[nb][1] = bn[nb][1]; }
  }
  pn = pi;
}

constexpr int L_RING = 0;
constexpr int L_X = 4 * RINGB, L_Y = L_X + 16384, L_Z = L_Y + 16384;
constexpr int L_STAT = L_Z + 16384;
constexpr int GROW = 136, GSZ = 36 * GROW;
constexpr int L_DW = L_STAT + 2048;
constexpr int L_TOTAL = L_DW + 2560;

template <int NB>
DI void ln_stats(const f32x16 (&v)[NB][2], LAS char* lds, int w, int lane, float (&mean)[NB], float (&rstd)[NB]) {
  const int r = lane & 31;
#pragma unroll
  for (int nb = 0; nb < NB; ++nb) {
    const f32x16 t = v[nb][0] + v[nb][1];
    const float s = ((t[0] + t[1]) + (t[2] + t[3])) + ((t[4] + t[5]) + (t[6] + t[7])) + (((t[8] + t[9]) + (t[10] + t[11])) + ((t[12] + t[13]) + (t[14] + t[15])));
    const float mw = halfsum(s) * (1.0f / 64.0f);
    const f32x16 d0 = v[nb][0] - mw, d1 = v[nb][1] - mw;
    const f32x16 q = d0 * d0 + d1 * d1;
    const float m2 = halfsum(((q[0] + q[1]) + (q[2] + q[3])) + ((q[4] + q[5]) + (q[6] + q[7])) + (((q[8] + q[9]) + (q[10] + q[11])) + ((q[12] + q[13]) + (q[14] + q[15]))));
    if (lane < 32) *(LAS f32x2*)(lds + L_STAT + ((nb * 4 + w) * 32 + r) * 8) = (f32x2){mw, m2};
  }
  lds_barrier();
#pragma unroll
  for (int nb = 0; nb < NB; ++nb) {
    f32x2 s0 = *(LAS f32x2*)(lds + L_STAT + ((nb * 4 + 0) * 32 + r) * 8), s1 = *(LAS f32x2*)(lds + L_STAT + ((nb * 4 + 1) * 32 + r) * 8);
    f32x2 s2 = *(LAS f32x2*)(lds + L_STAT + ((nb * 4 + 2) * 32 + r) * 8), s3 = *(LAS f32x2*)(lds + L_STAT + ((nb * 4 + 3) * 32 + r) * 8);
    const float m = 0.25f * (s0[0] + s1[0] + s2[0] + s3[0]);
    const float e0 = s0[0] - m, e1 = s1[0] - m, e2 = s2[0] - m, e3 = s3[0] - m;
    const float M2 = s0[1] + s1[1] + s2[1] + s3[1] + 64.0f * (e0 * e0 + e1 * e1 + e2 * e2 + e3 * e3);
    mean[nb] = m; rstd[nb] = __builtin_amdgcn_rsqf(M2 * (1.0f / 256.0f) + LN_EPS);
  }
}
template <int NB>
DI void ln_to_frags(const f32x16 (&v)[NB][2], LAS char* lds, int w, int lane) {
  float mean[NB], rstd[NB];
  ln_stats<NB>(v, lds, w, lane, mean, rstd);
  LAS char* fb = lds + L_X + w * 4096 + lane * 16;
#pragma unroll
  for (int nb = 0; nb < NB; ++nb)
#pragma unroll
    for (int nt = 0; nt < 2; ++nt) {
      const f32x16 n = (v[nb][nt] - mean[nb]) * rstd[nb];
#pragma unroll
      for (int s = 0; s < 2; ++s) *(LAS bf16x8*)(fb + nb * 16384 + (2 * nt + s) * 1024) = pack8(n, s);
    }
  lds_barrier();
}

template <bool LAST>
DI void ff_block(Stream& st, Panel& pa0, const char* wa, const char* wb, Panel& pnext, LAS char* lds, int w, int lane, f32x16 (&out)[1][2]) {
  out[0][0] = f32x16{}; out[0][1] = f32x16{};
  LAS char* xb = lds + L_X + lane * 16;
  LAS char* yb = lds + L_Y + lane * 16;
  Panel pc = pa0;
#pragma unroll 1
  for (int c = 0; c < 4; ++c) {
    Panel pn = (c == 0) ? make_panel(wa + (size_t)(w + 4) * 128, 2048, lane) : make_panel(wb + (size_t)(c - 1) * 256 * 512 + w * 128, 512, lane);
    f32x16 hn[1][2]; hn[0][0] = f32x16{}; hn[0][1] = f32x16{};
    if (c == 0) gemm_k256<1, false, false>(st, pc, pn, hn, xb, NoWork{});
    else gemm_k256<1, false, false>(st, pc, pn, hn, xb, SiluWork{yb + ((c - 1) & 1) * 16384 + w * 4096});
    STAMP(LAST ? 2 : 0, 10 + 3 * c);
    if (c > 0) lds_barrier();
#pragma unroll
    for (int f = 0; f < 4; ++f) *(LAS bf16x8*)(yb + (c & 1) * 16384 + w * 4096 + f * 1024) = pack8(hn[0][f >> 1], f & 1);
    if (c > 0) {
      pc = pn;
      pn = (c < 3) ? make_panel(wa + (size_t)(w + 4 * c + 4) * 128, 2048, lane) : make_panel(wb + (size_t)3 * 256 * 512 + w * 128, 512, lane);
      if (c < 3) gemm_k256<1, false, false>(st, pc, pn, out, yb + ((c - 1) & 1) * 16384, NoWork{});
      else gemm_k256<1, false, false>(st, pc, pn, out, yb, SiluWork{yb + 16384 + w * 4096});
      STAMP(LAST ? 2 : 0, 11 + 3 * c);
    }
    pc = pn;
  }
  lds_barrier();
  gemm_k256<1, false, LAST>(st, pc, pnext, out, yb + 16384, NoWork{});
}

DI void l2_warm(const char* p, int kb_total, int idx, int lane, u32x4& t) {
  const int per = (kb_total + 30) / 31;
  int b = idx * per, e = b + per; if (e > kb_total) e = kb_total;
  for (int i = b; i < e; ++i)
    asm volatile("global_load_dwordx4 %0, %1, %2" : "+v"(t) : "v"(lane * 16 + i * 1024), "s"(p));
}

__global__ __launch_bounds__(320, 1) void k1_kernel(const char* __restrict__ x, const char* __restrict__ w1a, const char* __restrict__ w1b,
                                                    const char* __restrict__ wqkv, char* __restrict__ ws) {
  extern __shared__ __attribute__((aligned(16))) char smem[];
  LAS char* lds = (LAS char*)smem;
  const int lane = threadIdx.x & 63, w = __builtin_amdgcn_readfirstlane(threadIdx.x >> 6), r = lane & 31, h = lane >> 5;
  const int tb = blockIdx.x;
  if (w == 4) {
    const int idx = tb >> 3;
    u32x4 t = {0u, 0u, 0u, 0u};
    if (idx < 31) { l2_warm(w1a, 512, idx, lane, t); l2_warm(w1b, 512, idx, lane, t); l2_warm(wqkv, 384, idx, lane, t); }
    asm volatile("s_barrier\n\ts_barrier\n\ts_waitcnt vmcnt(0)" : "+v"(t) :: "memory");
    return;
  }
  STAMP(0, 0);
  Stream st;
  st.init((unsigned)(uintptr_t)(lds + L_RING + w * RINGB), lane);
  Panel pa0 = make_panel(w1a + (size_t)w * 128, 2048, lane);
  f32x16 xv[1][2];
  {
    const char* xrow = x + (size_t)(tb * 32 + r) * 512 + (w * 64 + 4 * h) * 2;
    u32x2 u[8];
#pragma unroll
    for (int g = 0; g < 8; ++g) asm volatile("global_load_dwordx2 %0, %1, off offset:%2" : "=v"(u[g]) : "v"(xrow), "n"(g * 16));
    st.start(pa0);
    asm volatile("s_waitcnt vmcnt(%8)" : "+v"(u[0]), "+v"(u[1]), "+v"(u[2]), "+v"(u[3]), "+v"(u[4]), "+v"(u[5]), "+v"(u[6]), "+v"(u[7]) : "n"(4 * RING));
#pragma unroll
    for (int g = 0; g < 8; ++g) {
      xv[0][g >> 2][4 * (g & 3)] = bflo(u[g][0]); xv[0][g >> 2][4 * (g & 3) + 1] = bfhi(u[g][0]);
      xv[0][g >> 2][4 * (g & 3) + 2] = bflo(u[g][1]); xv[0][g >> 2][4 * (g & 3) + 3] = bfhi(u[g][1]);
    }
  }
  STAMP(0, 1);
  ln_to_frags<1>(xv, lds, w, lane);
  st.first();
  STAMP(0, 2);
  f32x16 acc[1][2];
  Panel pq = make_panel(wqkv + w * 128, 1536, lane);
  ff_block<false>(st, pa0, w1a, w1b, pq, lds, w, lane, acc);
  STAMP(0, 3);
#pragma unroll
  for (int nt = 0; nt < 2; ++nt) {
#pragma unroll
    for (int i = 0; i < 16; ++i) xv[0][nt][i] = rbf(xv[0][nt][i] + 0.5f * acc[0][nt][i]);
#pragma unroll
    for (int s = 0; s < 2; ++s) *(bf16x8*)(ws + WS_X1F + ((size_t)tb * 16 + 4 * w + 2 * nt + s) * 1024 + lane * 16) = pack8(xv[0][nt], s);
  }
  ln_to_frags<1>(xv, lds, w, lane);
  STAMP(0, 4);
  const int b = tb / NBLK, blk = tb - b * NBLK, bh = b * 4 + w;
  char* kvdst = ws + WS_KVF + (size_t)(bh * NBLK + blk) * 8192 + lane * 16;
#pragma unroll 1
  for (int c = 0; c < 2; ++c) {
    Panel pc = pq;
    pq = make_panel(wqkv + (c + 1) * 512 + w * 128, 1536, lane);
    f32x16 a[1][2]; a[0][0] = f32x16{}; a[0][1] = f32x16{};
    gemm_k256<1, false, false>(st, pc, pq, a, lds + L_X + lane * 16, NoWork{});
    const float sc = c == 0 ? QSCALE : 1.0f;
    char* dst = c == 0 ? ws + WS_QF + (size_t)(bh * NBLK + blk) * 4096 + lane * 16 : kvdst;
#pragma unroll
    for (int nt = 0; nt < 2; ++nt) {
      a[0][nt] = a[0][nt] * sc;
#pragma unroll
      for (int s = 0; s < 2; ++s) *(bf16x8*)(dst + (2 * nt + s) * 1024) = pack8(a[0][nt], s);
    }
  }
  STAMP(0, 5);
  {
    f32x16 a[1][2]; a[0][0] = f32x16{}; a[0][1] = f32x16{};
    gemm_k256<1, true, true>(st, pq, pq, a, lds + L_X + lane * 16, NoWork{});
#pragma unroll
    for (int nt = 0; nt < 2; ++nt)
#pragma unroll
      for (int s = 0; s < 2; ++s) *(bf16x8*)(kvdst + (4 + 2 * nt + s) * 1024) = pack8(a[0][nt], s);
  }
  STAMP(0, 29);
}

constexpr int A_KG = 2, A_SBLK = A_KG * 2, A_HST = A_SBLK * 4096, A_NST = (NBLK + A_SBLK - 1) / A_SBLK, A_NFULL = NBLK / A_SBLK;
constexpr int A_VOFF = 3 * A_HST, A_LDS = 6 * A_HST;
__global__ __launch_bounds__(256 * A_KG, A_KG) void attn_kernel(char* __restrict__ ws, const char* __restrict__ wo, const char* __restrict__ pw1,
                                                                const char* __restrict__ pw2, const char* __restrict__ w2a, const char* __restrict__ w2b) {
  extern __shared__ __attribute__((aligned(16))) char smem[];
  LAS char* lds = (LAS char*)smem;
  const int lane = threadIdx.x & 63, w = __builtin_amdgcn_readfirstlane(threadIdx.x >> 6);
  const int bh = blockIdx.x & 7, qg = blockIdx.x >> 3, qi = w & 3, kg = w >> 2;
  const int qb_raw = qg * 4 + qi, qb = qb_raw < NBLK ? qb_raw : NBLK - 1;
  STAMP(1, 0);
  const char* kv = ws + WS_KVF + (size_t)bh * NBLK * 8192;
  const unsigned lds0 = (unsigned)(uintptr_t)lds;
  const unsigned voff = (w >> 1) * 8192 + (w & 1) * 2048 + lane * 16;
  auto dma2 = [&](int st, int isv, unsigned ldsoff) {
    asm volatile("s_mov_b32 m0, %2\n\ts_nop 0\n\tglobal_load_lds_dwordx4 %0, %1\n\tglobal_load_lds_dwordx4 %0, %1 offset:1024"
                 :: "v"(voff), "s"(kv + (size_t)st * (2 * A_HST) + isv * 4096), "s"(lds0 + ldsoff + w * 2048));
  };
  dma2(0, 0, 0); dma2(1, 0, A_HST); dma2(0, 1, A_VOFF); dma2(2, 0, 2 * A_HST); dma2(1, 1, A_VOFF + A_HST);
  bf16x8 qf[4];
#pragma unroll
  for (int f = 0; f < 4; ++f) qf[f] = *(const bf16x8*)(ws + WS_QF + ((size_t)(bh * NBLK + qb) * 4 + f) * 1024 + lane * 16);
#pragma unroll
  for (int f = 0; f < 4; ++f) asm volatile("" : "+v"(qf[f]));
  u32x4 wt = {0u, 0u, 0u, 0u};
  {
    const int i0 = (qg * 8 + w) * 6;
#pragma unroll
    for (int i = 0; i < 6; ++i) {
      const int kb = i0 + i;
      const char* p = kb < 128 ? wo + kb * 1024 : kb < 384 ? pw1 + (kb - 128) * 1024 : kb < 512 ? pw2 + (kb - 384) * 1024 : kb < 1024 ? w2a + (kb - 512) * 1024 : w2b + (kb - 1024) * 1024;
      asm volatile("global_load_dwordx4 %0, %1, %2" : "+v"(wt) : "v"(lane * 16), "s"(p));
    }
  }
  const int rem = NBLK - A_NFULL * A_SBLK - kg * 2;
  const int ntail = rem < 0 ? 0 : (rem > 2 ? 2 : rem);
  LAS const char* base = lds + kg * 8192 + lane * 16;
  f32x16 o0 = f32x16{}, o1 = f32x16{}, negr, sa, sb;
  f32x4 lacc = {0.f, 0.f, 0.f, 0.f};
  bf16x8 sel;
  { const short one = (((lane >> 4) ^ lane) & 1) ? (short)0 : (short)0x3f80;
#pragma unroll
    for (int j = 0; j < 8; ++j) sel[j] = one; }
  bf16x8 kf[4], vf[4]; unsigned pa[8], pb[8];
#pragma unroll
  for (int i = 0; i < 8; ++i) pa[i] = 0u;
  float ref;
  asm volatile("s_waitcnt vmcnt(0)\n\ts_barrier" : "+v"(wt) :: "memory");
  {
#pragma unroll
    for (int f = 0; f < 4; ++f) kf[f] = ldsfrag(base + f * 1024);
    f32x16 z = f32x16{};
    sa = MFMA(kf[0], qf[0], z); sa = MFMA(kf[1], qf[1], sa); sa = MFMA(kf[2], qf[2], sa); sa = MFMA(kf[3], qf[3], sa);
    float mx = sa[0];
#pragma unroll
    for (int i = 1; i < 16; ++i) mx = fmaxf(mx, sa[i]);
    ref = halfmax(mx) + 7.0f;
#pragma unroll
    for (int i = 0; i < 16; ++i) { sa[i] -= ref; negr[i] = -ref; }
#pragma unroll
    for (int f = 0; f < 4; ++f) { kf[f] = ldsfrag(base + 4096 + f * 1024); vf[f] = bf16x8{}; }
  }
#define SLOT3(MF, RF, i) do { MF; RF; e[2*(i)] = __builtin_amdgcn_exp2f(SC_[2*(i)]); e[2*(i)+1] = __builtin_amdgcn_exp2f(SC_[2*(i)+1]); \
    if ((i) > 0) PN_[(i)-1] = pk2(e[2*(i)-2], e[2*(i)-1]); SB(); } while (0)
  auto iter = [&](f32x16& SC_, f32x16& SN_, unsigned (&PP_)[8], unsigned (&PN_)[8], LAS const char* kn, LAS const char* vc) {
    float e[16];
    const bf16x8 p0 = __builtin_bit_cast(bf16x8, (u32x4){PP_[0], PP_[1], PP_[2], PP_[3]}), p1 = __builtin_bit_cast(bf16x8, (u32x4){PP_[4], PP_[5], PP_[6], PP_[7]});
    SB();
    SLOT3(SN_ = MFMA(kf[0], qf[0], negr), kf[0] = ldsfrag(kn), 0);
    SLOT3(o0 = MFMA(vf[0], p0, o0), vf[0] = ldsfrag(vc), 1);
    SLOT3(SN_ = MFMA(kf[1], qf[1], SN_), kf[1] = ldsfrag(kn + 1024), 2);
    SLOT3(o1 = MFMA(vf[2], p0, o1); lacc = MFMA16(sel, p0, lacc), vf[2] = ldsfrag(vc + 2048), 3);
    SLOT3(SN_ = MFMA(kf[2], qf[2], SN_), kf[2] = ldsfrag(kn + 2048), 4);
    SLOT3(o0 = MFMA(vf[1], p1, o0), vf[1] = ldsfrag(vc + 1024), 5);
    SLOT3(SN_ = MFMA(kf[3], qf[3], SN_), kf[3] = ldsfrag(kn + 3072), 6);
    SLOT3(o1 = MFMA(vf[3], p1, o1); lacc = MFMA16(sel, p1, lacc), vf[3] = ldsfrag(vc + 3072), 7);
    PN_[7] = pk2(e[14], e[15]);
    const unsigned orall = (PN_[0] | PN_[1] | PN_[2]) | (PN_[3] | PN_[4] | PN_[5]) | (PN_[6] | PN_[7]);
    if (__builtin_expect(__any((orall & 0x40004000u) != 0u), 0)) {
      float mx = SC_[0];
#pragma unroll
      for (int i = 1; i < 16; ++i) mx = fmaxf(mx, SC_[i]);
      mx = halfmax(mx);
      const float delta = (mx >= 0.5f) ? mx + 7.0f : 0.0f;
      const float alpha = __builtin_amdgcn_exp2f(-delta);
      const float alo = __shfl_xor(alpha, 16);
      const float aA = (lane & 16) ? alo : alpha, aB = (lane & 16) ? alpha : alo;
      lacc = lacc * (f32x4){aA, aB, aA, aB};
      o0 = o0 * alpha; o1 = o1 * alpha; negr = negr - delta; SN_ = SN_ - delta;
      asm volatile("" : "+v"(o0), "+v"(o1), "+v"(negr), "+v"(SN_), "+v"(lacc));
      ref = -negr[0];
#pragma unroll
      for (int i = 0; i < 8; ++i) PN_[i] = pk2(__builtin_amdgcn_exp2f(SC_[2*i] - delta), __builtin_amdgcn_exp2f(SC_[2*i+1] - delta));
    }
  };
  STAMP(1, 1);
  int r0 = 0, r1 = A_HST, r2 = 2 * A_HST;
  for (int st = 0; st < A_NFULL; ++st) {
    asm volatile("s_waitcnt vmcnt(4) lgkmcnt(0)\n\ts_barrier" ::: "memory");
    dma2(st + 3 < A_NST ? st + 3 : A_NST - 1, 0, r0); dma2(st + 2 < A_NST ? st + 2 : A_NST - 1, 1, A_VOFF + r2);
    iter(sa, sb, pa, pb, base + r1, base + A_VOFF + r0);
    iter(sb, sa, pb, pa, base + r1 + 4096, base + A_VOFF + r0 + 4096);
    const int t = r0; r0 = r1; r1 = r2; r2 = t;
  }
  asm volatile("s_waitcnt vmcnt(0) lgkmcnt(0)\n\ts_barrier" ::: "memory");
  if (ntail > 0) {
    iter(sa, sb, pa, pb, base + r1, base + A_VOFF + r0);
    if (ntail > 1) iter(sb, sa, pb, pa, base + r1 + 4096, base + A_VOFF + r0 + 4096);
    else {
#pragma unroll
      for (int i = 0; i < 8; ++i) pa[i] = pb[i];
    }
  }
  {
    const bf16x8 p0 = __builtin_bit_cast(bf16x8, (u32x4){pa[0], pa[1], pa[2], pa[3]}), p1 = __builtin_bit_cast(bf16x8, (u32x4){pa[4], pa[5], pa[6], pa[7]});
    o0 = MFMA(vf[0], p0, o0); o1 = MFMA(vf[2], p0, o1); o0 = MFMA(vf[1], p1, o0); o1 = MFMA(vf[3], p1, o1);
    lacc = MFMA16(sel, p0, lacc); lacc = MFMA16(sel, p1, lacc);
  }
  float lsum = (lane & 16) ? lacc[1] : lacc[0];
  STAMP(1, 2);
  lds_barrier();
  if (kg > 0) {
    LAS char* cb = lds + ((kg - 1) * 4 + qi) * 9216 + lane * 16;
#pragma unroll
    for (int g = 0; g < 4; ++g) {
      *(LAS f32x4*)(cb + g * 1024) = (f32x4){o0[4 * g], o0[4 * g + 1], o0[4 * g + 2], o0[4 * g + 3]};
      *(LAS f32x4*)(cb + 4096 + g * 1024) = (f32x4){o1[4 * g], o1[4 * g + 1], o1[4 * g + 2], o1[4 * g + 3]};
    }
    *(LAS f32x2*)(lds + ((kg - 1) * 4 + qi) * 9216 + 8192 + lane * 8) = (f32x2){ref, lsum};
  }
  lds_barrier();
  if (kg == 0 && qb_raw < NBLK) {
#pragma unroll
    for (int k = 1; k < A_KG; ++k) {
      LAS char* cb = lds + ((k - 1) * 4 + qi) * 9216 + lane * 16;
      const f32x2 rl = *(LAS f32x2*)(lds + ((k - 1) * 4 + qi) * 9216 + 8192 + lane * 8);
      const float m = fmaxf(ref, rl[0]);
      const float c0 = __builtin_amdgcn_exp2f(ref - m), c1 = __builtin_amdgcn_exp2f(rl[0] - m);
      lsum = lsum * c0 + rl[1] * c1; ref = m;
#pragma unroll
      for (int g = 0; g < 4; ++g) {
        const f32x4 u0 = *(LAS f32x4*)(cb + g * 1024), u1 = *(LAS f32x4*)(cb + 4096 + g * 1024);
#pragma unroll
        for (int j = 0; j < 4; ++j) { o0[4 * g + j] = o0[4 * g + j] * c0 + u0[j] * c1; o1[4 * g + j] = o1[4 * g + j] * c0 + u1[j] * c1; }
      }
    }
    const float inv = 1.0f / lsum;
#pragma unroll
    for (int i = 0; i < 16; ++i) { o0[i] *= inv; o1[i] *= inv; }
    char* dst = ws + WS_ATTF + ((size_t)((bh >> 2) * NBLK + qb) * 16 + (bh & 3) * 4) * 1024 + lane * 16;
    *(bf16x8*)(dst) = pack8(o0, 0); *(bf16x8*)(dst + 1024) = pack8(o0, 1);
    *(bf16x8*)(dst + 2048) = pack8(o1, 0); *(bf16x8*)(dst + 3072) = pack8(o1, 1);
  }
  STAMP(1, 29);
}

__global__ __launch_bounds__(256, 1) void k3_kernel(const char* __restrict__ wo, const char* __restrict__ pw1, const char* __restrict__ dw,
                                                    const char* __restrict__ pw2, const char* __restrict__ w2a, const char* __restrict__ w2b,
                                                    const char* __restrict__ ws, char* __restrict__ out) {
  extern __shared__ __attribute__((aligned(16))) char smem[];
  LAS char* lds = (LAS char*)smem;
  const int lane = threadIdx.x & 63, w = __builtin_amdgcn_readfirstlane(threadIdx.x >> 6), r = lane & 31, h = lane >> 5;
  const int tb = blockIdx.x, b = tb / NBLK, t0 = (tb - b * NBLK) * 32;
  STAMP(2, 0);
  Stream st;
  st.init((unsigned)(uintptr_t)(lds + L_RING + w * RINGB), lane);
  Panel pwo = make_panel(wo + w * 128, 512, lane);
  const int hc = r & 3, ht = (hc < 2) ? t0 - 2 + hc : t0 + 30 + hc;
  const bool hvalid = ht >= 0 && ht < TSEQ;
  const int hg = b * TSEQ + (ht < 0 ? 0 : (ht >= TSEQ ? TSEQ - 1 : ht));
  const unsigned hoff = (unsigned)(hg >> 5) * 16384 + ((hg & 31) + 32 * h) * 16 + w * 4096;
  const unsigned moff = (unsigned)tb * 16384 + lane * 16 + w * 4096;
  {
    const unsigned ly = (unsigned)(uintptr_t)(lds + L_Y + w * 4096);
    asm volatile("s_mov_b32 m0, %3\n\ts_nop 0\n\tglobal_load_lds_dwordx4 %0, %2\n\tglobal_load_lds_dwordx4 %0, %2 offset:1024\n\t"
                 "global_load_lds_dwordx4 %0, %2 offset:2048\n\tglobal_load_lds_dwordx4 %0, %2 offset:3072\n\t"
                 "s_mov_b32 m0, %4\n\ts_nop 0\n\tglobal_load_lds_dwordx4 %1, %2\n\tglobal_load_lds_dwordx4 %1, %2 offset:1024\n\t"
                 "global_load_lds_dwordx4 %1, %2 offset:2048\n\tglobal_load_lds_dwordx4 %1, %2 offset:3072"
                 :: "v"(moff), "v"(hoff), "s"(ws + WS_ATTF), "s"(ly), "s"(ly + 16384));
  }
  u32x4 xr[2][4], dwv;
#pragma unroll
  for (int f = 0; f < 4; ++f) {
    asm volatile("global_load_dwordx4 %0, %1, %2 offset:%3" : "=v"(xr[0][f]) : "v"(moff), "s"(ws + WS_X1F), "n"(f * 1024));
    asm volatile("global_load_dwordx4 %0, %1, %2 offset:%3" : "=v"(xr[1][f]) : "v"(hoff), "s"(ws + WS_X1F), "n"(f * 1024));
  }
  asm volatile("global_load_dwordx4 %0, %1, %2" : "=v"(dwv) : "v"((threadIdx.x < 160 ? threadIdx.x : 159) * 16), "s"(dw));
  st.start(pwo);
  asm volatile("s_waitcnt vmcnt(%9)" : "+v"(xr[0][0]), "+v"(xr[0][1]), "+v"(xr[0][2]), "+v"(xr[0][3]), "+v"(xr[1][0]), "+v"(xr[1][1]), "+v"(xr[1][2]), "+v"(xr[1][3]), "+v"(dwv)
               : "n"(4 * RING) : "memory");
  if (threadIdx.x < 160) *(LAS u32x4*)(lds + L_DW + threadIdx.x * 16) = dwv;
  st.first();
  lds_barrier();
  STAMP(2, 1);
  Panel p1a = make_panel(pw1 + w * 128, 1024, lane);
  f32x16 xv[2][2];
  {
#pragma unroll
    for (int nb = 0; nb < 2; ++nb)
#pragma unroll
      for (int nt = 0; nt < 2; ++nt) { unpack8(xr[nb][2 * nt], xv[nb][nt], 0); unpack8(xr[nb][2 * nt + 1], xv[nb][nt], 1); }
    gemm_k256<2, false, false>(st, pwo, p1a, xv, lds + L_Y + lane * 16, NoWork{});
  }
  STAMP(2, 2);
  ln_to_frags<2>(xv, lds, w, lane);
  STAMP(2, 3);
  Panel pp2 = make_panel(pw2 + w * 128, 512, lane);
  {
    f32x16 a[2][2], g[2][2];
#pragma unroll
    for (int nb = 0; nb < 2; ++nb) { a[nb][0] = f32x16{}; a[nb][1] = f32x16{}; g[nb][0] = f32x16{}; g[nb][1] = f32x16{}; }
    Panel p1b = make_panel(pw1 + 512 + w * 128, 1024, lane);
    gemm_k256<2, false, false>(st, p1a, p1b, a, lds + L_X + lane * 16, NoWork{});
    gemm_k256<2, false, false>(st, p1b, pp2, g, lds + L_X + lane * 16, NoWork{});
    STAMP(2, 4);
    LAS char* G = lds + L_Y + w * GSZ;
    lds_barrier();
#pragma unroll
    for (int nb = 0; nb < 2; ++nb) {
      const int row = nb == 0 ? r + 2 : (hc < 2 ? hc : 32 + hc);
      const bool wr = nb == 0 || r < 4;
      const float keep = (nb == 0 || hvalid) ? 1.0f : 0.0f;
#pragma unroll
      for (int nt = 0; nt < 2; ++nt)
#pragma unroll
        for (int q = 0; q < 4; ++q) {
          float e[4];
#pragma unroll
          for (int j = 0; j < 4; ++j) e[j] = keep * a[nb][nt][4 * q + j] * sigmoidf_(g[nb][nt][4 * q + j]);
          if (wr) *(LAS u32x2*)(G + row * GROW + (nt * 32 + 8 * q + 4 * h) * 2) = (u32x2){pk2(e[0], e[1]), pk2(e[2], e[3])};
        }
    }
    asm volatile("s_waitcnt lgkmcnt(0)" ::: "memory");
#pragma unroll
    for (int nt = 0; nt < 2; ++nt) {
      f32x16 c;
#pragma unroll
      for (int q = 0; q < 4; ++q) {
        float e[4] = {0.f, 0.f, 0.f, 0.f};
#pragma unroll
        for (int i = 0; i < 5; ++i) {
          const u32x2 gv = *(LAS u32x2*)(G + (r + i) * GROW + (nt * 32 + 8 * q + 4 * h) * 2);
          const u32x2 dv = *(LAS u32x2*)(lds + L_DW + i * 512 + (w * 64 + nt * 32 + 8 * q + 4 * h) * 2);
          e[0] += bflo(gv[0]) * bflo(dv[0]); e[1] += bfhi(gv[0]) * bfhi(dv[0]);
          e[2] += bflo(gv[1]) * bflo(dv[1]); e[3] += bfhi(gv[1]) * bfhi(dv[1]);
        }
#pragma unroll
        for (int j = 0; j < 4; ++j) c[4 * q + j] = siluf_(e[j]);
      }
#pragma unroll
      for (int s = 0; s < 2; ++s) *(LAS bf16x8*)(lds + L_X + (4 * w + 2 * nt + s) * 1024 + lane * 16) = pack8(c, s);
    }
    lds_barrier();
  }
  STAMP(2, 5);
  f32x16 x3[1][2];
  x3[0][0] = xv[0][0]; x3[0][1] = xv[0][1];
  Panel pa0 = make_panel(w2a + (size_t)w * 128, 2048, lane);
  gemm_k256<1, false, false>(st, pp2, pa0, x3, lds + L_X + lane * 16, NoWork{});
  STAMP(2, 6);
  ln_to_frags<1>(x3, lds, w, lane);
  STAMP(2, 7);
  f32x16 acc[1][2];
  ff_block<true>(st, pa0, w2a, w2b, pa0, lds, w, lane, acc);
  x3[0][0] += 0.5f * acc[0][0]; x3[0][1] += 0.5f * acc[0][1];
  STAMP(2, 8);
  float mean[1], rstd[1];
  ln_stats<1>(x3, lds, w, lane, mean, rstd);
  char* orow = out + (size_t)(tb * 32 + r) * 512 + (w * 64 + 4 * h) * 2;
#pragma unroll
  for (int nt = 0; nt < 2; ++nt) {
    const f32x16 n = (x3[0][nt] - mean[0]) * rstd[0];
#pragma unroll
    for (int q = 0; q < 4; ++q) *(u32x2*)(orow + (nt * 32 + 8 * q) * 2) = (u32x2){pk2(n[4 * q], n[4 * q + 1]), pk2(n[4 * q + 2], n[4 * q + 3])};
  }
  STAMP(2, 29);
}

extern "C" void kernel_launch(void* const* d_in, const int* in_sizes, int n_in, void* d_out, int out_size, void* d_ws, size_t ws_size,
                              hipStream_t stream) {
  const char* x = (const char*)d_in[0];
  const char* w1a = (const char*)d_in[1];
  const char* w1b = (const char*)d_in[2];
  const char* wqkv = (const char*)d_in[3];
  const char* wo = (const char*)d_in[4];
  const char* pw1 = (const char*)d_in[5];
  const char* dw = (const char*)d_in[6];
  const char* pw2 = (const char*)d_in[7];
  const char* w2a = (const char*)d_in[8];
  const char* w2b = (const char*)d_in[9];
  char* ws = (char*)d_ws;
  (void)hipFuncSetAttribute((const void*)k1_kernel, hipFuncAttributeMaxDynamicSharedMemorySize, L_TOTAL);
  (void)hipFuncSetAttribute((const void*)attn_kernel, hipFuncAttributeMaxDynamicSharedMemorySize, A_LDS);
  (void)hipFuncSetAttribute((const void*)k3_kernel, hipFuncAttributeMaxDynamicSharedMemorySize, L_TOTAL);
  k1_kernel<<<NTB, 320, L_TOTAL, stream>>>(x, w1a, w1b, wqkv, ws);
  attn_kernel<<<256, 256 * A_KG, A_LDS, stream>>>(ws, wo, pw1, pw2, w2a, w2b);
  k3_kernel<<<NTB, 256, L_TOTAL, stream>>>(wo, pw1, dw, pw2, w2a, w2b, ws, (char*)d_out);
}
```

```cpp
#include <hip/hip_runtime.h>
#include <hip/hip_bf16.h>
#include <stdint.h>

typedef short bf16x8 __attribute__((ext_vector_type(8)));
typedef short s16x4 __attribute__((ext_vector_type(4)));
typedef float f32x16 __attribute__((ext_vector_type(16)));
typedef float f32x2 __attribute__((ext_vector_type(2)));
typedef __bf16 bf16x2_t __attribute__((ext_vector_type(2)));
typedef unsigned u32x4 __attribute__((ext_vector_type(4)));
typedef unsigned u32x2 __attribute__((ext_vector_type(2)));
#define LAS __attribute__((address_space(3)))
#define DI __device__ __forceinline__
#define MFMA(a, b, c) __builtin_amdgcn_mfma_f32_32x32x16_bf16((a), (b), (c), 0, 0, 0)
#define MFMA16(a, b, c) __builtin_amdgcn_mfma_f32_16x16x32_bf16((a), (b), (c), 0, 0, 0)
#define SB() __builtin_amdgcn_sched_barrier(0)
#define STAMP(k, i) do {} while (0)
typedef float f32x4 __attribute__((ext_vector_type(4)));

constexpr int TSEQ = 4000, NBLK = 125, NTB = 250;
constexpr size_t WS_X1F = 0;
constexpr size_t WS_QF = 4u << 20;
constexpr size_t WS_KVF = 8u << 20;
constexpr size_t WS_ATTF = 17u << 20;
constexpr float LN_EPS = 1e-5f;
constexpr float LOG2E = 1.4426950408889634f;
constexpr float QSCALE = 0.125f * LOG2E;

DI unsigned pk2(float a, float b) { f32x2 v = {a, b}; return __builtin_bit_cast(unsigned, __builtin_convertvector(v, bf16x2_t)); }
DI float bflo(unsigned u) { return __uint_as_float(u << 16); }
DI float bfhi(unsigned u) { return __uint_as_float(u & 0xffff0000u); }
DI float rbf(float a) { return bflo(pk2(a, a)); }
DI bf16x8 pack8(const f32x16& v, int s) {
  u32x4 p = {pk2(v[8 * s], v[8 * s + 1]), pk2(v[8 * s + 2], v[8 * s + 3]), pk2(v[8 * s + 4], v[8 * s + 5]), pk2(v[8 * s + 6], v[8 * s + 7])};
  return __builtin_bit_cast(bf16x8, p);
}
DI void unpack8(u32x4 p, f32x16& v, int s) {
#pragma unroll
  for (int i = 0; i < 4; ++i) { v[8 * s + 2 * i] = bflo(p[i]); v[8 * s + 2 * i + 1] = bfhi(p[i]); }
}
DI float halfsum(float x) {
  auto r = __builtin_amdgcn_permlane32_swap(__float_as_uint(x), __float_as_uint(x), false, false);
  return __uint_as_float(r[0]) + __uint_as_float(r[1]);
}
DI float halfmax(float x) {
  auto r = __builtin_amdgcn_permlane32_swap(__float_as_uint(x), __float_as_uint(x), false, false);
  return fmaxf(__uint_as_float(r[0]), __uint_as_float(r[1]));
}
DI float sigmoidf_(float x) { return __builtin_amdgcn_rcpf(1.0f + __builtin_amdgcn_exp2f(-LOG2E * x)); }
DI float siluf_(float x) { return x * sigmoidf_(x); }
DI void lds_barrier() { asm volatile("s_waitcnt lgkmcnt(0)\n\ts_barrier" ::: "memory"); }
typedef short v4i16_t __attribute__((ext_vector_type(4)));
DI s16x4 trread(LAS const char* p) { return __builtin_bit_cast(s16x4, __builtin_amdgcn_ds_read_tr16_b64_v4i16((LAS v4i16_t*)p)); }
DI bf16x8 ldsfrag(LAS const char* p) { return *(LAS const bf16x8*)p; }

constexpr size_t WS_WT1 = 24u << 20, WS_WT3 = 26u << 20;
constexpr int NP1 = 11, NP3 = 12;
struct WPtrs { const char *w1a, *w1b, *wqkv, *wo, *pw1, *pw2, *w2a, *w2b; };
DI void ff_panel(const char* wa, const char* wb, int p, int w, const char*& base, unsigned& ld) {
  const bool isb = (p == 2 || p == 4 || p >= 6);
  const int c = p < 2 ? p : (p == 3 ? 2 : (p == 5 ? 3 : (p == 2 ? 0 : (p == 4 ? 1 : p - 4))));
  if (isb) { base = wb + (size_t)c * 256 * 512 + w * 128; ld = 512; } else { base = wa + (size_t)(w + 4 * c) * 128; ld = 2048; }
}
__global__ __launch_bounds__(256) void prep_kernel(WPtrs wp, char* __restrict__ ws) {
  __shared__ __attribute__((aligned(16))) char sm[4 * 4096];
  const int lane = threadIdx.x & 63, wv = __builtin_amdgcn_readfirstlane(threadIdx.x >> 6);
  const int g = blockIdx.x * 4 + wv;
  const bool k3 = g >= 4 * NP1 * 8;
  const int gg = k3 ? g - 4 * NP1 * 8 : g, np = k3 ? NP3 : NP1;
  const int w = gg / (np * 8), p = (gg >> 3) % np, sl = gg & 7;
  const char* base; unsigned ld;
  if (!k3) {
    if (p < 8) ff_panel(wp.w1a, wp.w1b, p, w, base, ld);
    else { base = wp.wqkv + (p - 8) * 512 + w * 128; ld = 1536; }
  } else {
    if (p == 0) { base = wp.wo + w * 128; ld = 512; }
    else if (p < 3) { base = wp.pw1 + (p - 1) * 512 + w * 128; ld = 1024; }
    else if (p == 3) { base = wp.pw2 + w * 128; ld = 512; }
    else ff_panel(wp.w2a, wp.w2b, p - 4, w, base, ld);
  }
  LAS char* my = (LAS char*)sm + wv * 4096;
  const char* src = base + (size_t)(sl * 32 + (lane >> 3)) * ld + (((lane & 7) ^ (((lane >> 4) & 1) << 2)) << 4);
  u32x4 t[4];
#pragma unroll
  for (int i = 0; i < 4; ++i) t[i] = *(const u32x4*)(src + (size_t)i * 8 * ld);
#pragma unroll
  for (int i = 0; i < 4; ++i) *(LAS u32x4*)(my + i * 1024 + lane * 16) = t[i];
  asm volatile("s_waitcnt lgkmcnt(0)" ::: "memory");
  const int h = lane >> 5, cb = (lane >> 4) & 1, q = (lane & 15) >> 2, pp = lane & 3;
  char* dst = ws + (k3 ? WS_WT3 : WS_WT1) + (size_t)gg * 4096 + lane * 16;
#pragma unroll
  for (int i = 0; i < 4; ++i) {
    LAS const char* rp = my + (4 * h + q) * 128 + (((i & 1) ^ (q >> 1)) * 64) + cb * 32 + pp * 8 + (i >> 1) * 2048;
    s16x4 l = trread(rp), hh = trread(rp + 1024);
    *(bf16x8*)(dst + i * 1024) = __builtin_shufflevector(l, hh, 0, 1, 2, 3, 4, 5, 6, 7);
  }
}

struct Stream {
  const char* p;
  bf16x8 wf[8][4];
  DI void start(const char* base) {
    p = base;
#pragma unroll
    for (int sl = 0; sl < 8; ++sl)
#pragma unroll
      for (int i = 0; i < 4; ++i) wf[sl][i] = *(const bf16x8*)(p + sl * 4096 + i * 1024);
    p += 32768;
  }
};
struct NoWork { DI void init() {} DI void step(int, int) {} };
struct SiluWork {
  LAS char* p;
  u32x2 raw; float x[4], e[4];
  DI void init() { raw = *(LAS u32x2*)p; }
  DI void step(int sl, int i) {
    if (i == 0) {
      x[0] = bflo(raw[0]); x[1] = bfhi(raw[0]); x[2] = bflo(raw[1]); x[3] = bfhi(raw[1]);
#pragma unroll
      for (int j = 0; j < 4; ++j) e[j] = -LOG2E * x[j];
      asm volatile("" : "+v"(e[0]), "+v"(e[1]), "+v"(e[2]), "+v"(e[3]), "+v"(x[0]), "+v"(x[1]), "+v"(x[2]), "+v"(x[3]));
      if (sl < 7) raw = *(LAS u32x2*)(p + ((sl + 1) >> 1) * 1024 + ((sl + 1) & 1) * 8);
    } else if (i == 1) {
#pragma unroll
      for (int j = 0; j < 4; ++j) e[j] = __builtin_amdgcn_exp2f(e[j]);
      asm volatile("" : "+v"(e[0]), "+v"(e[1]), "+v"(e[2]), "+v"(e[3]));
    } else if (i == 2) {
#pragma unroll
      for (int j = 0; j < 4; ++j) e[j] = __builtin_amdgcn_rcpf(1.0f + e[j]);
      asm volatile("" : "+v"(e[0]), "+v"(e[1]), "+v"(e[2]), "+v"(e[3]));
    } else {
      *(LAS u32x2*)(p + (sl >> 1) * 1024 + (sl & 1) * 8) = (u32x2){pk2(x[0] * e[0], x[1] * e[1]), pk2(x[2] * e[2], x[3] * e[3])};
    }
  }
};
template <int NB, bool SWAP, bool LAST, class W>
DI void gemm_k256(Stream& st, f32x16 (&acc)[NB][2], LAS const char* bl, W work) {
  bf16x8 bq[2][NB][2];
#pragma unroll
  for (int nb = 0; nb < NB; ++nb) { bq[0][nb][0] = ldsfrag(bl + nb * 16384); bq[0][nb][1] = ldsfrag(bl + nb * 16384 + 1024); }
  work.init();
#pragma unroll
  for (int sl = 0; sl < 8; ++sl) {
    SB();
#pragma unroll
    for (int i = 0; i < 4; ++i) {
      const int ks = i >> 1, nt = i & 1;
#pragma unroll
      for (int nb = 0; nb < NB; ++nb)
        acc[nb][nt] = SWAP ? MFMA(bq[sl & 1][nb][ks], st.wf[sl][i], acc[nb][nt]) : MFMA(st.wf[sl][i], bq[sl & 1][nb][ks], acc[nb][nt]);
      if (!(i & 1) && sl < 7) {
#pragma unroll
        for (int nb = 0; nb < NB; ++nb) bq[(sl + 1) & 1][nb][ks] = ldsfrag(bl + nb * 16384 + (sl + 1) * 2048 + ks * 1024);
      }
      if (!LAST && sl > 0) st.wf[sl - 1][i] = *(const bf16x8*)(st.p + (sl - 1) * 4096 + i * 1024);
      work.step(sl, i);
      SB();
    }
  }
  if (!LAST) {
#pragma unroll
    for (int i = 0; i < 4; ++i) st.wf[7][i] = *(const bf16x8*)(st.p + 7 * 4096 + i * 1024);
    st.p += 32768;
  }
}

constexpr int L_X = 0, L_Y = L_X + 16384, L_Z = L_Y + 16384;
constexpr int L_STAT = L_Z + 16384;
constexpr int GROW = 136, GSZ = 36 * GROW;
constexpr int L_DW = L_STAT + 2048;
constexpr int L_TOTAL = L_DW + 2560;

template <int NB>
DI void ln_stats(const f32x16 (&v)[NB][2], LAS char* lds, int w, int lane, float (&mean)[NB], float (&rstd)[NB]) {
  const int r = lane & 31;
#pragma unroll
  for (int nb = 0; nb < NB; ++nb) {
    const f32x16 t = v[nb][0] + v[nb][1];
    const float s = ((t[0] + t[1]) + (t[2] + t[3])) + ((t[4] + t[5]) + (t[6] + t[7])) + (((t[8] + t[9]) + (t[10] + t[11])) + ((t[12] + t[13]) + (t[14] + t[15])));
    const float mw = halfsum(s) * (1.0f / 64.0f);
    const f32x16 d0 = v[nb][0] - mw, d1 = v[nb][1] - mw;
    const f32x16 q = d0 * d0 + d1 * d1;
    const float m2 = halfsum(((q[0] + q[1]) + (q[2] + q[3])) + ((q[4] + q[5]) + (q[6] + q[7])) + (((q[8] + q[9]) + (q[10] + q[11])) + ((q[12] + q[13]) + (q[14] + q[15]))));
    if (lane < 32) *(LAS f32x2*)(lds + L_STAT + ((nb * 4 + w) * 32 + r) * 8) = (f32x2){mw, m2};
  }
  lds_barrier();
#pragma unroll
  for (int nb = 0; nb < NB; ++nb) {
    f32x2 s0 = *(LAS f32x2*)(lds + L_STAT + ((nb * 4 + 0) * 32 + r) * 8), s1 = *(LAS f32x2*)(lds + L_STAT + ((nb * 4 + 1) * 32 + r) * 8);
    f32x2 s2 = *(LAS f32x2*)(lds + L_STAT + ((nb * 4 + 2) * 32 + r) * 8), s3 = *(LAS f32x2*)(lds + L_STAT + ((nb * 4 + 3) * 32 + r) * 8);
    const float m = 0.25f * (s0[0] + s1[0] + s2[0] + s3[0]);
    const float e0 = s0[0] - m, e1 = s1[0] - m, e2 = s2[0] - m, e3 = s3[0] - m;
    const float M2 = s0[1] + s1[1] + s2[1] + s3[1] + 64.0f * (e0 * e0 + e1 * e1 + e2 * e2 + e3 * e3);
    mean[nb] = m; rstd[nb] = __builtin_amdgcn_rsqf(M2 * (1.0f / 256.0f) + LN_EPS);
  }
}
template <int NB>
DI void ln_to_frags(const f32x16 (&v)[NB][2], LAS char* lds, int w, int lane) {
  float mean[NB], rstd[NB];
  ln_stats<NB>(v, lds, w, lane, mean, rstd);
  LAS char* fb = lds + L_X + w * 4096 + lane * 16;
#pragma unroll
  for (int nb = 0; nb < NB; ++nb)
#pragma unroll
    for (int nt = 0; nt < 2; ++nt) {
      const f32x16 n = (v[nb][nt] - mean[nb]) * rstd[nb];
#pragma unroll
      for (int s = 0; s < 2; ++s) *(LAS bf16x8*)(fb + nb * 16384 + (2 * nt + s) * 1024) = pack8(n, s);
    }
  lds_barrier();
}

template <bool LAST>
DI void ff_block(Stream& st, LAS char* lds, int w, int lane, f32x16 (&out)[1][2]) {
  out[0][0] = f32x16{}; out[0][1] = f32x16{};
  LAS char* xb = lds + L_X + lane * 16;
  LAS char* yb = lds + L_Y + lane * 16;
#pragma unroll 1
  for (int c = 0; c < 4; ++c) {
    f32x16 hn[1][2]; hn[0][0] = f32x16{}; hn[0][1] = f32x16{};
    if (c == 0) gemm_k256<1, false, false>(st, hn, xb, NoWork{});
    else gemm_k256<1, false, false>(st, hn, xb, SiluWork{yb + ((c - 1) & 1) * 16384 + w * 4096});
    STAMP(LAST ? 2 : 0, 10 + 3 * c);
    if (c > 0) lds_barrier();
#pragma unroll
    for (int f = 0; f < 4; ++f) *(LAS bf16x8*)(yb + (c & 1) * 16384 + w * 4096 + f * 1024) = pack8(hn[0][f >> 1], f & 1);
    if (c > 0) {
      if (c < 3) gemm_k256<1, false, false>(st, out, yb + ((c - 1) & 1) * 16384, NoWork{});
      else gemm_k256<1, false, false>(st, out, yb, SiluWork{yb + 16384 + w * 4096});
      STAMP(LAST ? 2 : 0, 11 + 3 * c);
    }
  }
  lds_barrier();
  gemm_k256<1, false, LAST>(st, out, yb + 16384, NoWork{});
}

DI void l2_warm(const char* p, int kb_total, int idx, int lane, u32x4& t) {
  const int per = (kb_total + 123) / 124;
  int b = idx * per, e = b + per; if (e > kb_total) e = kb_total;
  for (int i = b; i < e; ++i)
    asm volatile("global_load_dwordx4 %0, %1, %2" : "+v"(t) : "v"(lane * 16 + i * 1024), "s"(p));
}

__global__ __launch_bounds__(256, 1) void k1_kernel(const char* __restrict__ x, char* __restrict__ ws) {
  extern __shared__ __attribute__((aligned(16))) char smem[];
  LAS char* lds = (LAS char*)smem;
  const int lane = threadIdx.x & 63, w = __builtin_amdgcn_readfirstlane(threadIdx.x >> 6), r = lane & 31, h = lane >> 5;
  const int tb = blockIdx.x;
  STAMP(0, 0);
  f32x16 xv[1][2];
  Stream st;
  u32x4 wt = {0u, 0u, 0u, 0u};
  {
    const char* xrow = x + (size_t)(tb * 32 + r) * 512 + (w * 64 + 4 * h) * 2;
    u32x2 u[8];
#pragma unroll
    for (int g = 0; g < 8; ++g) u[g] = *(const u32x2*)(xrow + g * 16);
    SB();
    st.start(ws + WS_WT1 + (size_t)w * NP1 * 32768 + lane * 16);
    SB();
    if ((tb >> 3) < 31) l2_warm(ws + WS_WT1, 4 * NP1 * 32, (tb >> 3) * 4 + w, lane, wt);
#pragma unroll
    for (int g = 0; g < 8; ++g) {
      xv[0][g >> 2][4 * (g & 3)] = bflo(u[g][0]); xv[0][g >> 2][4 * (g & 3) + 1] = bfhi(u[g][0]);
      xv[0][g >> 2][4 * (g & 3) + 2] = bflo(u[g][1]); xv[0][g >> 2][4 * (g & 3) + 3] = bfhi(u[g][1]);
    }
  }
  STAMP(0, 1);
  ln_to_frags<1>(xv, lds, w, lane);
  STAMP(0, 2);
  f32x16 acc[1][2];
  ff_block<false>(st, lds, w, lane, acc);
  asm volatile("" :: "v"(wt));
  STAMP(0, 3);
#pragma unroll
  for (int nt = 0; nt < 2; ++nt) {
#pragma unroll
    for (int i = 0; i < 16; ++i) xv[0][nt][i] = rbf(xv[0][nt][i] + 0.5f * acc[0][nt][i]);
#pragma unroll
    for (int s = 0; s < 2; ++s) *(bf16x8*)(ws + WS_X1F + ((size_t)tb * 16 + 4 * w + 2 * nt + s) * 1024 + lane * 16) = pack8(xv[0][nt], s);
  }
  ln_to_frags<1>(xv, lds, w, lane);
  STAMP(0, 4);
  const int b = tb / NBLK, blk = tb - b * NBLK, bh = b * 4 + w;
  char* kvdst = ws + WS_KVF + (size_t)(bh * NBLK + blk) * 8192 + lane * 16;
#pragma unroll 1
  for (int c = 0; c < 2; ++c) {
    f32x16 a[1][2]; a[0][0] = f32x16{}; a[0][1] = f32x16{};
    gemm_k256<1, false, false>(st, a, lds + L_X + lane * 16, NoWork{});
    const float sc = c == 0 ? QSCALE : 1.0f;
    char* dst = c == 0 ? ws + WS_QF + (size_t)(bh * NBLK + blk) * 4096 + lane * 16 : kvdst;
#pragma unroll
    for (int nt = 0; nt < 2; ++nt) {
      a[0][nt] = a[0][nt] * sc;
#pragma unroll
      for (int s = 0; s < 2; ++s) *(bf16x8*)(dst + (2 * nt + s) * 1024) = pack8(a[0][nt], s);
    }
  }
  STAMP(0, 5);
  {
    f32x16 a[1][2]; a[0][0] = f32x16{}; a[0][1] = f32x16{};
    gemm_k256<1, true, true>(st, a, lds + L_X + lane * 16, NoWork{});
#pragma unroll
    for (int nt = 0; nt < 2; ++nt)
#pragma unroll
      for (int s = 0; s < 2; ++s) *(bf16x8*)(kvdst + (4 + 2 * nt + s) * 1024) = pack8(a[0][nt], s);
  }
  STAMP(0, 29);
}

constexpr int A_KG = 2, A_SBLK = A_KG * 2, A_HST = A_SBLK * 4096, A_NST = (NBLK + A_SBLK - 1) / A_SBLK, A_NFULL = NBLK / A_SBLK;
constexpr int A_VOFF = 3 * A_HST, A_LDS = 6 * A_HST;
__global__ __launch_bounds__(256 * A_KG, A_KG) void attn_kernel(char* __restrict__ ws) {
  extern __shared__ __attribute__((aligned(16))) char smem[];
  LAS char* lds = (LAS char*)smem;
  const int lane = threadIdx.x & 63, w = __builtin_amdgcn_readfirstlane(threadIdx.x >> 6);
  const int bh = blockIdx.x & 7, qg = blockIdx.x >> 3, qi = w & 3, kg = w >> 2;
  const int qb_raw = qg * 4 + qi, qb = qb_raw < NBLK ? qb_raw : NBLK - 1;
  STAMP(1, 0);
  const char* kv = ws + WS_KVF + (size_t)bh * NBLK * 8192;
  const unsigned lds0 = (unsigned)(uintptr_t)lds;
  const unsigned voff = (w >> 1) * 8192 + (w & 1) * 2048 + lane * 16;
  auto dma2 = [&](int st, int isv, unsigned ldsoff) {
    asm volatile("s_mov_b32 m0, %2\n\ts_nop 0\n\tglobal_load_lds_dwordx4 %0, %1\n\tglobal_load_lds_dwordx4 %0, %1 offset:1024"
                 :: "v"(voff), "s"(kv + (size_t)st * (2 * A_HST) + isv * 4096), "s"(lds0 + ldsoff + w * 2048));
  };
  dma2(0, 0, 0); dma2(1, 0, A_HST); dma2(0, 1, A_VOFF); dma2(2, 0, 2 * A_HST); dma2(1, 1, A_VOFF + A_HST);
  bf16x8 qf[4];
#pragma unroll
  for (int f = 0; f < 4; ++f) qf[f] = *(const bf16x8*)(ws + WS_QF + ((size_t)(bh * NBLK + qb) * 4 + f) * 1024 + lane * 16);
#pragma unroll
  for (int f = 0; f < 4; ++f) asm volatile("" : "+v"(qf[f]));
  u32x4 wt = {0u, 0u, 0u, 0u};
  {
    const int i0 = (qg * 8 + w) * 6;
#pragma unroll
    for (int i = 0; i < 6; ++i) {
      const char* p = ws + WS_WT3 + (i0 + i) * 1024;
      asm volatile("global_load_dwordx4 %0, %1, %2" : "+v"(wt) : "v"(lane * 16), "s"(p));
    }
  }
  const int rem = NBLK - A_NFULL * A_SBLK - kg * 2;
  const int ntail = rem < 0 ? 0 : (rem > 2 ? 2 : rem);
  LAS const char* base = lds + kg * 8192 + lane * 16;
  f32x16 o0 = f32x16{}, o1 = f32x16{}, negr, sa, sb;
  f32x4 lacc = {0.f, 0.f, 0.f, 0.f};
  bf16x8 sel;
  { const short one = (((lane >> 4) ^ lane) & 1) ? (short)0 : (short)0x3f80;
#pragma unroll
    for (int j = 0; j < 8; ++j) sel[j] = one; }
  bf16x8 kf[4], vf[4], kg2[4], vg2[4]; unsigned pa[8], pb[8];
#pragma unroll
  for (int i = 0; i < 8; ++i) pa[i] = 0u;
  float ref;
  asm volatile("s_waitcnt vmcnt(0)\n\ts_barrier" : "+v"(wt) :: "memory");
  {
#pragma unroll
    for (int f = 0; f < 4; ++f) kf[f] = ldsfrag(base + f * 1024);
    f32x16 z = f32x16{};
    sa = MFMA(kf[0], qf[0], z); sa = MFMA(kf[1], qf[1], sa); sa = MFMA(kf[2], qf[2], sa); sa = MFMA(kf[3], qf[3], sa);
    float mx = sa[0];
#pragma unroll
    for (int i = 1; i < 16; ++i) mx = fmaxf(mx, sa[i]);
    ref = halfmax(mx) + 7.0f;
#pragma unroll
    for (int i = 0; i < 16; ++i) { sa[i] -= ref; negr[i] = -ref; }
#pragma unroll
    for (int f = 0; f < 4; ++f) { kf[f] = ldsfrag(base + 4096 + f * 1024); vf[f] = bf16x8{}; }
  }
#define SLOT3(MF, RF, i) do { MF; RF; e[2*(i)] = __builtin_amdgcn_exp2f(SC_[2*(i)]); e[2*(i)+1] = __builtin_amdgcn_exp2f(SC_[2*(i)+1]); \
    if ((i) > 0) PN_[(i)-1] = pk2(e[2*(i)-2], e[2*(i)-1]); SB(); } while (0)
  auto iter = [&](f32x16& SC_, f32x16& SN_, unsigned (&PP_)[8], unsigned (&PN_)[8], bf16x8 (&KC)[4], bf16x8 (&VC)[4], bf16x8 (&KN)[4], bf16x8 (&VN)[4], LAS const char* kn, LAS const char* vc) {
    float e[16];
    const bf16x8 p0 = __builtin_bit_cast(bf16x8, (u32x4){PP_[0], PP_[1], PP_[2], PP_[3]}), p1 = __builtin_bit_cast(bf16x8, (u32x4){PP_[4], PP_[5], PP_[6], PP_[7]});
    SB();
    SLOT3(SN_ = MFMA(KC[0], qf[0], negr), KN[0] = ldsfrag(kn), 0);
    SLOT3(o0 = MFMA(VC[0], p0, o0), VN[0] = ldsfrag(vc), 1);
    SLOT3(SN_ = MFMA(KC[1], qf[1], SN_), KN[1] = ldsfrag(kn + 1024), 2);
    SLOT3(o1 = MFMA(VC[2], p0, o1); lacc = MFMA16(sel, p0, lacc), VN[2] = ldsfrag(vc + 2048), 3);
    SLOT3(SN_ = MFMA(KC[2], qf[2], SN_), KN[2] = ldsfrag(kn + 2048), 4);
    SLOT3(o0 = MFMA(VC[1], p1, o0), VN[1] = ldsfrag(vc + 1024), 5);
    SLOT3(SN_ = MFMA(KC[3], qf[3], SN_), KN[3] = ldsfrag(kn + 3072), 6);
    SLOT3(o1 = MFMA(VC[3], p1, o1); lacc = MFMA16(sel, p1, lacc), VN[3] = ldsfrag(vc + 3072), 7);
    PN_[7] = pk2(e[14], e[15]);
    const unsigned orall = (PN_[0] | PN_[1] | PN_[2]) | (PN_[3] | PN_[4] | PN_[5]) | (PN_[6] | PN_[7]);
    if (__builtin_expect(__any((orall & 0x40004000u) != 0u), 0)) {
      float mx = SC_[0];
#pragma unroll
      for (int i = 1; i < 16; ++i) mx = fmaxf(mx, SC_[i]);
      mx = halfmax(mx);
      const float delta = (mx >= 0.5f) ? mx + 7.0f : 0.0f;
      const float alpha = __builtin_amdgcn_exp2f(-delta);
      const float alo = __shfl_xor(alpha, 16);
      const float aA = (lane & 16) ? alo : alpha, aB = (lane & 16) ? alpha : alo;
      lacc = lacc * (f32x4){aA, aB, aA, aB};
      o0 = o0 * alpha; o1 = o1 * alpha; negr = negr - delta; SN_ = SN_ - delta;
      asm volatile("" : "+v"(o0), "+v"(o1), "+v"(negr), "+v"(SN_), "+v"(lacc));
      ref = -negr[0];
#pragma unroll
      for (int i = 0; i < 8; ++i) PN_[i] = pk2(__builtin_amdgcn_exp2f(SC_[2*i] - delta), __builtin_amdgcn_exp2f(SC_[2*i+1] - delta));
    }
  };
  STAMP(1, 1);
  int r0 = 0, r1 = A_HST, r2 = 2 * A_HST;
  for (int st = 0; st < A_NFULL; ++st) {
    asm volatile("s_waitcnt vmcnt(4) lgkmcnt(0)\n\ts_barrier" ::: "memory");
    dma2(st + 3 < A_NST ? st + 3 : A_NST - 1, 0, r0); dma2(st + 2 < A_NST ? st + 2 : A_NST - 1, 1, A_VOFF + r2);
    iter(sa, sb, pa, pb, kf, vf, kg2, vg2, base + r1, base + A_VOFF + r0);
    iter(sb, sa, pb, pa, kg2, vg2, kf, vf, base + r1 + 4096, base + A_VOFF + r0 + 4096);
    const int t = r0; r0 = r1; r1 = r2; r2 = t;
  }
  asm volatile("s_waitcnt vmcnt(0) lgkmcnt(0)\n\ts_barrier" ::: "memory");
  if (ntail > 0) {
    iter(sa, sb, pa, pb, kf, vf, kg2, vg2, base + r1, base + A_VOFF + r0);
    if (ntail > 1) iter(sb, sa, pb, pa, kg2, vg2, kf, vf, base + r1 + 4096, base + A_VOFF + r0 + 4096);
    else {
#pragma unroll
      for (int i = 0; i < 8; ++i) pa[i] = pb[i];
#pragma unroll
      for (int i = 0; i < 4; ++i) vf[i] = vg2[i];
    }
  }
  {
    const bf16x8 p0 = __builtin_bit_cast(bf16x8, (u32x4){pa[0], pa[1], pa[2], pa[3]}), p1 = __builtin_bit_cast(bf16x8, (u32x4){pa[4], pa[5], pa[6], pa[7]});
    o0 = MFMA(vf[0], p0, o0); o1 = MFMA(vf[2], p0, o1); o0 = MFMA(vf[1], p1, o0); o1 = MFMA(vf[3], p1, o1);
    lacc = MFMA16(sel, p0, lacc); lacc = MFMA16(sel, p1, lacc);
  }
  float lsum = (lane & 16) ? lacc[1] : lacc[0];
  STAMP(1, 2);
  lds_barrier();
  if (kg > 0) {
    LAS char* cb = lds + ((kg - 1) * 4 + qi) * 9216 + lane * 16;
#pragma unroll
    for (int g = 0; g < 4; ++g) {
      *(LAS f32x4*)(cb + g * 1024) = (f32x4){o0[4 * g], o0[4 * g + 1], o0[4 * g + 2], o0[4 * g + 3]};
      *(LAS f32x4*)(cb + 4096 + g * 1024) = (f32x4){o1[4 * g], o1[4 * g + 1], o1[4 * g + 2], o1[4 * g + 3]};
    }
    *(LAS f32x2*)(lds + ((kg - 1) * 4 + qi) * 9216 + 8192 + lane * 8) = (f32x2){ref, lsum};
  }
  lds_barrier();
  if (kg == 0 && qb_raw < NBLK) {
#pragma unroll
    for (int k = 1; k < A_KG; ++k) {
      LAS char* cb = lds + ((k - 1) * 4 + qi) * 9216 + lane * 16;
      const f32x2 rl = *(LAS f32x2*)(lds + ((k - 1) * 4 + qi) * 9216 + 8192 + lane * 8);
      const float m = fmaxf(ref, rl[0]);
      const float c0 = __builtin_amdgcn_exp2f(ref - m), c1 = __builtin_amdgcn_exp2f(rl[0] - m);
      lsum = lsum * c0 + rl[1] * c1; ref = m;
#pragma unroll
      for (int g = 0; g < 4; ++g) {
        const f32x4 u0 = *(LAS f32x4*)(cb + g * 1024), u1 = *(LAS f32x4*)(cb + 4096 + g * 1024);
#pragma unroll
        for (int j = 0; j < 4; ++j) { o0[4 * g + j] = o0[4 * g + j] * c0 + u0[j] * c1; o1[4 * g + j] = o1[4 * g + j] * c0 + u1[j] * c1; }
      }
    }
    const float inv = 1.0f / lsum;
#pragma unroll
    for (int i = 0; i < 16; ++i) { o0[i] *= inv; o1[i] *= inv; }
    char* dst = ws + WS_ATTF + ((size_t)((bh >> 2) * NBLK + qb) * 16 + (bh & 3) * 4) * 1024 + lane * 16;
    *(bf16x8*)(dst) = pack8(o0, 0); *(bf16x8*)(dst + 1024) = pack8(o0, 1);
    *(bf16x8*)(dst + 2048) = pack8(o1, 0); *(bf16x8*)(dst + 3072) = pack8(o1, 1);
  }
  STAMP(1, 29);
}

__global__ __launch_bounds__(256, 1) void k3_kernel(const char* __restrict__ dw, const char* __restrict__ ws, char* __restrict__ out) {
  extern __shared__ __attribute__((aligned(16))) char smem[];
  LAS char* lds = (LAS char*)smem;
  const int lane = threadIdx.x & 63, w = __builtin_amdgcn_readfirstlane(threadIdx.x >> 6), r = lane & 31, h = lane >> 5;
  const int tb = blockIdx.x, b = tb / NBLK, t0 = (tb - b * NBLK) * 32;
  STAMP(2, 0);
  Stream st;
  const int hc = r & 3, ht = (hc < 2) ? t0 - 2 + hc : t0 + 30 + hc;
  const bool hvalid = ht >= 0 && ht < TSEQ;
  const int hg = b * TSEQ + (ht < 0 ? 0 : (ht >= TSEQ ? TSEQ - 1 : ht));
  const unsigned hoff = (unsigned)(hg >> 5) * 16384 + ((hg & 31) + 32 * h) * 16 + w * 4096;
  const unsigned moff = (unsigned)tb * 16384 + lane * 16 + w * 4096;
  {
    const unsigned ly = (unsigned)(uintptr_t)(lds + L_Y + w * 4096);
    asm volatile("s_mov_b32 m0, %3\n\ts_nop 0\n\tglobal_load_lds_dwordx4 %0, %2\n\tglobal_load_lds_dwordx4 %0, %2 offset:1024\n\t"
                 "global_load_lds_dwordx4 %0, %2 offset:2048\n\tglobal_load_lds_dwordx4 %0, %2 offset:3072\n\t"
                 "s_mov_b32 m0, %4\n\ts_nop 0\n\tglobal_load_lds_dwordx4 %1, %2\n\tglobal_load_lds_dwordx4 %1, %2 offset:1024\n\t"
                 "global_load_lds_dwordx4 %1, %2 offset:2048\n\tglobal_load_lds_dwordx4 %1, %2 offset:3072"
                 :: "v"(moff), "v"(hoff), "s"(ws + WS_ATTF), "s"(ly), "s"(ly + 16384));
  }
  u32x4 xr[2][4];
#pragma unroll
  for (int f = 0; f < 4; ++f) {
    xr[0][f] = *(const u32x4*)(ws + WS_X1F + moff + f * 1024);
    xr[1][f] = *(const u32x4*)(ws + WS_X1F + hoff + f * 1024);
  }
  const u32x4 dwv = *(const u32x4*)(dw + (threadIdx.x < 160 ? threadIdx.x : 159) * 16);
  SB();
  st.start(ws + WS_WT3 + (size_t)w * NP3 * 32768 + lane * 16);
  SB();
  if (threadIdx.x < 160) *(LAS u32x4*)(lds + L_DW + threadIdx.x * 16) = dwv;
  asm volatile("s_waitcnt vmcnt(32)" ::: "memory");
  lds_barrier();
  STAMP(2, 1);
  f32x16 xv[2][2];
  {
#pragma unroll
    for (int nb = 0; nb < 2; ++nb)
#pragma unroll
      for (int nt = 0; nt < 2; ++nt) { unpack8(xr[nb][2 * nt], xv[nb][nt], 0); unpack8(xr[nb][2 * nt + 1], xv[nb][nt], 1); }
    gemm_k256<2, false, false>(st, xv, lds + L_Y + lane * 16, NoWork{});
  }
  STAMP(2, 2);
  ln_to_frags<2>(xv, lds, w, lane);
  STAMP(2, 3);
  {
    f32x16 a[2][2], g[2][2];
#pragma unroll
    for (int nb = 0; nb < 2; ++nb) { a[nb][0] = f32x16{}; a[nb][1] = f32x16{}; g[nb][0] = f32x16{}; g[nb][1] = f32x16{}; }
    gemm_k256<2, false, false>(st, a, lds + L_X + lane * 16, NoWork{});
    gemm_k256<2, false, false>(st, g, lds + L_X + lane * 16, NoWork{});
    STAMP(2, 4);
    LAS char* G = lds + L_Y + w * GSZ;
    lds_barrier();
#pragma unroll
    for (int nb = 0; nb < 2; ++nb) {
      const int row = nb == 0 ? r + 2 : (hc < 2 ? hc : 32 + hc);
      const bool wr = nb == 0 || r < 4;
      const float keep = (nb == 0 || hvalid) ? 1.0f : 0.0f;
#pragma unroll
      for (int nt = 0; nt < 2; ++nt)
#pragma unroll
        for (int q = 0; q < 4; ++q) {
          float e[4];
#pragma unroll
          for (int j = 0; j < 4; ++j) e[j] = keep * a[nb][nt][4 * q + j] * sigmoidf_(g[nb][nt][4 * q + j]);
          if (wr) *(LAS u32x2*)(G + row * GROW + (nt * 32 + 8 * q + 4 * h) * 2) = (u32x2){pk2(e[0], e[1]), pk2(e[2], e[3])};
        }
    }
    asm volatile("s_waitcnt lgkmcnt(0)" ::: "memory");
#pragma unroll
    for (int nt = 0; nt < 2; ++nt) {
      f32x16 c;
#pragma unroll
      for (int q = 0; q < 4; ++q) {
        float e[4] = {0.f, 0.f, 0.f, 0.f};
#pragma unroll
        for (int i = 0; i < 5; ++i) {
          const u32x2 gv = *(LAS u32x2*)(G + (r + i) * GROW + (nt * 32 + 8 * q + 4 * h) * 2);
          const u32x2 dv = *(LAS u32x2*)(lds + L_DW + i * 512 + (w * 64 + nt * 32 + 8 * q + 4 * h) * 2);
          e[0] += bflo(gv[0]) * bflo(dv[0]); e[1] += bfhi(gv[0]) * bfhi(dv[0]);
          e[2] += bflo(gv[1]) * bflo(dv[1]); e[3] += bfhi(gv[1]) * bfhi(dv[1]);
        }
#pragma unroll
        for (int j = 0; j < 4; ++j) c[4 * q + j] = siluf_(e[j]);
      }
#pragma unroll
      for (int s = 0; s < 2; ++s) *(LAS bf16x8*)(lds + L_X + (4 * w + 2 * nt + s) * 1024 + lane * 16) = pack8(c, s);
    }
    lds_barrier();
  }
  STAMP(2, 5);
  f32x16 x3[1][2];
  x3[0][0] = xv[0][0]; x3[0][1] = xv[0][1];
  gemm_k256<1, false, false>(st, x3, lds + L_X + lane * 16, NoWork{});
  STAMP(2, 6);
  ln_to_frags<1>(x3, lds, w, lane);
  STAMP(2, 7);
  f32x16 acc[1][2];
  ff_block<true>(st, lds, w, lane, acc);
  x3[0][0] += 0.5f * acc[0][0]; x3[0][1] += 0.5f * acc[0][1];
  STAMP(2, 8);
  float mean[1], rstd[1];
  ln_stats<1>(x3, lds, w, lane, mean, rstd);
  char* orow = out + (size_t)(tb * 32 + r) * 512 + (w * 64 + 4 * h) * 2;
#pragma unroll
  for (int nt = 0; nt < 2; ++nt) {
    const f32x16 n = (x3[0][nt] - mean[0]) * rstd[0];
#pragma unroll
    for (int q = 0; q < 4; ++q) *(u32x2*)(orow + (nt * 32 + 8 * q) * 2) = (u32x2){pk2(n[4 * q], n[4 * q + 1]), pk2(n[4 * q + 2], n[4 * q + 3])};
  }
  STAMP(2, 29);
}

extern "C" void kernel_launch(void* const* d_in, const int* in_sizes, int n_in, void* d_out, int out_size, void* d_ws, size_t ws_size,
                              hipStream_t stream) {
  const char* x = (const char*)d_in[0];
  const char* w1a = (const char*)d_in[1];
  const char* w1b = (const char*)d_in[2];
  const char* wqkv = (const char*)d_in[3];
  const char* wo = (const char*)d_in[4];
  const char* pw1 = (const char*)d_in[5];
  const char* dw = (const char*)d_in[6];
  const char* pw2 = (const char*)d_in[7];
  const char* w2a = (const char*)d_in[8];
  const char* w2b = (const char*)d_in[9];
  char* ws = (char*)d_ws;
  (void)hipFuncSetAttribute((const void*)attn_kernel, hipFuncAttributeMaxDynamicSharedMemorySize, A_LDS);
  WPtrs wp{w1a, w1b, wqkv, wo, pw1, pw2, w2a, w2b};
  prep_kernel<<<(NP1 + NP3) * 8, 256, 0, stream>>>(wp, ws);
  k1_kernel<<<NTB, 256, L_TOTAL, stream>>>(x, ws);
  attn_kernel<<<256, 256 * A_KG, A_LDS, stream>>>(ws);
  k3_kernel<<<NTB, 256, L_TOTAL, stream>>>(dw, ws, (char*)d_out);
}
```

```cpp
#include <hip/hip_runtime.h>
#include <hip/hip_bf16.h>
#include <stdint.h>

typedef short bf16x8 __attribute__((ext_vector_type(8)));
typedef short s16x4 __attribute__((ext_vector_type(4)));
typedef float f32x16 __attribute__((ext_vector_type(16)));
typedef float f32x2 __attribute__((ext_vector_type(2)));
typedef __bf16 bf16x2_t __attribute__((ext_vector_type(2)));
typedef unsigned u32x4 __attribute__((ext_vector_type(4)));
typedef unsigned u32x2 __attribute__((ext_vector_type(2)));
#define LAS __attribute__((address_space(3)))
#define DI __device__ __forceinline__
#define MFMA(a, b, c) __builtin_amdgcn_mfma_f32_32x32x16_bf16((a), (b), (c), 0, 0, 0)
#define MFMA16(a, b, c) __builtin_amdgcn_mfma_f32_16x16x32_bf16((a), (b), (c), 0, 0, 0)
#define SB() __builtin_amdgcn_sched_barrier(0)
#define STAMP(k, i) do {} while (0)
typedef float f32x4 __attribute__((ext_vector_type(4)));

constexpr int TSEQ = 4000, NBLK = 125, NTB = 250;
constexpr size_t WS_X1F = 0;
constexpr size_t WS_QF = 4u << 20;
constexpr size_t WS_KVF = 8u << 20;
constexpr size_t WS_ATTF = 17u << 20;
constexpr float LN_EPS = 1e-5f;
constexpr float LOG2E = 1.4426950408889634f;
constexpr float QSCALE = 0.125f * LOG2E;

DI unsigned pk2(float a, float b) { f32x2 v = {a, b}; return __builtin_bit_cast(unsigned, __builtin_convertvector(v, bf16x2_t)); }
DI float bflo(unsigned u) { return __uint_as_float(u << 16); }
DI float bfhi(unsigned u) { return __uint_as_float(u & 0xffff0000u); }
DI float rbf(float a) { return bflo(pk2(a, a)); }
DI bf16x8 pack8(const f32x16& v, int s) {
  u32x4 p = {pk2(v[8 * s], v[8 * s + 1]), pk2(v[8 * s + 2], v[8 * s + 3]), pk2(v[8 * s + 4], v[8 * s + 5]), pk2(v[8 * s + 6], v[8 * s + 7])};
  return __builtin_bit_cast(bf16x8, p);
}
DI void unpack8(u32x4 p, f32x16& v, int s) {
#pragma unroll
  for (int i = 0; i < 4; ++i) { v[8 * s + 2 * i] = bflo(p[i]); v[8 * s + 2 * i + 1] = bfhi(p[i]); }
}
DI float halfsum(float x) {
  auto r = __builtin_amdgcn_permlane32_swap(__float_as_uint(x), __float_as_uint(x), false, false);
  return __uint_as_float(r[0]) + __uint_as_float(r[1]);
}
DI float halfmax(float x) {
  auto r = __builtin_amdgcn_permlane32_swap(__float_as_uint(x), __float_as_uint(x), false, false);
  return fmaxf(__uint_as_float(r[0]), __uint_as_float(r[1]));
}
DI float sigmoidf_(float x) { return __builtin_amdgcn_rcpf(1.0f + __builtin_amdgcn_exp2f(-LOG2E * x)); }
DI float siluf_(float x) { return x * sigmoidf_(x); }
DI void lds_barrier() { asm volatile("s_waitcnt lgkmcnt(0)\n\ts_barrier" ::: "memory"); }
typedef short v4i16_t __attribute__((ext_vector_type(4)));
DI s16x4 trread(LAS const char* p) { return __builtin_bit_cast(s16x4, __builtin_amdgcn_ds_read_tr16_b64_v4i16((LAS v4i16_t*)p)); }
DI bf16x8 ldsfrag(LAS const char* p) { return *(LAS const bf16x8*)p; }

constexpr size_t WS_WT1 = 24u << 20, WS_WT3 = 26u << 20;
constexpr int NP1 = 11, NP3 = 12;
struct WPtrs { const char *w1a, *w1b, *wqkv, *wo, *pw1, *pw2, *w2a, *w2b; };
DI void ff_panel(const char* wa, const char* wb, int p, int w2, const char*& base, unsigned& ld) {
  const bool isb = (p == 2 || p == 4 || p >= 6);
  const int c = p < 2 ? p : (p == 3 ? 2 : (p == 5 ? 3 : (p == 2 ? 0 : (p == 4 ? 1 : p - 4))));
  if (isb) { base = wb + (size_t)c * 256 * 512 + w2 * 128; ld = 512; } else { base = wa + (size_t)(w2 + 4 * c) * 128; ld = 2048; }
}
__global__ __launch_bounds__(256) void prep_kernel(WPtrs wp, char* __restrict__ ws) {
  __shared__ __attribute__((aligned(16))) char sm[4 * 4096];
  const int lane = threadIdx.x & 63, wv = __builtin_amdgcn_readfirstlane(threadIdx.x >> 6);
  const int g = blockIdx.x * 4 + wv;
  const bool k3 = g >= 4 * NP1 * 8;
  const int gg = k3 ? g - 4 * NP1 * 8 : g, np = k3 ? NP3 : NP1;
  const int w2 = gg / (np * 8), p = (gg >> 3) % np, sl = gg & 7;
  const char* base; unsigned ld;
  if (!k3) {
    if (p < 8) ff_panel(wp.w1a, wp.w1b, p, w2, base, ld);
    else { base = wp.wqkv + (p - 8) * 512 + w2 * 128; ld = 1536; }
  } else {
    if (p == 0) { base = wp.wo + w2 * 128; ld = 512; }
    else if (p < 3) { base = wp.pw1 + (p - 1) * 512 + w2 * 128; ld = 1024; }
    else if (p == 3) { base = wp.pw2 + w2 * 128; ld = 512; }
    else ff_panel(wp.w2a, wp.w2b, p - 4, w2, base, ld);
  }
  LAS char* my = (LAS char*)sm + wv * 4096;
  const char* src = base + (size_t)(sl * 32 + (lane >> 3)) * ld + (((lane & 7) ^ (((lane >> 4) & 1) << 2)) << 4);
  u32x4 t[4];
#pragma unroll
  for (int i = 0; i < 4; ++i) t[i] = *(const u32x4*)(src + (size_t)i * 8 * ld);
#pragma unroll
  for (int i = 0; i < 4; ++i) *(LAS u32x4*)(my + i * 1024 + lane * 16) = t[i];
  asm volatile("s_waitcnt lgkmcnt(0)" ::: "memory");
  const int h = lane >> 5, cb = (lane >> 4) & 1, q = (lane & 15) >> 2, pp = lane & 3;
  char* dst = ws + (k3 ? WS_WT3 : WS_WT1) + (size_t)((2 * w2) * np + p) * 16384 + sl * 2048 + lane * 16;
#pragma unroll
  for (int i = 0; i < 4; ++i) {
    LAS const char* rp = my + (4 * h + q) * 128 + (((i & 1) ^ (q >> 1)) * 64) + cb * 32 + pp * 8 + (i >> 1) * 2048;
    s16x4 l = trread(rp), hh = trread(rp + 1024);
    *(bf16x8*)(dst + (size_t)(i & 1) * np * 16384 + (i >> 1) * 1024) = __builtin_shufflevector(l, hh, 0, 1, 2, 3, 4, 5, 6, 7);
  }
}

struct Stream {
  const char* p;
  bf16x8 wf[16];
  DI void start(const char* base) {
    p = base;
#pragma unroll
    for (int i = 0; i < 16; ++i) wf[i] = *(const bf16x8*)(p + i * 1024);
    p += 16384;
  }
};
struct NoWork { DI void init() {} DI void step(int) {} };
struct SiluWork {
  LAS char* p;
  u32x2 raw; float x[4], e[4];
  DI void init() { raw = *(LAS u32x2*)p; }
  DI void step(int ks) {
    const int g = ks >> 2, i = ks & 3;
    if (i == 0) {
      x[0] = bflo(raw[0]); x[1] = bfhi(raw[0]); x[2] = bflo(raw[1]); x[3] = bfhi(raw[1]);
#pragma unroll
      for (int j = 0; j < 4; ++j) e[j] = -LOG2E * x[j];
      asm volatile("" : "+v"(e[0]), "+v"(e[1]), "+v"(e[2]), "+v"(e[3]), "+v"(x[0]), "+v"(x[1]), "+v"(x[2]), "+v"(x[3]));
      if (g < 3) raw = *(LAS u32x2*)(p + ((g + 1) >> 1) * 1024 + ((g + 1) & 1) * 8);
    } else if (i == 1) {
#pragma unroll
      for (int j = 0; j < 4; ++j) e[j] = __builtin_amdgcn_exp2f(e[j]);
      asm volatile("" : "+v"(e[0]), "+v"(e[1]), "+v"(e[2]), "+v"(e[3]));
    } else if (i == 2) {
#pragma unroll
      for (int j = 0; j < 4; ++j) e[j] = __builtin_amdgcn_rcpf(1.0f + e[j]);
      asm volatile("" : "+v"(e[0]), "+v"(e[1]), "+v"(e[2]), "+v"(e[3]));
    } else {
      *(LAS u32x2*)(p + (g >> 1) * 1024 + (g & 1) * 8) = (u32x2){pk2(x[0] * e[0], x[1] * e[1]), pk2(x[2] * e[2], x[3] * e[3])};
    }
  }
};
template <int NB, bool SWAP, bool LAST, class W>
DI void gemm_k256(Stream& st, f32x16 (&acc)[NB], LAS const char* bl, W work) {
  bf16x8 bq[3][NB];
#pragma unroll
  for (int nb = 0; nb < NB; ++nb) { bq[0][nb] = ldsfrag(bl + nb * 16384); bq[1][nb] = ldsfrag(bl + nb * 16384 + 1024); }
  work.init();
#pragma unroll
  for (int ks = 0; ks < 16; ++ks) {
    SB();
#pragma unroll
    for (int nb = 0; nb < NB; ++nb)
      acc[nb] = SWAP ? MFMA(bq[ks % 3][nb], st.wf[ks], acc[nb]) : MFMA(st.wf[ks], bq[ks % 3][nb], acc[nb]);
    if (ks < 14) {
#pragma unroll
      for (int nb = 0; nb < NB; ++nb) bq[(ks + 2) % 3][nb] = ldsfrag(bl + nb * 16384 + (ks + 2) * 1024);
    }
    if (!LAST && ks > 0) st.wf[ks - 1] = *(const bf16x8*)(st.p + (ks - 1) * 1024);
    work.step(ks);
  }
  SB();
  if (!LAST) { st.wf[15] = *(const bf16x8*)(st.p + 15 * 1024); st.p += 16384; }
}

constexpr int L_X = 0, L_Y = L_X + 16384, L_Z = L_Y + 16384;
constexpr int L_STAT = L_Z + 16384;
constexpr int GROW = 72, GSZ = 36 * GROW;
constexpr int L_DW = L_STAT + 4096;
constexpr int L_TOTAL = L_DW + 2560;

DI float sum16(const f32x16& t) {
  return (((t[0] + t[1]) + (t[2] + t[3])) + ((t[4] + t[5]) + (t[6] + t[7]))) + (((t[8] + t[9]) + (t[10] + t[11])) + ((t[12] + t[13]) + (t[14] + t[15])));
}
template <int NB>
DI void ln_stats(const f32x16 (&v)[NB], LAS char* lds, int w, int lane, float (&mean)[NB], float (&rstd)[NB]) {
  const int r = lane & 31;
#pragma unroll
  for (int nb = 0; nb < NB; ++nb) {
    const float mw = halfsum(sum16(v[nb])) * (1.0f / 32.0f);
    const f32x16 d = v[nb] - mw;
    const float m2 = halfsum(sum16(d * d));
    if (lane < 32) *(LAS f32x2*)(lds + L_STAT + ((nb * 32 + r) * 8 + w) * 8) = (f32x2){mw, m2};
  }
  lds_barrier();
#pragma unroll
  for (int nb = 0; nb < NB; ++nb) {
    f32x4 s[4];
#pragma unroll
    for (int i = 0; i < 4; ++i) s[i] = *(LAS f32x4*)(lds + L_STAT + (nb * 32 + r) * 64 + i * 16);
    const float m = 0.125f * (((s[0][0] + s[0][2]) + (s[1][0] + s[1][2])) + ((s[2][0] + s[2][2]) + (s[3][0] + s[3][2])));
    float M2 = 0.f;
#pragma unroll
    for (int i = 0; i < 4; ++i) { const float e0 = s[i][0] - m, e1 = s[i][2] - m; M2 += s[i][1] + s[i][3] + 32.0f * (e0 * e0 + e1 * e1); }
    mean[nb] = m; rstd[nb] = __builtin_amdgcn_rsqf(M2 * (1.0f / 256.0f) + LN_EPS);
  }
}
template <int NB>
DI void ln_to_frags(const f32x16 (&v)[NB], LAS char* lds, int w, int lane) {
  float mean[NB], rstd[NB];
  ln_stats<NB>(v, lds, w, lane, mean, rstd);
  LAS char* fb = lds + L_X + w * 2048 + lane * 16;
#pragma unroll
  for (int nb = 0; nb < NB; ++nb) {
    const f32x16 n = (v[nb] - mean[nb]) * rstd[nb];
#pragma unroll
    for (int s = 0; s < 2; ++s) *(LAS bf16x8*)(fb + nb * 16384 + s * 1024) = pack8(n, s);
  }
  lds_barrier();
}

template <bool LAST>
DI void ff_block(Stream& st, LAS char* lds, int w, int lane, f32x16 (&out)[1]) {
  out[0] = f32x16{};
  LAS char* xb = lds + L_X + lane * 16;
  LAS char* yb = lds + L_Y + lane * 16;
#pragma unroll
  for (int c = 0; c < 4; ++c) {
    f32x16 hn[1]; hn[0] = f32x16{};
    if (c == 0) gemm_k256<1, false, false>(st, hn, xb, NoWork{});
    else gemm_k256<1, false, false>(st, hn, xb, SiluWork{yb + ((c - 1) & 1) * 16384 + w * 2048});
    STAMP(LAST ? 2 : 0, 10 + 3 * c);
    if (c > 0) lds_barrier();
#pragma unroll
    for (int f = 0; f < 2; ++f) *(LAS bf16x8*)(yb + (c & 1) * 16384 + w * 2048 + f * 1024) = pack8(hn[0], f);
    if (c > 0) {
      if (c < 3) gemm_k256<1, false, false>(st, out, yb + ((c - 1) & 1) * 16384, NoWork{});
      else gemm_k256<1, false, false>(st, out, yb, SiluWork{yb + 16384 + w * 2048});
      STAMP(LAST ? 2 : 0, 11 + 3 * c);
    }
  }
  lds_barrier();
  gemm_k256<1, false, LAST>(st, out, yb + 16384, NoWork{});
}

DI void l2_warm(const char* p, int kb_total, int idx, int lane, u32x4& t) {
  const int per = (kb_total + 247) / 248;
  int b = idx * per, e = b + per; if (e > kb_total) e = kb_total;
  for (int i = b; i < e; ++i)
    asm volatile("global_load_dwordx4 %0, %1, %2" : "+v"(t) : "v"(lane * 16 + i * 1024), "s"(p));
}

__global__ __launch_bounds__(512, 2) void k1_kernel(const char* __restrict__ x, char* __restrict__ ws) {
  extern __shared__ __attribute__((aligned(16))) char smem[];
  LAS char* lds = (LAS char*)smem;
  const int lane = threadIdx.x & 63, w = __builtin_amdgcn_readfirstlane(threadIdx.x >> 6), r = lane & 31, h = lane >> 5;
  const int tb = blockIdx.x;
  STAMP(0, 0);
  f32x16 xv[1];
  Stream st;
  u32x4 wt = {0u, 0u, 0u, 0u};
  {
    const char* xrow = x + (size_t)(tb * 32 + r) * 512 + (w * 32 + 4 * h) * 2;
    u32x2 u[4];
#pragma unroll
    for (int g = 0; g < 4; ++g) u[g] = *(const u32x2*)(xrow + g * 16);
    SB();
    st.start(ws + WS_WT1 + (size_t)w * NP1 * 16384 + lane * 16);
    SB();
    if ((tb >> 3) < 31) l2_warm(ws + WS_WT1, 8 * NP1 * 16, (tb >> 3) * 8 + w, lane, wt);
#pragma unroll
    for (int g = 0; g < 4; ++g) {
      xv[0][4 * g] = bflo(u[g][0]); xv[0][4 * g + 1] = bfhi(u[g][0]); xv[0][4 * g + 2] = bflo(u[g][1]); xv[0][4 * g + 3] = bfhi(u[g][1]);
    }
  }
  STAMP(0, 1);
  ln_to_frags<1>(xv, lds, w, lane);
  STAMP(0, 2);
  f32x16 acc[1];
  ff_block<false>(st, lds, w, lane, acc);
  asm volatile("" :: "v"(wt));
  STAMP(0, 3);
#pragma unroll
  for (int i = 0; i < 16; ++i) xv[0][i] = rbf(xv[0][i] + 0.5f * acc[0][i]);
#pragma unroll
  for (int s = 0; s < 2; ++s) *(bf16x8*)(ws + WS_X1F + ((size_t)tb * 16 + 2 * w + s) * 1024 + lane * 16) = pack8(xv[0], s);
  ln_to_frags<1>(xv, lds, w, lane);
  STAMP(0, 4);
  const int b = tb / NBLK, blk = tb - b * NBLK, bh = b * 4 + (w >> 1);
  char* kvdst = ws + WS_KVF + (size_t)(bh * NBLK + blk) * 8192 + (w & 1) * 2048 + lane * 16;
  {
    f32x16 a[1]; a[0] = f32x16{};
    gemm_k256<1, false, false>(st, a, lds + L_X + lane * 16, NoWork{});
    a[0] = a[0] * QSCALE;
    char* dst = ws + WS_QF + (size_t)(bh * NBLK + blk) * 4096 + (w & 1) * 2048 + lane * 16;
    *(bf16x8*)(dst) = pack8(a[0], 0); *(bf16x8*)(dst + 1024) = pack8(a[0], 1);
  }
  {
    f32x16 a[1]; a[0] = f32x16{};
    gemm_k256<1, false, false>(st, a, lds + L_X + lane * 16, NoWork{});
    *(bf16x8*)(kvdst) = pack8(a[0], 0); *(bf16x8*)(kvdst + 1024) = pack8(a[0], 1);
  }
  STAMP(0, 5);
  {
    f32x16 a[1]; a[0] = f32x16{};
    gemm_k256<1, true, true>(st, a, lds + L_X + lane * 16, NoWork{});
    *(bf16x8*)(kvdst + 4096) = pack8(a[0], 0); *(bf16x8*)(kvdst + 5120) = pack8(a[0], 1);
  }
  STAMP(0, 29);
}

constexpr int A_KG = 2, A_SBLK = A_KG * 2, A_HST = A_SBLK * 4096, A_NST = (NBLK + A_SBLK - 1) / A_SBLK, A_NFULL = NBLK / A_SBLK;
constexpr int A_VOFF = 3 * A_HST, A_LDS = 6 * A_HST;
__global__ __launch_bounds__(256 * A_KG, A_KG) void attn_kernel(char* __restrict__ ws) {
  extern __shared__ __attribute__((aligned(16))) char smem[];
  LAS char* lds = (LAS char*)smem;
  const int lane = threadIdx.x & 63, w = __builtin_amdgcn_readfirstlane(threadIdx.x >> 6);
  const int bh = blockIdx.x & 7, qg = blockIdx.x >> 3, qi = w & 3, kg = w >> 2;
  const int qb_raw = qg * 4 + qi, qb = qb_raw < NBLK ? qb_raw : NBLK - 1;
  STAMP(1, 0);
  const char* kv = ws + WS_KVF + (size_t)bh * NBLK * 8192;
  const unsigned lds0 = (unsigned)(uintptr_t)lds;
  const unsigned voff = (w >> 1) * 8192 + (w & 1) * 2048 + lane * 16;
  auto dma2 = [&](int st, int isv, unsigned ldsoff) {
    asm volatile("s_mov_b32 m0, %2\n\ts_nop 0\n\tglobal_load_lds_dwordx4 %0, %1\n\tglobal_load_lds_dwordx4 %0, %1 offset:1024"
                 :: "v"(voff), "s"(kv + (size_t)st * (2 * A_HST) + isv * 4096), "s"(lds0 + ldsoff + w * 2048));
  };
  dma2(0, 0, 0); dma2(1, 0, A_HST); dma2(0, 1, A_VOFF); dma2(2, 0, 2 * A_HST); dma2(1, 1, A_VOFF + A_HST);
  bf16x8 qf[4];
#pragma unroll
  for (int f = 0; f < 4; ++f) qf[f] = *(const bf16x8*)(ws + WS_QF + ((size_t)(bh * NBLK + qb) * 4 + f) * 1024 + lane * 16);
#pragma unroll
  for (int f = 0; f < 4; ++f) asm volatile("" : "+v"(qf[f]));
  u32x4 wt = {0u, 0u, 0u, 0u};
  {
    const int i0 = (qg * 8 + w) * 6;
#pragma unroll
    for (int i = 0; i < 6; ++i) {
      const char* p = ws + WS_WT3 + (i0 + i) * 1024;
      asm volatile("global_load_dwordx4 %0, %1, %2" : "+v"(wt) : "v"(lane * 16), "s"(p));
    }
  }
  const int rem = NBLK - A_NFULL * A_SBLK - kg * 2;
  const int ntail = rem < 0 ? 0 : (rem > 2 ? 2 : rem);
  LAS const char* base = lds + kg * 8192 + lane * 16;
  f32x16 o0 = f32x16{}, o1 = f32x16{}, negr, sa, sb;
  f32x4 lacc = {0.f, 0.f, 0.f, 0.f};
  bf16x8 sel;
  { const short one = (((lane >> 4) ^ lane) & 1) ? (short)0 : (short)0x3f80;
#pragma unroll
    for (int j = 0; j < 8; ++j) sel[j] = one; }
  bf16x8 kf[4], vf[4], kg2[4], vg2[4]; unsigned pa[8], pb[8];
#pragma unroll
  for (int i = 0; i < 8; ++i) pa[i] = 0u;
  float ref;
  asm volatile("s_waitcnt vmcnt(0)\n\ts_barrier" : "+v"(wt) :: "memory");
  {
#pragma unroll
    for (int f = 0; f < 4; ++f) kf[f] = ldsfrag(base + f * 1024);
    f32x16 z = f32x16{};
    sa = MFMA(kf[0], qf[0], z); sa = MFMA(kf[1], qf[1], sa); sa = MFMA(kf[2], qf[2], sa); sa = MFMA(kf[3], qf[3], sa);
    float mx = sa[0];
#pragma unroll
    for (int i = 1; i < 16; ++i) mx = fmaxf(mx, sa[i]);
    ref = halfmax(mx) + 7.0f;
#pragma unroll
    for (int i = 0; i < 16; ++i) { sa[i] -= ref; negr[i] = -ref; }
#pragma unroll
    for (int f = 0; f < 4; ++f) { kf[f] = ldsfrag(base + 4096 + f * 1024); vf[f] = bf16x8{}; }
  }
#define SLOT3(MF, RF, i) do { MF; RF; e[2*(i)] = __builtin_amdgcn_exp2f(SC_[2*(i)]); e[2*(i)+1] = __builtin_amdgcn_exp2f(SC_[2*(i)+1]); \
    if ((i) > 0) PN_[(i)-1] = pk2(e[2*(i)-2], e[2*(i)-1]); SB(); } while (0)
  auto iter = [&](f32x16& SC_, f32x16& SN_, unsigned (&PP_)[8], unsigned (&PN_)[8], bf16x8 (&KC)[4], bf16x8 (&VC)[4], bf16x8 (&KN)[4], bf16x8 (&VN)[4], LAS const char* kn, LAS const char* vc) {
    float e[16];
    const bf16x8 p0 = __builtin_bit_cast(bf16x8, (u32x4){PP_[0], PP_[1], PP_[2], PP_[3]}), p1 = __builtin_bit_cast(bf16x8, (u32x4){PP_[4], PP_[5], PP_[6], PP_[7]});
    SB();
    SLOT3(SN_ = MFMA(KC[0], qf[0], negr), KN[0] = ldsfrag(kn), 0);
    SLOT3(o0 = MFMA(VC[0], p0, o0), VN[0] = ldsfrag(vc), 1);
    SLOT3(SN_ = MFMA(KC[1], qf[1], SN_), KN[1] = ldsfrag(kn + 1024), 2);
    SLOT3(o1 = MFMA(VC[2], p0, o1); lacc = MFMA16(sel, p0, lacc), VN[2] = ldsfrag(vc + 2048), 3);
    SLOT3(SN_ = MFMA(KC[2], qf[2], SN_), KN[2] = ldsfrag(kn + 2048), 4);
    SLOT3(o0 = MFMA(VC[1], p1, o0), VN[1] = ldsfrag(vc + 1024), 5);
    SLOT3(SN_ = MFMA(KC[3], qf[3], SN_), KN[3] = ldsfrag(kn + 3072), 6);
    SLOT3(o1 = MFMA(VC[3], p1, o1); lacc = MFMA16(sel, p1, lacc), VN[3] = ldsfrag(vc + 3072), 7);
    PN_[7] = pk2(e[14], e[15]);
    const unsigned orall = (PN_[0] | PN_[1] | PN_[2]) | (PN_[3] | PN_[4] | PN_[5]) | (PN_[6] | PN_[7]);
    if (__builtin_expect(__any((orall & 0x40004000u) != 0u), 0)) {
      float mx = SC_[0];
#pragma unroll
      for (int i = 1; i < 16; ++i) mx = fmaxf(mx, SC_[i]);
      mx = halfmax(mx);
      const float delta = (mx >= 0.5f) ? mx + 7.0f : 0.0f;
      const float alpha = __builtin_amdgcn_exp2f(-delta);
      const float alo = __shfl_xor(alpha, 16);
      const float aA = (lane & 16) ? alo : alpha, aB = (lane & 16) ? alpha : alo;
      lacc = lacc * (f32x4){aA, aB, aA, aB};
      o0 = o0 * alpha; o1 = o1 * alpha; negr = negr - delta; SN_ = SN_ - delta;
      asm volatile("" : "+v"(o0), "+v"(o1), "+v"(negr), "+v"(SN_), "+v"(lacc));
      ref = -negr[0];
#pragma unroll
      for (int i = 0; i < 8; ++i) PN_[i] = pk2(__builtin_amdgcn_exp2f(SC_[2*i] - delta), __builtin_amdgcn_exp2f(SC_[2*i+1] - delta));
    }
  };
  STAMP(1, 1);
  int r0 = 0, r1 = A_HST, r2 = 2 * A_HST;
  for (int st = 0; st < A_NFULL; ++st) {
    asm volatile("s_waitcnt vmcnt(4) lgkmcnt(0)\n\ts_barrier" ::: "memory");
    dma2(st + 3 < A_NST ? st + 3 : A_NST - 1, 0, r0); dma2(st + 2 < A_NST ? st + 2 : A_NST - 1, 1, A_VOFF + r2);
    iter(sa, sb, pa, pb, kf, vf, kg2, vg2, base + r1, base + A_VOFF + r0);
    iter(sb, sa, pb, pa, kg2, vg2, kf, vf, base + r1 + 4096, base + A_VOFF + r0 + 4096);
    const int t = r0; r0 = r1; r1 = r2; r2 = t;
  }
  asm volatile("s_waitcnt vmcnt(0) lgkmcnt(0)\n\ts_barrier" ::: "memory");
  if (ntail > 0) {
    iter(sa, sb, pa, pb, kf, vf, kg2, vg2, base + r1, base + A_VOFF + r0);
    if (ntail > 1) iter(sb, sa, pb, pa, kg2, vg2, kf, vf, base + r1 + 4096, base + A_VOFF + r0 + 4096);
    else {
#pragma unroll
      for (int i = 0; i < 8; ++i) pa[i] = pb[i];
#pragma unroll
      for (int i = 0; i < 4; ++i) vf[i] = vg2[i];
    }
  }
  {
    const bf16x8 p0 = __builtin_bit_cast(bf16x8, (u32x4){pa[0], pa[1], pa[2], pa[3]}), p1 = __builtin_bit_cast(bf16x8, (u32x4){pa[4], pa[5], pa[6], pa[7]});
    o0 = MFMA(vf[0], p0, o0); o1 = MFMA(vf[2], p0, o1); o0 = MFMA(vf[1], p1, o0); o1 = MFMA(vf[3], p1, o1);
    lacc = MFMA16(sel, p0, lacc); lacc = MFMA16(sel, p1, lacc);
  }
  float lsum = (lane & 16) ? lacc[1] : lacc[0];
  STAMP(1, 2);
  lds_barrier();
  if (kg > 0) {
    LAS char* cb = lds + ((kg - 1) * 4 + qi) * 9216 + lane * 16;
#pragma unroll
    for (int g = 0; g < 4; ++g) {
      *(LAS f32x4*)(cb + g * 1024) = (f32x4){o0[4 * g], o0[4 * g + 1], o0[4 * g + 2], o0[4 * g + 3]};
      *(LAS f32x4*)(cb + 4096 + g * 1024) = (f32x4){o1[4 * g], o1[4 * g + 1], o1[4 * g + 2], o1[4 * g + 3]};
    }
    *(LAS f32x2*)(lds + ((kg - 1) * 4 + qi) * 9216 + 8192 + lane * 8) = (f32x2){ref, lsum};
  }
  lds_barrier();
  if (kg == 0 && qb_raw < NBLK) {
#pragma unroll
    for (int k = 1; k < A_KG; ++k) {
      LAS char* cb = lds + ((k - 1) * 4 + qi) * 9216 + lane * 16;
      const f32x2 rl = *(LAS f32x2*)(lds + ((k - 1) * 4 + qi) * 9216 + 8192 + lane * 8);
      const float m = fmaxf(ref, rl[0]);
      const float c0 = __builtin_amdgcn_exp2f(ref - m), c1 = __builtin_amdgcn_exp2f(rl[0] - m);
      lsum = lsum * c0 + rl[1] * c1; ref = m;
#pragma unroll
      for (int g = 0; g < 4; ++g) {
        const f32x4 u0 = *(LAS f32x4*)(cb + g * 1024), u1 = *(LAS f32x4*)(cb + 4096 + g * 1024);
#pragma unroll
        for (int j = 0; j < 4; ++j) { o0[4 * g + j] = o0[4 * g + j] * c0 + u0[j] * c1; o1[4 * g + j] = o1[4 * g + j] * c0 + u1[j] * c1; }
      }
    }
    const float inv = 1.0f / lsum;
#pragma unroll
    for (int i = 0; i < 16; ++i) { o0[i] *= inv; o1[i] *= inv; }
    char* dst = ws + WS_ATTF + ((size_t)((bh >> 2) * NBLK + qb) * 16 + (bh & 3) * 4) * 1024 + lane * 16;
    *(bf16x8*)(dst) = pack8(o0, 0); *(bf16x8*)(dst + 1024) = pack8(o0, 1);
    *(bf16x8*)(dst + 2048) = pack8(o1, 0); *(bf16x8*)(dst + 3072) = pack8(o1, 1);
  }
  STAMP(1, 29);
}

__global__ __launch_bounds__(512, 2) void k3_kernel(const char* __restrict__ dw, const char* __restrict__ ws, char* __restrict__ out) {
  extern __shared__ __attribute__((aligned(16))) char smem[];
  LAS char* lds = (LAS char*)smem;
  const int lane = threadIdx.x & 63, w = __builtin_amdgcn_readfirstlane(threadIdx.x >> 6), r = lane & 31, h = lane >> 5;
  const int tb = blockIdx.x, b = tb / NBLK, t0 = (tb - b * NBLK) * 32;
  STAMP(2, 0);
  Stream st;
  const int hc = r & 3, ht = (hc < 2) ? t0 - 2 + hc : t0 + 30 + hc;
  const bool hvalid = ht >= 0 && ht < TSEQ;
  const int hg = b * TSEQ + (ht < 0 ? 0 : (ht >= TSEQ ? TSEQ - 1 : ht));
  const unsigned hoff = (unsigned)(hg >> 5) * 16384 + ((hg & 31) + 32 * h) * 16 + w * 2048;
  const unsigned moff = (unsigned)tb * 16384 + lane * 16 + w * 2048;
  {
    const unsigned ly = (unsigned)(uintptr_t)(lds + L_Y + w * 2048);
    asm volatile("s_mov_b32 m0, %3\n\ts_nop 0\n\tglobal_load_lds_dwordx4 %0, %2\n\tglobal_load_lds_dwordx4 %0, %2 offset:1024\n\t"
                 "s_mov_b32 m0, %4\n\ts_nop 0\n\tglobal_load_lds_dwordx4 %1, %2\n\tglobal_load_lds_dwordx4 %1, %2 offset:1024"
                 :: "v"(moff), "v"(hoff), "s"(ws + WS_ATTF), "s"(ly), "s"(ly + 16384));
  }
  u32x4 xr[2][2];
#pragma unroll
  for (int f = 0; f < 2; ++f) {
    xr[0][f] = *(const u32x4*)(ws + WS_X1F + moff + f * 1024);
    xr[1][f] = *(const u32x4*)(ws + WS_X1F + hoff + f * 1024);
  }
  const u32x4 dwv = *(const u32x4*)(dw + (threadIdx.x < 160 ? threadIdx.x : 159) * 16);
  SB();
  st.start(ws + WS_WT3 + (size_t)w * NP3 * 16384 + lane * 16);
  SB();
  if (threadIdx.x < 160) *(LAS u32x4*)(lds + L_DW + threadIdx.x * 16) = dwv;
  asm volatile("s_waitcnt vmcnt(16)" ::: "memory");
  lds_barrier();
  STAMP(2, 1);
  f32x16 xv[2];
#pragma unroll
  for (int nb = 0; nb < 2; ++nb) { unpack8(xr[nb][0], xv[nb], 0); unpack8(xr[nb][1], xv[nb], 1); }
  gemm_k256<2, false, false>(st, xv, lds + L_Y + lane * 16, NoWork{});
  STAMP(2, 2);
  ln_to_frags<2>(xv, lds, w, lane);
  STAMP(2, 3);
  {
    f32x16 a[2], g[2];
#pragma unroll
    for (int nb = 0; nb < 2; ++nb) { a[nb] = f32x16{}; g[nb] = f32x16{}; }
    gemm_k256<2, false, false>(st, a, lds + L_X + lane * 16, NoWork{});
    gemm_k256<2, false, false>(st, g, lds + L_X + lane * 16, NoWork{});
    STAMP(2, 4);
    LAS char* G = lds + L_Y + w * GSZ;
    lds_barrier();
#pragma unroll
    for (int nb = 0; nb < 2; ++nb) {
      const int row = nb == 0 ? r + 2 : (hc < 2 ? hc : 32 + hc);
      const bool wr = nb == 0 || r < 4;
      const float keep = (nb == 0 || hvalid) ? 1.0f : 0.0f;
#pragma unroll
      for (int q = 0; q < 4; ++q) {
        float e[4];
#pragma unroll
        for (int j = 0; j < 4; ++j) e[j] = keep * a[nb][4 * q + j] * sigmoidf_(g[nb][4 * q + j]);
        if (wr) *(LAS u32x2*)(G + row * GROW + (8 * q + 4 * h) * 2) = (u32x2){pk2(e[0], e[1]), pk2(e[2], e[3])};
      }
    }
    asm volatile("s_waitcnt lgkmcnt(0)" ::: "memory");
    f32x16 c;
#pragma unroll
    for (int q = 0; q < 4; ++q) {
      float e[4] = {0.f, 0.f, 0.f, 0.f};
#pragma unroll
      for (int i = 0; i < 5; ++i) {
        const u32x2 gv = *(LAS u32x2*)(G + (r + i) * GROW + (8 * q + 4 * h) * 2);
        const u32x2 dv = *(LAS u32x2*)(lds + L_DW + i * 512 + (w * 32 + 8 * q + 4 * h) * 2);
        e[0] += bflo(gv[0]) * bflo(dv[0]); e[1] += bfhi(gv[0]) * bfhi(dv[0]);
        e[2] += bflo(gv[1]) * bflo(dv[1]); e[3] += bfhi(gv[1]) * bfhi(dv[1]);
      }
#pragma unroll
      for (int j = 0; j < 4; ++j) c[4 * q + j] = siluf_(e[j]);
    }
#pragma unroll
    for (int s = 0; s < 2; ++s) *(LAS bf16x8*)(lds + L_X + (2 * w + s) * 1024 + lane * 16) = pack8(c, s);
    lds_barrier();
  }
  STAMP(2, 5);
  f32x16 x3[1];
  x3[0] = xv[0];
  gemm_k256<1, false, false>(st, x3, lds + L_X + lane * 16, NoWork{});
  STAMP(2, 6);
  ln_to_frags<1>(x3, lds, w, lane);
  STAMP(2, 7);
  f32x16 acc[1];
  ff_block<true>(st, lds, w, lane, acc);
  x3[0] += 0.5f * acc[0];
  STAMP(2, 8);
  float mean[1], rstd[1];
  ln_stats<1>(x3, lds, w, lane, mean, rstd);
  char* orow = out + (size_t)(tb * 32 + r) * 512 + (w * 32 + 4 * h) * 2;
  const f32x16 n = (x3[0] - mean[0]) * rstd[0];
#pragma unroll
  for (int q = 0; q < 4; ++q) *(u32x2*)(orow + q * 16) = (u32x2){pk2(n[4 * q], n[4 * q + 1]), pk2(n[4 * q + 2], n[4 * q + 3])};
  STAMP(2, 29);
}

extern "C" void kernel_launch(void* const* d_in, const int* in_sizes, int n_in, void* d_out, int out_size, void* d_ws, size_t ws_size,
                              hipStream_t stream) {
  const char* x = (const char*)d_in[0];
  const char* w1a = (const char*)d_in[1];
  const char* w1b = (const char*)d_in[2];
  const char* wqkv = (const char*)d_in[3];
  const char* wo = (const char*)d_in[4];
  const char* pw1 = (const char*)d_in[5];
  const char* dw = (const char*)d_in[6];
  const char* pw2 = (const char*)d_in[7];
  const char* w2a = (const char*)d_in[8];
  const char* w2b = (const char*)d_in[9];
  char* ws = (char*)d_ws;
  (void)hipFuncSetAttribute((const void*)attn_kernel, hipFuncAttributeMaxDynamicSharedMemorySize, A_LDS);
  WPtrs wp{w1a, w1b, wqkv, wo, pw1, pw2, w2a, w2b};
  prep_kernel<<<(NP1 + NP3) * 8, 256, 0, stream>>>(wp, ws);
  k1_kernel<<<NTB, 512, L_TOTAL, stream>>>(x, ws);
  attn_kernel<<<256, 256 * A_KG, A_LDS, stream>>>(ws);
  k3_kernel<<<NTB, 512, L_TOTAL, stream>>>(dw, ws, (char*)d_out);
}
```

```cpp
#include <hip/hip_runtime.h>
#include <hip/hip_bf16.h>
#include <stdint.h>

typedef short bf16x8 __attribute__((ext_vector_type(8)));
typedef short s16x4 __attribute__((ext_vector_type(4)));
typedef float f32x16 __attribute__((ext_vector_type(16)));
typedef float f32x2 __attribute__((ext_vector_type(2)));
typedef __bf16 bf16x2_t __attribute__((ext_vector_type(2)));
typedef unsigned u32x4 __attribute__((ext_vector_type(4)));
typedef unsigned u32x2 __attribute__((ext_vector_type(2)));
#define LAS __attribute__((address_space(3)))
#define DI __device__ __forceinline__
#define MFMA(a, b, c) __builtin_amdgcn_mfma_f32_32x32x16_bf16((a), (b), (c), 0, 0, 0)
#define MFMA16(a, b, c) __builtin_amdgcn_mfma_f32_16x16x32_bf16((a), (b), (c), 0, 0, 0)
#define SB() __builtin_amdgcn_sched_barrier(0)
#define STAMP(k, i) do {} while (0)
typedef float f32x4 __attribute__((ext_vector_type(4)));

constexpr int TSEQ = 4000, NBLK = 125, NTB = 250;
constexpr size_t WS_X1F = 0;
constexpr size_t WS_QF = 4u << 20;
constexpr size_t WS_KVF = 8u << 20;
constexpr size_t WS_ATTF = 17u << 20;
constexpr float LN_EPS = 1e-5f;
constexpr float LOG2E = 1.4426950408889634f;
constexpr float QSCALE = 0.125f * LOG2E;

DI unsigned pk2(float a, float b) { f32x2 v = {a, b}; return __builtin_bit_cast(unsigned, __builtin_convertvector(v, bf16x2_t)); }
DI float bflo(unsigned u) { return __uint_as_float(u << 16); }
DI float bfhi(unsigned u) { return __uint_as_float(u & 0xffff0000u); }
DI float rbf(float a) { return bflo(pk2(a, a)); }
DI bf16x8 pack8(const f32x16& v, int s) {
  u32x4 p = {pk2(v[8 * s], v[8 * s + 1]), pk2(v[8 * s + 2], v[8 * s + 3]), pk2(v[8 * s + 4], v[8 * s + 5]), pk2(v[8 * s + 6], v[8 * s + 7])};
  return __builtin_bit_cast(bf16x8, p);
}
DI void unpack8(u32x4 p, f32x16& v, int s) {
#pragma unroll
  for (int i = 0; i < 4; ++i) { v[8 * s + 2 * i] = bflo(p[i]); v[8 * s + 2 * i + 1] = bfhi(p[i]); }
}
DI float halfsum(float x) {
  auto r = __builtin_amdgcn_permlane32_swap(__float_as_uint(x), __float_as_uint(x), false, false);
  return __uint_as_float(r[0]) + __uint_as_float(r[1]);
}
DI float halfmax(float x) {
  auto r = __builtin_amdgcn_permlane32_swap(__float_as_uint(x), __float_as_uint(x), false, false);
  return fmaxf(__uint_as_float(r[0]), __uint_as_float(r[1]));
}
DI float sigmoidf_(float x) { return __builtin_amdgcn_rcpf(1.0f + __builtin_amdgcn_exp2f(-LOG2E * x)); }
DI float siluf_(float x) { return x * sigmoidf_(x); }
DI void lds_barrier() { asm volatile("s_waitcnt lgkmcnt(0)\n\ts_barrier" ::: "memory"); }
typedef short v4i16_t __attribute__((ext_vector_type(4)));
DI s16x4 trread(LAS const char* p) { return __builtin_bit_cast(s16x4, __builtin_amdgcn_ds_read_tr16_b64_v4i16((LAS v4i16_t*)p)); }
DI bf16x8 ldsfrag(LAS const char* p) { return *(LAS const bf16x8*)p; }

constexpr size_t WS_WT1 = 24u << 20, WS_WT3 = 26u << 20;
constexpr int NP1 = 11, NP3 = 12;
struct WPtrs { const char *w1a, *w1b, *wqkv, *wo, *pw1, *pw2, *w2a, *w2b; };
DI void ff_panel(const char* wa, const char* wb, int p, int w2, const char*& base, unsigned& ld) {
  const bool isb = (p == 2 || p == 4 || p >= 6);
  const int c = p < 2 ? p : (p == 3 ? 2 : (p == 5 ? 3 : (p == 2 ? 0 : (p == 4 ? 1 : p - 4))));
  if (isb) { base = wb + (size_t)c * 256 * 512 + w2 * 128; ld = 512; } else { base = wa + (size_t)(w2 + 4 * c) * 128; ld = 2048; }
}
__global__ __launch_bounds__(256) void prep_kernel(WPtrs wp, char* __restrict__ ws) {
  __shared__ __attribute__((aligned(16))) char sm[4 * 4096];
  const int lane = threadIdx.x & 63, wv = __builtin_amdgcn_readfirstlane(threadIdx.x >> 6);
  const int g = blockIdx.x * 4 + wv;
  const bool k3 = g >= 4 * NP1 * 8;
  const int gg = k3 ? g - 4 * NP1 * 8 : g, np = k3 ? NP3 : NP1;
  const int w2 = gg / (np * 8), p = (gg >> 3) % np, sl = gg & 7;
  const char* base; unsigned ld;
  if (!k3) {
    if (p < 8) ff_panel(wp.w1a, wp.w1b, p, w2, base, ld);
    else { base = wp.wqkv + (p - 8) * 512 + w2 * 128; ld = 1536; }
  } else {
    if (p == 0) { base = wp.wo + w2 * 128; ld = 512; }
    else if (p < 3) { base = wp.pw1 + (p - 1) * 512 + w2 * 128; ld = 1024; }
    else if (p == 3) { base = wp.pw2 + w2 * 128; ld = 512; }
    else ff_panel(wp.w2a, wp.w2b, p - 4, w2, base, ld);
  }
  LAS char* my = (LAS char*)sm + wv * 4096;
  const char* src = base + (size_t)(sl * 32 + (lane >> 3)) * ld + (((lane & 7) ^ (((lane >> 4) & 1) << 2)) << 4);
  u32x4 t[4];
#pragma unroll
  for (int i = 0; i < 4; ++i) t[i] = *(const u32x4*)(src + (size_t)i * 8 * ld);
#pragma unroll
  for (int i = 0; i < 4; ++i) *(LAS u32x4*)(my + i * 1024 + lane * 16) = t[i];
  asm volatile("s_waitcnt lgkmcnt(0)" ::: "memory");
  const int h = lane >> 5, cb = (lane >> 4) & 1, q = (lane & 15) >> 2, pp = lane & 3;
  char* dst = ws + (k3 ? WS_WT3 : WS_WT1) + (size_t)((2 * w2) * np + p) * 16384 + sl * 2048 + lane * 16;
#pragma unroll
  for (int i = 0; i < 4; ++i) {
    LAS const char* rp = my + (4 * h + q) * 128 + (((i & 1) ^ (q >> 1)) * 64) + cb * 32 + pp * 8 + (i >> 1) * 2048;
    s16x4 l = trread(rp), hh = trread(rp + 1024);
    *(bf16x8*)(dst + (size_t)(i & 1) * np * 16384 + (i >> 1) * 1024) = __builtin_shufflevector(l, hh, 0, 1, 2, 3, 4, 5, 6, 7);
  }
}

struct Stream {
  const char* p;
  bf16x8 wf[2][16];
  DI void start(const char* base) {
    p = base;
#pragma unroll
    for (int i = 0; i < 32; ++i) wf[i >> 4][i & 15] = *(const bf16x8*)(p + i * 1024);
  }
};
struct NoWork { DI void init() {} DI void step(int) {} };
struct SiluWork {
  LAS char* p;
  u32x2 raw; float x[4], e[4];
  DI void init() { raw = *(LAS u32x2*)p; }
  DI void step(int ks) {
    const int g = ks >> 2, i = ks & 3;
    if (i == 0) {
      x[0] = bflo(raw[0]); x[1] = bfhi(raw[0]); x[2] = bflo(raw[1]); x[3] = bfhi(raw[1]);
#pragma unroll
      for (int j = 0; j < 4; ++j) e[j] = -LOG2E * x[j];
      asm volatile("" : "+v"(e[0]), "+v"(e[1]), "+v"(e[2]), "+v"(e[3]), "+v"(x[0]), "+v"(x[1]), "+v"(x[2]), "+v"(x[3]));
      if (g < 3) raw = *(LAS u32x2*)(p + ((g + 1) >> 1) * 1024 + ((g + 1) & 1) * 8);
    } else if (i == 1) {
#pragma unroll
      for (int j = 0; j < 4; ++j) e[j] = __builtin_amdgcn_exp2f(e[j]);
      asm volatile("" : "+v"(e[0]), "+v"(e[1]), "+v"(e[2]), "+v"(e[3]));
    } else if (i == 2) {
#pragma unroll
      for (int j = 0; j < 4; ++j) e[j] = __builtin_amdgcn_rcpf(1.0f + e[j]);
      asm volatile("" : "+v"(e[0]), "+v"(e[1]), "+v"(e[2]), "+v"(e[3]));
    } else {
      *(LAS u32x2*)(p + (g >> 1) * 1024 + (g & 1) * 8) = (u32x2){pk2(x[0] * e[0], x[1] * e[1]), pk2(x[2] * e[2], x[3] * e[3])};
    }
  }
};
template <int NB, bool SWAP, int P, int NP, class W>
DI void gemm_k256(Stream& st, f32x16 (&acc)[NB], LAS const char* bl, W work) {
  constexpr bool REFILL = P + 2 < NP;
  bf16x8 bq[3][NB];
#pragma unroll
  for (int nb = 0; nb < NB; ++nb) { bq[0][nb] = ldsfrag(bl + nb * 16384); bq[1][nb] = ldsfrag(bl + nb * 16384 + 1024); }
  work.init();
#pragma unroll
  for (int ks = 0; ks < 16; ++ks) {
    SB();
#pragma unroll
    for (int nb = 0; nb < NB; ++nb)
      acc[nb] = SWAP ? MFMA(bq[ks % 3][nb], st.wf[P & 1][ks], acc[nb]) : MFMA(st.wf[P & 1][ks], bq[ks % 3][nb], acc[nb]);
    if (ks < 14) {
#pragma unroll
      for (int nb = 0; nb < NB; ++nb) bq[(ks + 2) % 3][nb] = ldsfrag(bl + nb * 16384 + (ks + 2) * 1024);
    }
    if (REFILL && ks > 0) st.wf[P & 1][ks - 1] = *(const bf16x8*)(st.p + (P + 2) * 16384 + (ks - 1) * 1024);
    work.step(ks);
  }
  SB();
  if (REFILL) st.wf[P & 1][15] = *(const bf16x8*)(st.p + (P + 2) * 16384 + 15 * 1024);
}

constexpr int L_X = 0, L_Y = L_X + 16384, L_Z = L_Y + 16384;
constexpr int L_STAT = L_Z + 16384 + 9216;
constexpr int GROW = 144, GSZ = 36 * GROW;
constexpr int L_DW = L_STAT + 4096;
constexpr int L_TOTAL = L_DW + 5120;

DI float sum16(const f32x16& t) {
  return (((t[0] + t[1]) + (t[2] + t[3])) + ((t[4] + t[5]) + (t[6] + t[7]))) + (((t[8] + t[9]) + (t[10] + t[11])) + ((t[12] + t[13]) + (t[14] + t[15])));
}
template <int NB>
DI void ln_stats(const f32x16 (&v)[NB], LAS char* lds, int w, int lane, float (&mean)[NB], float (&rstd)[NB]) {
  const int r = lane & 31;
#pragma unroll
  for (int nb = 0; nb < NB; ++nb) {
    const float mw = halfsum(sum16(v[nb])) * (1.0f / 32.0f);
    const f32x16 d = v[nb] - mw;
    const float m2 = halfsum(sum16(d * d));
    if (lane < 32) *(LAS f32x2*)(lds + L_STAT + ((nb * 32 + r) * 8 + w) * 8) = (f32x2){mw, m2};
  }
  lds_barrier();
#pragma unroll
  for (int nb = 0; nb < NB; ++nb) {
    f32x4 s[4];
#pragma unroll
    for (int i = 0; i < 4; ++i) s[i] = *(LAS f32x4*)(lds + L_STAT + (nb * 32 + r) * 64 + i * 16);
    const float m = 0.125f * (((s[0][0] + s[0][2]) + (s[1][0] + s[1][2])) + ((s[2][0] + s[2][2]) + (s[3][0] + s[3][2])));
    float M2 = 0.f;
#pragma unroll
    for (int i = 0; i < 4; ++i) { const float e0 = s[i][0] - m, e1 = s[i][2] - m; M2 += s[i][1] + s[i][3] + 32.0f * (e0 * e0 + e1 * e1); }
    mean[nb] = m; rstd[nb] = __builtin_amdgcn_rsqf(M2 * (1.0f / 256.0f) + LN_EPS);
  }
}
template <int NB>
DI void ln_to_frags(const f32x16 (&v)[NB], LAS char* lds, int w, int lane) {
  float mean[NB], rstd[NB];
  ln_stats<NB>(v, lds, w, lane, mean, rstd);
  LAS char* fb = lds + L_X + w * 2048 + lane * 16;
#pragma unroll
  for (int nb = 0; nb < NB; ++nb) {
    const f32x16 n = (v[nb] - mean[nb]) * rstd[nb];
#pragma unroll
    for (int s = 0; s < 2; ++s) *(LAS bf16x8*)(fb + nb * 16384 + s * 1024) = pack8(n, s);
  }
  lds_barrier();
}

template <int P0, int NP>
DI void ff_block(Stream& st, LAS char* lds, int w, int lane, f32x16 (&out)[1]) {
  out[0] = f32x16{};
  LAS char* xb = lds + L_X + lane * 16;
  LAS char* yb = lds + L_Y + lane * 16;
  LAS char* zb = lds + L_Z + lane * 16;
  auto put = [&](LAS char* dst, const f32x16& hv) { *(LAS bf16x8*)(dst + w * 2048) = pack8(hv, 0); *(LAS bf16x8*)(dst + w * 2048 + 1024) = pack8(hv, 1); };
  f32x16 hn[1];
  hn[0] = f32x16{}; gemm_k256<1, false, P0 + 0, NP>(st, hn, xb, NoWork{});
  put(yb, hn[0]);
  hn[0] = f32x16{}; gemm_k256<1, false, P0 + 1, NP>(st, hn, xb, SiluWork{yb + w * 2048});
  lds_barrier(); put(zb, hn[0]);
  gemm_k256<1, false, P0 + 2, NP>(st, out, yb, NoWork{});
  hn[0] = f32x16{}; gemm_k256<1, false, P0 + 3, NP>(st, hn, xb, SiluWork{zb + w * 2048});
  lds_barrier(); put(yb, hn[0]);
  gemm_k256<1, false, P0 + 4, NP>(st, out, zb, NoWork{});
  hn[0] = f32x16{}; gemm_k256<1, false, P0 + 5, NP>(st, hn, xb, SiluWork{yb + w * 2048});
  lds_barrier(); put(zb, hn[0]);
  gemm_k256<1, false, P0 + 6, NP>(st, out, yb, SiluWork{zb + w * 2048});
  lds_barrier();
  gemm_k256<1, false, P0 + 7, NP>(st, out, zb, NoWork{});
}

DI void l2_warm(const char* p, int kb_total, int idx, int lane, u32x4& t) {
  const int per = (kb_total + 247) / 248;
  int b = idx * per, e = b + per; if (e > kb_total) e = kb_total;
  for (int i = b; i < e; ++i)
    asm volatile("global_load_dwordx4 %0, %1, %2" : "+v"(t) : "v"(lane * 16 + i * 1024), "s"(p));
}

__global__ __launch_bounds__(512, 2) void k1_kernel(const char* __restrict__ x, char* __restrict__ ws) {
  extern __shared__ __attribute__((aligned(16))) char smem[];
  LAS char* lds = (LAS char*)smem;
  const int lane = threadIdx.x & 63, w = __builtin_amdgcn_readfirstlane(threadIdx.x >> 6), r = lane & 31, h = lane >> 5;
  const int tb = blockIdx.x;
  STAMP(0, 0);
  f32x16 xv[1];
  Stream st;
  u32x4 wt = {0u, 0u, 0u, 0u};
  {
    const char* xrow = x + (size_t)(tb * 32 + r) * 512 + (w * 32 + 4 * h) * 2;
    u32x2 u[4];
#pragma unroll
    for (int g = 0; g < 4; ++g) u[g] = *(const u32x2*)(xrow + g * 16);
    asm volatile("s_barrier" ::: "memory");
    SB();
    st.start(ws + WS_WT1 + (size_t)w * NP1 * 16384 + lane * 16);
    SB();
    if ((tb >> 3) < 31) l2_warm(ws + WS_WT1, 8 * NP1 * 16, (tb >> 3) * 8 + w, lane, wt);
#pragma unroll
    for (int g = 0; g < 4; ++g) {
      xv[0][4 * g] = bflo(u[g][0]); xv[0][4 * g + 1] = bfhi(u[g][0]); xv[0][4 * g + 2] = bflo(u[g][1]); xv[0][4 * g + 3] = bfhi(u[g][1]);
    }
  }
  STAMP(0, 1);
  ln_to_frags<1>(xv, lds, w, lane);
  STAMP(0, 2);
  f32x16 acc[1];
  ff_block<0, NP1>(st, lds, w, lane, acc);
  asm volatile("" :: "v"(wt));
  STAMP(0, 3);
#pragma unroll
  for (int i = 0; i < 16; ++i) xv[0][i] = rbf(xv[0][i] + 0.5f * acc[0][i]);
#pragma unroll
  for (int s = 0; s < 2; ++s) *(bf16x8*)(ws + WS_X1F + ((size_t)tb * 16 + 2 * w + s) * 1024 + lane * 16) = pack8(xv[0], s);
  ln_to_frags<1>(xv, lds, w, lane);
  STAMP(0, 4);
  const int b = tb / NBLK, blk = tb - b * NBLK, bh = b * 4 + (w >> 1);
  char* kvdst = ws + WS_KVF + (size_t)(bh * NBLK + blk) * 8192 + (w & 1) * 2048 + lane * 16;
  {
    f32x16 a[1]; a[0] = f32x16{};
    gemm_k256<1, false, 8, NP1>(st, a, lds + L_X + lane * 16, NoWork{});
    a[0] = a[0] * QSCALE;
    char* dst = ws + WS_QF + (size_t)(bh * NBLK + blk) * 4096 + (w & 1) * 2048 + lane * 16;
    *(bf16x8*)(dst) = pack8(a[0], 0); *(bf16x8*)(dst + 1024) = pack8(a[0], 1);
  }
  {
    f32x16 a[1]; a[0] = f32x16{};
    gemm_k256<1, false, 9, NP1>(st, a, lds + L_X + lane * 16, NoWork{});
    *(bf16x8*)(kvdst) = pack8(a[0], 0); *(bf16x8*)(kvdst + 1024) = pack8(a[0], 1);
  }
  STAMP(0, 5);
  {
    f32x16 a[1]; a[0] = f32x16{};
    gemm_k256<1, true, 10, NP1>(st, a, lds + L_X + lane * 16, NoWork{});
    *(bf16x8*)(kvdst + 4096) = pack8(a[0], 0); *(bf16x8*)(kvdst + 5120) = pack8(a[0], 1);
  }
  STAMP(0, 29);
}

constexpr int A_KG = 2, A_SBLK = A_KG * 2, A_HST = A_SBLK * 4096, A_NST = (NBLK + A_SBLK - 1) / A_SBLK, A_NFULL = NBLK / A_SBLK;
constexpr int A_VOFF = 3 * A_HST, A_LDS = 6 * A_HST;
__global__ __launch_bounds__(256 * A_KG, A_KG) void attn_kernel(char* __restrict__ ws) {
  extern __shared__ __attribute__((aligned(16))) char smem[];
  LAS char* lds = (LAS char*)smem;
  const int lane = threadIdx.x & 63, w = __builtin_amdgcn_readfirstlane(threadIdx.x >> 6);
  const int bh = blockIdx.x & 7, qg = blockIdx.x >> 3, qi = w & 3, kg = w >> 2;
  const int qb_raw = qg * 4 + qi, qb = qb_raw < NBLK ? qb_raw : NBLK - 1;
  STAMP(1, 0);
  const char* kv = ws + WS_KVF + (size_t)bh * NBLK * 8192;
  const unsigned lds0 = (unsigned)(uintptr_t)lds;
  const unsigned voff = (w >> 1) * 8192 + (w & 1) * 2048 + lane * 16;
  auto dma2 = [&](int st, int isv, unsigned ldsoff) {
    asm volatile("s_mov_b32 m0, %2\n\ts_nop 0\n\tglobal_load_lds_dwordx4 %0, %1\n\tglobal_load_lds_dwordx4 %0, %1 offset:1024"
                 :: "v"(voff), "s"(kv + (size_t)st * (2 * A_HST) + isv * 4096), "s"(lds0 + ldsoff + w * 2048));
  };
  dma2(0, 0, 0); dma2(1, 0, A_HST); dma2(0, 1, A_VOFF); dma2(2, 0, 2 * A_HST); dma2(1, 1, A_VOFF + A_HST);
  bf16x8 qf[4];
#pragma unroll
  for (int f = 0; f < 4; ++f) qf[f] = *(const bf16x8*)(ws + WS_QF + ((size_t)(bh * NBLK + qb) * 4 + f) * 1024 + lane * 16);
#pragma unroll
  for (int f = 0; f < 4; ++f) asm volatile("" : "+v"(qf[f]));
  u32x4 wt = {0u, 0u, 0u, 0u};
  {
    const int i0 = (qg * 8 + w) * 6;
#pragma unroll
    for (int i = 0; i < 6; ++i) {
      const char* p = ws + WS_WT3 + (i0 + i) * 1024;
      asm volatile("global_load_dwordx4 %0, %1, %2" : "+v"(wt) : "v"(lane * 16), "s"(p));
    }
  }
  const int rem = NBLK - A_NFULL * A_SBLK - kg * 2;
  const int ntail = rem < 0 ? 0 : (rem > 2 ? 2 : rem);
  LAS const char* base = lds + kg * 8192 + lane * 16;
  f32x16 o0 = f32x16{}, o1 = f32x16{}, negr, sa, sb;
  f32x4 lacc = {0.f, 0.f, 0.f, 0.f};
  bf16x8 sel;
  { const short one = (((lane >> 4) ^ lane) & 1) ? (short)0 : (short)0x3f80;
#pragma unroll
    for (int j = 0; j < 8; ++j) sel[j] = one; }
  bf16x8 kf[4], vf[4], kg2[4], vg2[4]; unsigned pa[8], pb[8];
#pragma unroll
  for (int i = 0; i < 8; ++i) pa[i] = 0u;
  float ref;
  asm volatile("s_waitcnt vmcnt(0)\n\ts_barrier" : "+v"(wt) :: "memory");
  {
#pragma unroll
    for (int f = 0; f < 4; ++f) kf[f] = ldsfrag(base + f * 1024);
    f32x16 z = f32x16{};
    sa = MFMA(kf[0], qf[0], z); sa = MFMA(kf[1], qf[1], sa); sa = MFMA(kf[2], qf[2], sa); sa = MFMA(kf[3], qf[3], sa);
    float mx = sa[0];
#pragma unroll
    for (int i = 1; i < 16; ++i) mx = fmaxf(mx, sa[i]);
    ref = halfmax(mx) + 7.0f;
#pragma unroll
    for (int i = 0; i < 16; ++i) { sa[i] -= ref; negr[i] = -ref; }
#pragma unroll
    for (int f = 0; f < 4; ++f) { kf[f] = ldsfrag(base + 4096 + f * 1024); vf[f] = bf16x8{}; }
  }
#define SLOT3(MF, RF, i) do { MF; RF; e[2*(i)] = __builtin_amdgcn_exp2f(SC_[2*(i)]); e[2*(i)+1] = __builtin_amdgcn_exp2f(SC_[2*(i)+1]); \
    if ((i) > 0) PN_[(i)-1] = pk2(e[2*(i)-2], e[2*(i)-1]); SB(); } while (0)
  auto iter = [&](f32x16& SC_, f32x16& SN_, unsigned (&PP_)[8], unsigned (&PN_)[8], bf16x8 (&KC)[4], bf16x8 (&VC)[4], bf16x8 (&KN)[4], bf16x8 (&VN)[4], LAS const char* kn, LAS const char* vc) {
    float e[16];
    const bf16x8 p0 = __builtin_bit_cast(bf16x8, (u32x4){PP_[0], PP_[1], PP_[2], PP_[3]}), p1 = __builtin_bit_cast(bf16x8, (u32x4){PP_[4], PP_[5], PP_[6], PP_[7]});
    SB();
    SLOT3(SN_ = MFMA(KC[0], qf[0], negr), KN[0] = ldsfrag(kn), 0);
    SLOT3(o0 = MFMA(VC[0], p0, o0), VN[0] = ldsfrag(vc), 1);
    SLOT3(SN_ = MFMA(KC[1], qf[1], SN_), KN[1] = ldsfrag(kn + 1024), 2);
    SLOT3(o1 = MFMA(VC[2], p0, o1); lacc = MFMA16(sel, p0, lacc), VN[2] = ldsfrag(vc + 2048), 3);
    SLOT3(SN_ = MFMA(KC[2], qf[2], SN_), KN[2] = ldsfrag(kn + 2048), 4);
    SLOT3(o0 = MFMA(VC[1], p1, o0), VN[1] = ldsfrag(vc + 1024), 5);
    SLOT3(SN_ = MFMA(KC[3], qf[3], SN_), KN[3] = ldsfrag(kn + 3072), 6);
    SLOT3(o1 = MFMA(VC[3], p1, o1); lacc = MFMA16(sel, p1, lacc), VN[3] = ldsfrag(vc + 3072), 7);
    PN_[7] = pk2(e[14], e[15]);
    const unsigned orall = (PN_[0] | PN_[1] | PN_[2]) | (PN_[3] | PN_[4] | PN_[5]) | (PN_[6] | PN_[7]);
    if (__builtin_expect(__any((orall & 0x40004000u) != 0u), 0)) {
      float mx = SC_[0];
#pragma unroll
      for (int i = 1; i < 16; ++i) mx = fmaxf(mx, SC_[i]);
      mx = halfmax(mx);
      const float delta = (mx >= 0.5f) ? mx + 7.0f : 0.0f;
      const float alpha = __builtin_amdgcn_exp2f(-delta);
      const float alo = __shfl_xor(alpha, 16);
      const float aA = (lane & 16) ? alo : alpha, aB = (lane & 16) ? alpha : alo;
      lacc = lacc * (f32x4){aA, aB, aA, aB};
      o0 = o0 * alpha; o1 = o1 * alpha; negr = negr - delta; SN_ = SN_ - delta;
      asm volatile("" : "+v"(o0), "+v"(o1), "+v"(negr), "+v"(SN_), "+v"(lacc));
      ref = -negr[0];
#pragma unroll
      for (int i = 0; i < 8; ++i) PN_[i] = pk2(__builtin_amdgcn_exp2f(SC_[2*i] - delta), __builtin_amdgcn_exp2f(SC_[2*i+1] - delta));
    }
  };
  STAMP(1, 1);
  int r0 = 0, r1 = A_HST, r2 = 2 * A_HST;
  for (int st = 0; st < A_NFULL; ++st) {
    asm volatile("s_waitcnt vmcnt(4) lgkmcnt(0)\n\ts_barrier" ::: "memory");
    dma2(st + 3 < A_NST ? st + 3 : A_NST - 1, 0, r0); dma2(st + 2 < A_NST ? st + 2 : A_NST - 1, 1, A_VOFF + r2);
    iter(sa, sb, pa, pb, kf, vf, kg2, vg2, base + r1, base + A_VOFF + r0);
    iter(sb, sa, pb, pa, kg2, vg2, kf, vf, base + r1 + 4096, base + A_VOFF + r0 + 4096);
    const int t = r0; r0 = r1; r1 = r2; r2 = t;
  }
  asm volatile("s_waitcnt vmcnt(0) lgkmcnt(0)\n\ts_barrier" ::: "memory");
  if (ntail > 0) {
    iter(sa, sb, pa, pb, kf, vf, kg2, vg2, base + r1, base + A_VOFF + r0);
    if (ntail > 1) iter(sb, sa, pb, pa, kg2, vg2, kf, vf, base + r1 + 4096, base + A_VOFF + r0 + 4096);
    else {
#pragma unroll
      for (int i = 0; i < 8; ++i) pa[i] = pb[i];
#pragma unroll
      for (int i = 0; i < 4; ++i) vf[i] = vg2[i];
    }
  }
  {
    const bf16x8 p0 = __builtin_bit_cast(bf16x8, (u32x4){pa[0], pa[1], pa[2], pa[3]}), p1 = __builtin_bit_cast(bf16x8, (u32x4){pa[4], pa[5], pa[6], pa[7]});
    o0 = MFMA(vf[0], p0, o0); o1 = MFMA(vf[2], p0, o1); o0 = MFMA(vf[1], p1, o0); o1 = MFMA(vf[3], p1, o1);
    lacc = MFMA16(sel, p0, lacc); lacc = MFMA16(sel, p1, lacc);
  }
  float lsum = (lane & 16) ? lacc[1] : lacc[0];
  STAMP(1, 2);
  lds_barrier();
  if (kg > 0) {
    LAS char* cb = lds + ((kg - 1) * 4 + qi) * 9216 + lane * 16;
#pragma unroll
    for (int g = 0; g < 4; ++g) {
      *(LAS f32x4*)(cb + g * 1024) = (f32x4){o0[4 * g], o0[4 * g + 1], o0[4 * g + 2], o0[4 * g + 3]};
      *(LAS f32x4*)(cb + 4096 + g * 1024) = (f32x4){o1[4 * g], o1[4 * g + 1], o1[4 * g + 2], o1[4 * g + 3]};
    }
    *(LAS f32x2*)(lds + ((kg - 1) * 4 + qi) * 9216 + 8192 + lane * 8) = (f32x2){ref, lsum};
  }
  lds_barrier();
  if (kg == 0 && qb_raw < NBLK) {
#pragma unroll
    for (int k = 1; k < A_KG; ++k) {
      LAS char* cb = lds + ((k - 1) * 4 + qi) * 9216 + lane * 16;
      const f32x2 rl = *(LAS f32x2*)(lds + ((k - 1) * 4 + qi) * 9216 + 8192 + lane * 8);
      const float m = fmaxf(ref, rl[0]);
      const float c0 = __builtin_amdgcn_exp2f(ref - m), c1 = __builtin_amdgcn_exp2f(rl[0] - m);
      lsum = lsum * c0 + rl[1] * c1; ref = m;
#pragma unroll
      for (int g = 0; g < 4; ++g) {
        const f32x4 u0 = *(LAS f32x4*)(cb + g * 1024), u1 = *(LAS f32x4*)(cb + 4096 + g * 1024);
#pragma unroll
        for (int j = 0; j < 4; ++j) { o0[4 * g + j] = o0[4 * g + j] * c0 + u0[j] * c1; o1[4 * g + j] = o1[4 * g + j] * c0 + u1[j] * c1; }
      }
    }
    const float inv = 1.0f / lsum;
#pragma unroll
    for (int i = 0; i < 16; ++i) { o0[i] *= inv; o1[i] *= inv; }
    char* dst = ws + WS_ATTF + ((size_t)((bh >> 2) * NBLK + qb) * 16 + (bh & 3) * 4) * 1024 + lane * 16;
    *(bf16x8*)(dst) = pack8(o0, 0); *(bf16x8*)(dst + 1024) = pack8(o0, 1);
    *(bf16x8*)(dst + 2048) = pack8(o1, 0); *(bf16x8*)(dst + 3072) = pack8(o1, 1);
  }
  STAMP(1, 29);
}

__global__ __launch_bounds__(512, 2) void k3_kernel(const char* __restrict__ dw, const char* __restrict__ ws, char* __restrict__ out) {
  extern __shared__ __attribute__((aligned(16))) char smem[];
  LAS char* lds = (LAS char*)smem;
  const int lane = threadIdx.x & 63, w = __builtin_amdgcn_readfirstlane(threadIdx.x >> 6), r = lane & 31, h = lane >> 5;
  const int tb = blockIdx.x, b = tb / NBLK, t0 = (tb - b * NBLK) * 32;
  STAMP(2, 0);
  Stream st;
  const int hc = r & 3, ht = (hc < 2) ? t0 - 2 + hc : t0 + 30 + hc;
  const bool hvalid = ht >= 0 && ht < TSEQ;
  const int hg = b * TSEQ + (ht < 0 ? 0 : (ht >= TSEQ ? TSEQ - 1 : ht));
  const unsigned hoff = (unsigned)(hg >> 5) * 16384 + ((hg & 31) + 32 * h) * 16 + w * 2048;
  const unsigned moff = (unsigned)tb * 16384 + lane * 16 + w * 2048;
  {
    const unsigned ly = (unsigned)(uintptr_t)(lds + L_Y + w * 2048);
    asm volatile("s_mov_b32 m0, %3\n\ts_nop 0\n\tglobal_load_lds_dwordx4 %0, %2\n\tglobal_load_lds_dwordx4 %0, %2 offset:1024\n\t"
                 "s_mov_b32 m0, %4\n\ts_nop 0\n\tglobal_load_lds_dwordx4 %1, %2\n\tglobal_load_lds_dwordx4 %1, %2 offset:1024"
                 :: "v"(moff), "v"(hoff), "s"(ws + WS_ATTF), "s"(ly), "s"(ly + 16384));
  }
  u32x4 xr[2][2];
#pragma unroll
  for (int f = 0; f < 2; ++f) {
    xr[0][f] = *(const u32x4*)(ws + WS_X1F + moff + f * 1024);
    xr[1][f] = *(const u32x4*)(ws + WS_X1F + hoff + f * 1024);
  }
  const u32x4 dwv = *(const u32x4*)(dw + (threadIdx.x < 160 ? threadIdx.x : 159) * 16);
  asm volatile("s_barrier" ::: "memory");
  SB();
  st.start(ws + WS_WT3 + (size_t)w * NP3 * 16384 + lane * 16);
  SB();
  if (threadIdx.x < 160) {
    *(LAS f32x4*)(lds + L_DW + threadIdx.x * 32) = (f32x4){bflo(dwv[0]), bfhi(dwv[0]), bflo(dwv[1]), bfhi(dwv[1])};
    *(LAS f32x4*)(lds + L_DW + threadIdx.x * 32 + 16) = (f32x4){bflo(dwv[2]), bfhi(dwv[2]), bflo(dwv[3]), bfhi(dwv[3])};
  }
  asm volatile("s_waitcnt vmcnt(32)" ::: "memory");
  lds_barrier();
  STAMP(2, 1);
  f32x16 xv[2];
#pragma unroll
  for (int nb = 0; nb < 2; ++nb) { unpack8(xr[nb][0], xv[nb], 0); unpack8(xr[nb][1], xv[nb], 1); }
  gemm_k256<2, false, 0, NP3>(st, xv, lds + L_Y + lane * 16, NoWork{});
  STAMP(2, 2);
  ln_to_frags<2>(xv, lds, w, lane);
  STAMP(2, 3);
  {
    f32x16 a[2], g[2];
#pragma unroll
    for (int nb = 0; nb < 2; ++nb) { a[nb] = f32x16{}; g[nb] = f32x16{}; }
    gemm_k256<2, false, 1, NP3>(st, a, lds + L_X + lane * 16, NoWork{});
    gemm_k256<2, false, 2, NP3>(st, g, lds + L_X + lane * 16, NoWork{});
    STAMP(2, 4);
    LAS char* G = lds + L_Y + w * GSZ;
    lds_barrier();
#pragma unroll
    for (int nb = 0; nb < 2; ++nb) {
      const int row = nb == 0 ? r + 2 : (hc < 2 ? hc : 32 + hc);
      const bool wr = nb == 0 || r < 4;
      const float keep = (nb == 0 || hvalid) ? 1.0f : 0.0f;
#pragma unroll
      for (int q = 0; q < 4; ++q) {
        f32x4 e;
#pragma unroll
        for (int j = 0; j < 4; ++j) e[j] = keep * a[nb][4 * q + j] * sigmoidf_(g[nb][4 * q + j]);
        if (wr) *(LAS f32x4*)(G + row * GROW + (8 * q + 4 * h) * 4) = e;
      }
    }
    asm volatile("s_waitcnt lgkmcnt(0)" ::: "memory");
    f32x16 c;
#pragma unroll
    for (int q = 0; q < 4; ++q) {
      f32x4 e = {0.f, 0.f, 0.f, 0.f};
#pragma unroll
      for (int i = 0; i < 5; ++i)
        e += *(LAS f32x4*)(G + (r + i) * GROW + (8 * q + 4 * h) * 4) * *(LAS f32x4*)(lds + L_DW + i * 1024 + (w * 32 + 8 * q + 4 * h) * 4);
#pragma unroll
      for (int j = 0; j < 4; ++j) c[4 * q + j] = siluf_(e[j]);
    }
#pragma unroll
    for (int s = 0; s < 2; ++s) *(LAS bf16x8*)(lds + L_X + (2 * w + s) * 1024 + lane * 16) = pack8(c, s);
    lds_barrier();
  }
  STAMP(2, 5);
  f32x16 x3[1];
  x3[0] = xv[0];
  gemm_k256<1, false, 3, NP3>(st, x3, lds + L_X + lane * 16, NoWork{});
  STAMP(2, 6);
  ln_to_frags<1>(x3, lds, w, lane);
  STAMP(2, 7);
  f32x16 acc[1];
  ff_block<4, NP3>(st, lds, w, lane, acc);
  x3[0] += 0.5f * acc[0];
  STAMP(2, 8);
  float mean[1], rstd[1];
  ln_stats<1>(x3, lds, w, lane, mean, rstd);
  char* orow = out + (size_t)(tb * 32 + r) * 512 + (w * 32 + 4 * h) * 2;
  const f32x16 n = (x3[0] - mean[0]) * rstd[0];
#pragma unroll
  for (int q = 0; q < 4; ++q) *(u32x2*)(orow + q * 16) = (u32x2){pk2(n[4 * q], n[4 * q + 1]), pk2(n[4 * q + 2], n[4 * q + 3])};
  STAMP(2, 29);
}

extern "C" void kernel_launch(void* const* d_in, const int* in_sizes, int n_in, void* d_out, int out_size, void* d_ws, size_t ws_size,
                              hipStream_t stream) {
  const char* x = (const char*)d_in[0];
  const char* w1a = (const char*)d_in[1];
  const char* w1b = (const char*)d_in[2];
  const char* wqkv = (const char*)d_in[3];
  const char* wo = (const char*)d_in[4];
  const char* pw1 = (const char*)d_in[5];
  const char* dw = (const char*)d_in[6];
  const char* pw2 = (const char*)d_in[7];
  const char* w2a = (const char*)d_in[8];
  const char* w2b = (const char*)d_in[9];
  char* ws = (char*)d_ws;
  (void)hipFuncSetAttribute((const void*)k1_kernel, hipFuncAttributeMaxDynamicSharedMemorySize, L_TOTAL);
  (void)hipFuncSetAttribute((const void*)attn_kernel, hipFuncAttributeMaxDynamicSharedMemorySize, A_LDS);
  (void)hipFuncSetAttribute((const void*)k3_kernel, hipFuncAttributeMaxDynamicSharedMemorySize, L_TOTAL);
  WPtrs wp{w1a, w1b, wqkv, wo, pw1, pw2, w2a, w2b};
  prep_kernel<<<(NP1 + NP3) * 8, 256, 0, stream>>>(wp, ws);
  k1_kernel<<<NTB, 512, L_TOTAL, stream>>>(x, ws);
  attn_kernel<<<256, 256 * A_KG, A_LDS, stream>>>(ws);
  k3_kernel<<<NTB, 512, L_TOTAL, stream>>>(dw, ws, (char*)d_out);
}
```

```cpp
#include <hip/hip_runtime.h>
#include <hip/hip_bf16.h>
#include <stdint.h>

typedef short bf16x8 __attribute__((ext_vector_type(8)));
typedef short s16x4 __attribute__((ext_vector_type(4)));
typedef float f32x16 __attribute__((ext_vector_type(16)));
typedef float f32x2 __attribute__((ext_vector_type(2)));
typedef __bf16 bf16x2_t __attribute__((ext_vector_type(2)));
typedef unsigned u32x4 __attribute__((ext_vector_type(4)));
typedef unsigned u32x2 __attribute__((ext_vector_type(2)));
#define LAS __attribute__((address_space(3)))
#define DI __device__ __forceinline__
#define MFMA(a, b, c) __builtin_amdgcn_mfma_f32_32x32x16_bf16((a), (b), (c), 0, 0, 0)
#define MFMA16(a, b, c) __builtin_amdgcn_mfma_f32_16x16x32_bf16((a), (b), (c), 0, 0, 0)
#define SB() __builtin_amdgcn_sched_barrier(0)
#define STAMP(k, i) do {} while (0)
typedef float f32x4 __attribute__((ext_vector_type(4)));

constexpr int TSEQ = 4000, NBLK = 125, NTB = 250;
constexpr size_t WS_X1F = 0;
constexpr size_t WS_QF = 4u << 20;
constexpr size_t WS_KVF = 8u << 20;
constexpr size_t WS_ATTF = 17u << 20;
constexpr float LN_EPS = 1e-5f;
constexpr float LOG2E = 1.4426950408889634f;
constexpr float QSCALE = 0.125f * LOG2E;

DI unsigned pk2(float a, float b) { f32x2 v = {a, b}; return __builtin_bit_cast(unsigned, __builtin_convertvector(v, bf16x2_t)); }
DI float bflo(unsigned u) { return __uint_as_float(u << 16); }
DI float bfhi(unsigned u) { return __uint_as_float(u & 0xffff0000u); }
DI float rbf(float a) { return bflo(pk2(a, a)); }
DI bf16x8 pack8(const f32x16& v, int s) {
  u32x4 p = {pk2(v[8 * s], v[8 * s + 1]), pk2(v[8 * s + 2], v[8 * s + 3]), pk2(v[8 * s + 4], v[8 * s + 5]), pk2(v[8 * s + 6], v[8 * s + 7])};
  return __builtin_bit_cast(bf16x8, p);
}
DI void unpack8(u32x4 p, f32x16& v, int s) {
#pragma unroll
  for (int i = 0; i < 4; ++i) { v[8 * s + 2 * i] = bflo(p[i]); v[8 * s + 2 * i + 1] = bfhi(p[i]); }
}
DI float halfsum(float x) {
  auto r = __builtin_amdgcn_permlane32_swap(__float_as_uint(x), __float_as_uint(x), false, false);
  return __uint_as_float(r[0]) + __uint_as_float(r[1]);
}
DI float halfmax(float x) {
  auto r = __builtin_amdgcn_permlane32_swap(__float_as_uint(x), __float_as_uint(x), false, false);
  return fmaxf(__uint_as_float(r[0]), __uint_as_float(r[1]));
}
DI float sigmoidf_(float x) { return __builtin_amdgcn_rcpf(1.0f + __builtin_amdgcn_exp2f(-LOG2E * x)); }
DI float siluf_(float x) { return x * sigmoidf_(x); }
DI void lds_barrier() { asm volatile("s_waitcnt lgkmcnt(0)\n\ts_barrier" ::: "memory"); }
typedef short v4i16_t __attribute__((ext_vector_type(4)));
DI s16x4 trread(LAS const char* p) { return __builtin_bit_cast(s16x4, __builtin_amdgcn_ds_read_tr16_b64_v4i16((LAS v4i16_t*)p)); }
DI bf16x8 ldsfrag(LAS const char* p) { return *(LAS const bf16x8*)p; }

constexpr size_t WS_WT1 = 24u << 20, WS_WT3 = 26u << 20;
constexpr int NP1 = 11, NP3 = 12;
struct WPtrs { const char *w1a, *w1b, *wqkv, *wo, *pw1, *pw2, *w2a, *w2b; };
DI void ff_panel(const char* wa, const char* wb, int p, int w2, const char*& base, unsigned& ld) {
  const bool isb = (p == 2 || p == 4 || p >= 6);
  const int c = p < 2 ? p : (p == 3 ? 2 : (p == 5 ? 3 : (p == 2 ? 0 : (p == 4 ? 1 : p - 4))));
  if (isb) { base = wb + (size_t)c * 256 * 512 + w2 * 128; ld = 512; } else { base = wa + (size_t)(w2 + 4 * c) * 128; ld = 2048; }
}
__global__ __launch_bounds__(256) void prep_kernel(WPtrs wp, char* __restrict__ ws) {
  __shared__ __attribute__((aligned(16))) char sm[4 * 4096];
  const int lane = threadIdx.x & 63, wv = __builtin_amdgcn_readfirstlane(threadIdx.x >> 6);
  const int g = blockIdx.x * 4 + wv;
  const bool k3 = g >= 4 * NP1 * 8;
  const int gg = k3 ? g - 4 * NP1 * 8 : g, np = k3 ? NP3 : NP1;
  const int w2 = gg / (np * 8), p = (gg >> 3) % np, sl = gg & 7;
  const char* base; unsigned ld;
  if (!k3) {
    if (p < 8) ff_panel(wp.w1a, wp.w1b, p, w2, base, ld);
    else { base = wp.wqkv + (p - 8) * 512 + w2 * 128; ld = 1536; }
  } else {
    if (p == 0) { base = wp.wo + w2 * 128; ld = 512; }
    else if (p < 3) { base = wp.pw1 + (p - 1) * 512 + w2 * 128; ld = 1024; }
    else if (p == 3) { base = wp.pw2 + w2 * 128; ld = 512; }
    else ff_panel(wp.w2a, wp.w2b, p - 4, w2, base, ld);
  }
  LAS char* my = (LAS char*)sm + wv * 4096;
  const char* src = base + (size_t)(sl * 32 + (lane >> 3)) * ld + (((lane & 7) ^ (((lane >> 4) & 1) << 2)) << 4);
  u32x4 t[4];
#pragma unroll
  for (int i = 0; i < 4; ++i) t[i] = *(const u32x4*)(src + (size_t)i * 8 * ld);
#pragma unroll
  for (int i = 0; i < 4; ++i) *(LAS u32x4*)(my + i * 1024 + lane * 16) = t[i];
  asm volatile("s_waitcnt lgkmcnt(0)" ::: "memory");
  const int h = lane >> 5, cb = (lane >> 4) & 1, q = (lane & 15) >> 2, pp = lane & 3;
  char* dst = ws + (k3 ? WS_WT3 : WS_WT1) + (size_t)((2 * w2) * np + p) * 16384 + sl * 2048 + lane * 16;
#pragma unroll
  for (int i = 0; i < 4; ++i) {
    LAS const char* rp = my + (4 * h + q) * 128 + (((i & 1) ^ (q >> 1)) * 64) + cb * 32 + pp * 8 + (i >> 1) * 2048;
    s16x4 l = trread(rp), hh = trread(rp + 1024);
    *(bf16x8*)(dst + (size_t)(i & 1) * np * 16384 + (i >> 1) * 1024) = __builtin_shufflevector(l, hh, 0, 1, 2, 3, 4, 5, 6, 7);
  }
}

struct Stream {
  const char* p;
  bf16x8 wf[2][16];
  DI void start(const char* base) {
    p = base;
#pragma unroll
    for (int i = 0; i < 32; ++i) wf[i >> 4][i & 15] = *(const bf16x8*)(p + i * 1024);
  }
};
struct NoWork { DI void init() {} DI void step(int) {} };
struct SiluWork {
  LAS char* p;
  u32x2 raw; float x[4], e[4];
  DI void init() { raw = *(LAS u32x2*)p; }
  DI void step(int ks) {
    const int g = ks >> 2, i = ks & 3;
    if (i == 0) {
      x[0] = bflo(raw[0]); x[1] = bfhi(raw[0]); x[2] = bflo(raw[1]); x[3] = bfhi(raw[1]);
#pragma unroll
      for (int j = 0; j < 4; ++j) e[j] = -LOG2E * x[j];
      asm volatile("" : "+v"(e[0]), "+v"(e[1]), "+v"(e[2]), "+v"(e[3]), "+v"(x[0]), "+v"(x[1]), "+v"(x[2]), "+v"(x[3]));
      if (g < 3) raw = *(LAS u32x2*)(p + ((g + 1) >> 1) * 1024 + ((g + 1) & 1) * 8);
    } else if (i == 1) {
#pragma unroll
      for (int j = 0; j < 4; ++j) e[j] = __builtin_amdgcn_exp2f(e[j]);
      asm volatile("" : "+v"(e[0]), "+v"(e[1]), "+v"(e[2]), "+v"(e[3]));
    } else if (i == 2) {
#pragma unroll
      for (int j = 0; j < 4; ++j) e[j] = __builtin_amdgcn_rcpf(1.0f + e[j]);
      asm volatile("" : "+v"(e[0]), "+v"(e[1]), "+v"(e[2]), "+v"(e[3]));
    } else {
      *(LAS u32x2*)(p + (g >> 1) * 1024 + (g & 1) * 8) = (u32x2){pk2(x[0] * e[0], x[1] * e[1]), pk2(x[2] * e[2], x[3] * e[3])};
    }
  }
};
template <int NB, bool SWAP, int P, int NP, class W>
DI void gemm_k256(Stream& st, f32x16 (&acc)[NB], LAS const char* bl, W work) {
  constexpr bool REFILL = P + 2 < NP;
  bf16x8 bq[3][NB];
#pragma unroll
  for (int nb = 0; nb < NB; ++nb) { bq[0][nb] = ldsfrag(bl + nb * 16384); bq[1][nb] = ldsfrag(bl + nb * 16384 + 1024); }
  work.init();
#pragma unroll
  for (int ks = 0; ks < 16; ++ks) {
    SB();
#pragma unroll
    for (int nb = 0; nb < NB; ++nb)
      acc[nb] = SWAP ? MFMA(bq[ks % 3][nb], st.wf[P & 1][ks], acc[nb]) : MFMA(st.wf[P & 1][ks], bq[ks % 3][nb], acc[nb]);
    if (ks < 14) {
#pragma unroll
      for (int nb = 0; nb < NB; ++nb) bq[(ks + 2) % 3][nb] = ldsfrag(bl + nb * 16384 + (ks + 2) * 1024);
    }
    if (REFILL && ks > 0) st.wf[P & 1][ks - 1] = *(const bf16x8*)(st.p + (P + 2) * 16384 + (ks - 1) * 1024);
    work.step(ks);
  }
  SB();
  if (REFILL) st.wf[P & 1][15] = *(const bf16x8*)(st.p + (P + 2) * 16384 + 15 * 1024);
}

constexpr int L_X = 0, L_Y = L_X + 16384, L_Z = L_Y + 16384;
constexpr int L_STAT = L_Z + 16384 + 9216;
constexpr int GROW = 144, GSZ = 36 * GROW;
constexpr int L_DW = L_STAT + 4096;
constexpr int L_TOTAL = L_DW + 5120;

DI float sum16(const f32x16& t) {
  return (((t[0] + t[1]) + (t[2] + t[3])) + ((t[4] + t[5]) + (t[6] + t[7]))) + (((t[8] + t[9]) + (t[10] + t[11])) + ((t[12] + t[13]) + (t[14] + t[15])));
}
template <int NB>
DI void ln_stats(const f32x16 (&v)[NB], LAS char* lds, int w, int lane, float (&mean)[NB], float (&rstd)[NB]) {
  const int r = lane & 31;
#pragma unroll
  for (int nb = 0; nb < NB; ++nb) {
    const float mw = halfsum(sum16(v[nb])) * (1.0f / 32.0f);
    const f32x16 d = v[nb] - mw;
    const float m2 = halfsum(sum16(d * d));
    if (lane < 32) *(LAS f32x2*)(lds + L_STAT + ((nb * 32 + r) * 8 + w) * 8) = (f32x2){mw, m2};
  }
  lds_barrier();
#pragma unroll
  for (int nb = 0; nb < NB; ++nb) {
    f32x4 s[4];
#pragma unroll
    for (int i = 0; i < 4; ++i) s[i] = *(LAS f32x4*)(lds + L_STAT + (nb * 32 + r) * 64 + i * 16);
    const float m = 0.125f * (((s[0][0] + s[0][2]) + (s[1][0] + s[1][2])) + ((s[2][0] + s[2][2]) + (s[3][0] + s[3][2])));
    float M2 = 0.f;
#pragma unroll
    for (int i = 0; i < 4; ++i) { const float e0 = s[i][0] - m, e1 = s[i][2] - m; M2 += s[i][1] + s[i][3] + 32.0f * (e0 * e0 + e1 * e1); }
    mean[nb] = m; rstd[nb] = __builtin_amdgcn_rsqf(M2 * (1.0f / 256.0f) + LN_EPS);
  }
}
template <int NB>
DI void ln_to_frags(const f32x16 (&v)[NB], LAS char* lds, int w, int lane) {
  float mean[NB], rstd[NB];
  ln_stats<NB>(v, lds, w, lane, mean, rstd);
  LAS char* fb = lds + L_X + w * 2048 + lane * 16;
#pragma unroll
  for (int nb = 0; nb < NB; ++nb) {
    const f32x16 n = (v[nb] - mean[nb]) * rstd[nb];
#pragma unroll
    for (int s = 0; s < 2; ++s) *(LAS bf16x8*)(fb + nb * 16384 + s * 1024) = pack8(n, s);
  }
  lds_barrier();
}

template <int P0, int NP>
DI void ff_block(Stream& st, LAS char* lds, int w, int lane, f32x16 (&out)[1]) {
  out[0] = f32x16{};
  LAS char* xb = lds + L_X + lane * 16;
  LAS char* yb = lds + L_Y + lane * 16;
  LAS char* zb = lds + L_Z + lane * 16;
  auto put = [&](LAS char* dst, const f32x16& hv) { *(LAS bf16x8*)(dst + w * 2048) = pack8(hv, 0); *(LAS bf16x8*)(dst + w * 2048 + 1024) = pack8(hv, 1); };
  f32x16 hn[1];
  hn[0] = f32x16{}; gemm_k256<1, false, P0 + 0, NP>(st, hn, xb, NoWork{});
  put(yb, hn[0]);
  hn[0] = f32x16{}; gemm_k256<1, false, P0 + 1, NP>(st, hn, xb, SiluWork{yb + w * 2048});
  lds_barrier(); put(zb, hn[0]);
  gemm_k256<1, false, P0 + 2, NP>(st, out, yb, NoWork{});
  hn[0] = f32x16{}; gemm_k256<1, false, P0 + 3, NP>(st, hn, xb, SiluWork{zb + w * 2048});
  lds_barrier(); put(yb, hn[0]);
  gemm_k256<1, false, P0 + 4, NP>(st, out, zb, NoWork{});
  hn[0] = f32x16{}; gemm_k256<1, false, P0 + 5, NP>(st, hn, xb, SiluWork{yb + w * 2048});
  lds_barrier(); put(zb, hn[0]);
  gemm_k256<1, false, P0 + 6, NP>(st, out, yb, SiluWork{zb + w * 2048});
  lds_barrier();
  gemm_k256<1, false, P0 + 7, NP>(st, out, zb, NoWork{});
}

DI void l2_warm(const char* p, int kb_total, int idx, int lane, u32x4& t) {
  const int per = (kb_total + 247) / 248;
  int b = idx * per, e = b + per; if (e > kb_total) e = kb_total;
  for (int i = b; i < e; ++i)
    asm volatile("global_load_dwordx4 %0, %1, %2" : "+v"(t) : "v"(lane * 16 + i * 1024), "s"(p));
}

__global__ __launch_bounds__(512, 2) void k1_kernel(const char* __restrict__ x, char* __restrict__ ws) {
  extern __shared__ __attribute__((aligned(16))) char smem[];
  LAS char* lds = (LAS char*)smem;
  const int lane = threadIdx.x & 63, w = __builtin_amdgcn_readfirstlane(threadIdx.x >> 6), r = lane & 31, h = lane >> 5;
  const int tb = blockIdx.x;
  STAMP(0, 0);
  f32x16 xv[1];
  Stream st;
  u32x4 wt = {0u, 0u, 0u, 0u};
  {
    const char* xrow = x + (size_t)(tb * 32 + r) * 512 + (w * 32 + 4 * h) * 2;
    u32x2 u[4];
#pragma unroll
    for (int g = 0; g < 4; ++g) u[g] = *(const u32x2*)(xrow + g * 16);
    asm volatile("s_barrier" ::: "memory");
    SB();
    st.start(ws + WS_WT1 + (size_t)w * NP1 * 16384 + lane * 16);
    SB();
    if ((tb >> 3) < 31) l2_warm(ws + WS_WT1, 8 * NP1 * 16, (tb >> 3) * 8 + w, lane, wt);
#pragma unroll
    for (int g = 0; g < 4; ++g) {
      xv[0][4 * g] = bflo(u[g][0]); xv[0][4 * g + 1] = bfhi(u[g][0]); xv[0][4 * g + 2] = bflo(u[g][1]); xv[0][4 * g + 3] = bfhi(u[g][1]);
    }
  }
  STAMP(0, 1);
  ln_to_frags<1>(xv, lds, w, lane);
  STAMP(0, 2);
  f32x16 acc[1];
  ff_block<0, NP1>(st, lds, w, lane, acc);
  asm volatile("" :: "v"(wt));
  STAMP(0, 3);
#pragma unroll
  for (int i = 0; i < 16; ++i) xv[0][i] = rbf(xv[0][i] + 0.5f * acc[0][i]);
#pragma unroll
  for (int s = 0; s < 2; ++s) *(bf16x8*)(ws + WS_X1F + ((size_t)tb * 16 + 2 * w + s) * 1024 + lane * 16) = pack8(xv[0], s);
  ln_to_frags<1>(xv, lds, w, lane);
  STAMP(0, 4);
  const int b = tb / NBLK, blk = tb - b * NBLK, bh = b * 4 + (w >> 1);
  char* kvdst = ws + WS_KVF + (size_t)(bh * NBLK + blk) * 8192 + (w & 1) * 2048 + lane * 16;
  {
    f32x16 a[1]; a[0] = f32x16{};
    gemm_k256<1, false, 8, NP1>(st, a, lds + L_X + lane * 16, NoWork{});
    a[0] = a[0] * QSCALE;
    char* dst = ws + WS_QF + (size_t)(bh * NBLK + blk) * 4096 + (w & 1) * 2048 + lane * 16;
    *(bf16x8*)(dst) = pack8(a[0], 0); *(bf16x8*)(dst + 1024) = pack8(a[0], 1);
  }
  {
    f32x16 a[1]; a[0] = f32x16{};
    gemm_k256<1, false, 9, NP1>(st, a, lds + L_X + lane * 16, NoWork{});
    *(bf16x8*)(kvdst) = pack8(a[0], 0); *(bf16x8*)(kvdst + 1024) = pack8(a[0], 1);
  }
  STAMP(0, 5);
  {
    f32x16 a[1]; a[0] = f32x16{};
    gemm_k256<1, true, 10, NP1>(st, a, lds + L_X + lane * 16, NoWork{});
    *(bf16x8*)(kvdst + 4096) = pack8(a[0], 0); *(bf16x8*)(kvdst + 5120) = pack8(a[0], 1);
  }
  STAMP(0, 29);
}

constexpr int A_KG = 2, A_SBLK = A_KG * 2, A_HST = A_SBLK * 4096, A_NST = (NBLK + A_SBLK - 1) / A_SBLK, A_NFULL = NBLK / A_SBLK;
constexpr int A_VOFF = 3 * A_HST, A_LDS = 6 * A_HST;
__global__ __launch_bounds__(256 * A_KG, A_KG) void attn_kernel(char* __restrict__ ws) {
  extern __shared__ __attribute__((aligned(16))) char smem[];
  LAS char* lds = (LAS char*)smem;
  const int lane = threadIdx.x & 63, w = __builtin_amdgcn_readfirstlane(threadIdx.x >> 6);
  const int bh = blockIdx.x & 7, qg = blockIdx.x >> 3, qi = w & 3, kg = w >> 2;
  const int qb_raw = qg * 4 + qi, qb = qb_raw < NBLK ? qb_raw : NBLK - 1;
  STAMP(1, 0);
  const char* kv = ws + WS_KVF + (size_t)bh * NBLK * 8192;
  const unsigned lds0 = (unsigned)(uintptr_t)lds;
  const unsigned voff = (w >> 1) * 8192 + (w & 1) * 2048 + lane * 16;
  auto dma2 = [&](int st, int isv, unsigned ldsoff) {
    asm volatile("s_mov_b32 m0, %2\n\ts_nop 0\n\tglobal_load_lds_dwordx4 %0, %1\n\tglobal_load_lds_dwordx4 %0, %1 offset:1024"
                 :: "v"(voff), "s"(kv + (size_t)st * (2 * A_HST) + isv * 4096), "s"(lds0 + ldsoff + w * 2048));
  };
  dma2(0, 0, 0); dma2(1, 0, A_HST); dma2(0, 1, A_VOFF); dma2(2, 0, 2 * A_HST); dma2(1, 1, A_VOFF + A_HST);
  bf16x8 qf[4];
#pragma unroll
  for (int f = 0; f < 4; ++f) qf[f] = *(const bf16x8*)(ws + WS_QF + ((size_t)(bh * NBLK + qb) * 4 + f) * 1024 + lane * 16);
#pragma unroll
  for (int f = 0; f < 4; ++f) asm volatile("" : "+v"(qf[f]));
  u32x4 wt = {0u, 0u, 0u, 0u};
  const int rem = NBLK - A_NFULL * A_SBLK - kg * 2;
  const int ntail = rem < 0 ? 0 : (rem > 2 ? 2 : rem);
  LAS const char* base = lds + kg * 8192 + lane * 16;
  f32x16 o0 = f32x16{}, o1 = f32x16{}, negr, sa, sb;
  f32x4 lacc = {0.f, 0.f, 0.f, 0.f};
  bf16x8 sel;
  { const short one = (((lane >> 4) ^ lane) & 1) ? (short)0 : (short)0x3f80;
#pragma unroll
    for (int j = 0; j < 8; ++j) sel[j] = one; }
  bf16x8 kf[4], vf[4], kg2[4], vg2[4]; unsigned pa[8], pb[8];
#pragma unroll
  for (int i = 0; i < 8; ++i) pa[i] = 0u;
  float ref;
  asm volatile("s_waitcnt vmcnt(0)\n\ts_barrier" : "+v"(wt) :: "memory");
  {
#pragma unroll
    for (int f = 0; f < 4; ++f) kf[f] = ldsfrag(base + f * 1024);
    f32x16 z = f32x16{};
    sa = MFMA(kf[0], qf[0], z); sa = MFMA(kf[1], qf[1], sa); sa = MFMA(kf[2], qf[2], sa); sa = MFMA(kf[3], qf[3], sa);
    float mx = sa[0];
#pragma unroll
    for (int i = 1; i < 16; ++i) mx = fmaxf(mx, sa[i]);
    ref = halfmax(mx) + 7.0f;
#pragma unroll
    for (int i = 0; i < 16; ++i) { sa[i] -= ref; negr[i] = -ref; }
#pragma unroll
    for (int f = 0; f < 4; ++f) { kf[f] = ldsfrag(base + 4096 + f * 1024); vf[f] = bf16x8{}; }
  }
#define SLOT3(MF, RF, i) do { MF; RF; e[2*(i)] = __builtin_amdgcn_exp2f(SC_[2*(i)]); e[2*(i)+1] = __builtin_amdgcn_exp2f(SC_[2*(i)+1]); \
    if ((i) > 0) PN_[(i)-1] = pk2(e[2*(i)-2], e[2*(i)-1]); SB(); } while (0)
  auto iter = [&](f32x16& SC_, f32x16& SN_, unsigned (&PP_)[8], unsigned (&PN_)[8], bf16x8 (&KC)[4], bf16x8 (&VC)[4], bf16x8 (&KN)[4], bf16x8 (&VN)[4], LAS const char* kn, LAS const char* vc) {
    float e[16];
    const bf16x8 p0 = __builtin_bit_cast(bf16x8, (u32x4){PP_[0], PP_[1], PP_[2], PP_[3]}), p1 = __builtin_bit_cast(bf16x8, (u32x4){PP_[4], PP_[5], PP_[6], PP_[7]});
    SB();
    SLOT3(SN_ = MFMA(KC[0], qf[0], negr), KN[0] = ldsfrag(kn), 0);
    SLOT3(o0 = MFMA(VC[0], p0, o0), VN[0] = ldsfrag(vc), 1);
    SLOT3(SN_ = MFMA(KC[1], qf[1], SN_), KN[1] = ldsfrag(kn + 1024), 2);
    SLOT3(o1 = MFMA(VC[2], p0, o1); lacc = MFMA16(sel, p0, lacc), VN[2] = ldsfrag(vc + 2048), 3);
    SLOT3(SN_ = MFMA(KC[2], qf[2], SN_), KN[2] = ldsfrag(kn + 2048), 4);
    SLOT3(o0 = MFMA(VC[1], p1, o0), VN[1] = ldsfrag(vc + 1024), 5);
    SLOT3(SN_ = MFMA(KC[3], qf[3], SN_), KN[3] = ldsfrag(kn + 3072), 6);
    SLOT3(o1 = MFMA(VC[3], p1, o1); lacc = MFMA16(sel, p1, lacc), VN[3] = ldsfrag(vc + 3072), 7);
    PN_[7] = pk2(e[14], e[15]);
    const unsigned orall = (PN_[0] | PN_[1] | PN_[2]) | (PN_[3] | PN_[4] | PN_[5]) | (PN_[6] | PN_[7]);
    if (__builtin_expect(__any((orall & 0x40004000u) != 0u), 0)) {
      float mx = SC_[0];
#pragma unroll
      for (int i = 1; i < 16; ++i) mx = fmaxf(mx, SC_[i]);
      mx = halfmax(mx);
      const float delta = (mx >= 0.5f) ? mx + 7.0f : 0.0f;
      const float alpha = __builtin_amdgcn_exp2f(-delta);
      const float alo = __shfl_xor(alpha, 16);
      const float aA = (lane & 16) ? alo : alpha, aB = (lane & 16) ? alpha : alo;
      lacc = lacc * (f32x4){aA, aB, aA, aB};
      o0 = o0 * alpha; o1 = o1 * alpha; negr = negr - delta; SN_ = SN_ - delta;
      asm volatile("" : "+v"(o0), "+v"(o1), "+v"(negr), "+v"(SN_), "+v"(lacc));
      ref = -negr[0];
#pragma unroll
      for (int i = 0; i < 8; ++i) PN_[i] = pk2(__builtin_amdgcn_exp2f(SC_[2*i] - delta), __builtin_amdgcn_exp2f(SC_[2*i+1] - delta));
    }
  };
  STAMP(1, 1);
  int r0 = 0, r1 = A_HST, r2 = 2 * A_HST;
  for (int st = 0; st < A_NFULL; ++st) {
    asm volatile("s_waitcnt vmcnt(4) lgkmcnt(0)\n\ts_barrier" ::: "memory");
    dma2(st + 3 < A_NST ? st + 3 : A_NST - 1, 0, r0); dma2(st + 2 < A_NST ? st + 2 : A_NST - 1, 1, A_VOFF + r2);
    iter(sa, sb, pa, pb, kf, vf, kg2, vg2, base + r1, base + A_VOFF + r0);
    iter(sb, sa, pb, pa, kg2, vg2, kf, vf, base + r1 + 4096, base + A_VOFF + r0 + 4096);
    const int t = r0; r0 = r1; r1 = r2; r2 = t;
  }
  asm volatile("s_waitcnt vmcnt(0) lgkmcnt(0)\n\ts_barrier" ::: "memory");
  if (ntail > 0) {
    iter(sa, sb, pa, pb, kf, vf, kg2, vg2, base + r1, base + A_VOFF + r0);
    if (ntail > 1) iter(sb, sa, pb, pa, kg2, vg2, kf, vf, base + r1 + 4096, base + A_VOFF + r0 + 4096);
    else {
#pragma unroll
      for (int i = 0; i < 8; ++i) pa[i] = pb[i];
#pragma unroll
      for (int i = 0; i < 4; ++i) vf[i] = vg2[i];
    }
  }
  {
    const bf16x8 p0 = __builtin_bit_cast(bf16x8, (u32x4){pa[0], pa[1], pa[2], pa[3]}), p1 = __builtin_bit_cast(bf16x8, (u32x4){pa[4], pa[5], pa[6], pa[7]});
    o0 = MFMA(vf[0], p0, o0); o1 = MFMA(vf[2], p0, o1); o0 = MFMA(vf[1], p1, o0); o1 = MFMA(vf[3], p1, o1);
    lacc = MFMA16(sel, p0, lacc); lacc = MFMA16(sel, p1, lacc);
  }
  float lsum = (lane & 16) ? lacc[1] : lacc[0];
  STAMP(1, 2);
  lds_barrier();
  if (kg > 0) {
    LAS char* cb = lds + ((kg - 1) * 4 + qi) * 9216 + lane * 16;
#pragma unroll
    for (int g = 0; g < 4; ++g) {
      *(LAS f32x4*)(cb + g * 1024) = (f32x4){o0[4 * g], o0[4 * g + 1], o0[4 * g + 2], o0[4 * g + 3]};
      *(LAS f32x4*)(cb + 4096 + g * 1024) = (f32x4){o1[4 * g], o1[4 * g + 1], o1[4 * g + 2], o1[4 * g + 3]};
    }
    *(LAS f32x2*)(lds + ((kg - 1) * 4 + qi) * 9216 + 8192 + lane * 8) = (f32x2){ref, lsum};
  }
  lds_barrier();
  if (kg == 0 && qb_raw < NBLK) {
#pragma unroll
    for (int k = 1; k < A_KG; ++k) {
      LAS char* cb = lds + ((k - 1) * 4 + qi) * 9216 + lane * 16;
      const f32x2 rl = *(LAS f32x2*)(lds + ((k - 1) * 4 + qi) * 9216 + 8192 + lane * 8);
      const float m = fmaxf(ref, rl[0]);
      const float c0 = __builtin_amdgcn_exp2f(ref - m), c1 = __builtin_amdgcn_exp2f(rl[0] - m);
      lsum = lsum * c0 + rl[1] * c1; ref = m;
#pragma unroll
      for (int g = 0; g < 4; ++g) {
        const f32x4 u0 = *(LAS f32x4*)(cb + g * 1024), u1 = *(LAS f32x4*)(cb + 4096 + g * 1024);
#pragma unroll
        for (int j = 0; j < 4; ++j) { o0[4 * g + j] = o0[4 * g + j] * c0 + u0[j] * c1; o1[4 * g + j] = o1[4 * g + j] * c0 + u1[j] * c1; }
      }
    }
    const float inv = 1.0f / lsum;
#pragma unroll
    for (int i = 0; i < 16; ++i) { o0[i] *= inv; o1[i] *= inv; }
    char* dst = ws + WS_ATTF + ((size_t)((bh >> 2) * NBLK + qb) * 16 + (bh & 3) * 4) * 1024 + lane * 16;
    *(bf16x8*)(dst) = pack8(o0, 0); *(bf16x8*)(dst + 1024) = pack8(o0, 1);
    *(bf16x8*)(dst + 2048) = pack8(o1, 0); *(bf16x8*)(dst + 3072) = pack8(o1, 1);
  }
  STAMP(1, 29);
}

__global__ __launch_bounds__(512, 2) void k3_kernel(const char* __restrict__ dw, const char* __restrict__ ws, char* __restrict__ out) {
  extern __shared__ __attribute__((aligned(16))) char smem[];
  LAS char* lds = (LAS char*)smem;
  const int lane = threadIdx.x & 63, w = __builtin_amdgcn_readfirstlane(threadIdx.x >> 6), r = lane & 31, h = lane >> 5;
  const int tb = blockIdx.x, b = tb / NBLK, t0 = (tb - b * NBLK) * 32;
  STAMP(2, 0);
  Stream st;
  const int hc = r & 3, ht = (hc < 2) ? t0 - 2 + hc : t0 + 30 + hc;
  const bool hvalid = ht >= 0 && ht < TSEQ;
  const int hg = b * TSEQ + (ht < 0 ? 0 : (ht >= TSEQ ? TSEQ - 1 : ht));
  const unsigned hoff = (unsigned)(hg >> 5) * 16384 + ((hg & 31) + 32 * h) * 16 + w * 2048;
  const unsigned moff = (unsigned)tb * 16384 + lane * 16 + w * 2048;
  {
    const unsigned ly = (unsigned)(uintptr_t)(lds + L_Y + w * 2048);
    asm volatile("s_mov_b32 m0, %3\n\ts_nop 0\n\tglobal_load_lds_dwordx4 %0, %2\n\tglobal_load_lds_dwordx4 %0, %2 offset:1024\n\t"
                 "s_mov_b32 m0, %4\n\ts_nop 0\n\tglobal_load_lds_dwordx4 %1, %2\n\tglobal_load_lds_dwordx4 %1, %2 offset:1024"
                 :: "v"(moff), "v"(hoff), "s"(ws + WS_ATTF), "s"(ly), "s"(ly + 16384));
  }
  u32x4 xr[2][2];
#pragma unroll
  for (int f = 0; f < 2; ++f) {
    xr[0][f] = *(const u32x4*)(ws + WS_X1F + moff + f * 1024);
    xr[1][f] = *(const u32x4*)(ws + WS_X1F + hoff + f * 1024);
  }
  const u32x4 dwv = *(const u32x4*)(dw + (threadIdx.x < 160 ? threadIdx.x : 159) * 16);
  asm volatile("s_barrier" ::: "memory");
  SB();
  st.start(ws + WS_WT3 + (size_t)w * NP3 * 16384 + lane * 16);
  SB();
  if (threadIdx.x < 160) {
    *(LAS f32x4*)(lds + L_DW + threadIdx.x * 32) = (f32x4){bflo(dwv[0]), bfhi(dwv[0]), bflo(dwv[1]), bfhi(dwv[1])};
    *(LAS f32x4*)(lds + L_DW + threadIdx.x * 32 + 16) = (f32x4){bflo(dwv[2]), bfhi(dwv[2]), bflo(dwv[3]), bfhi(dwv[3])};
  }
  asm volatile("s_waitcnt vmcnt(32)" ::: "memory");
  lds_barrier();
  STAMP(2, 1);
  f32x16 xv[2];
#pragma unroll
  for (int nb = 0; nb < 2; ++nb) { unpack8(xr[nb][0], xv[nb], 0); unpack8(xr[nb][1], xv[nb], 1); }
  gemm_k256<2, false, 0, NP3>(st, xv, lds + L_Y + lane * 16, NoWork{});
  STAMP(2, 2);
  ln_to_frags<2>(xv, lds, w, lane);
  STAMP(2, 3);
  {
    f32x16 a[2], g[2];
#pragma unroll
    for (int nb = 0; nb < 2; ++nb) { a[nb] = f32x16{}; g[nb] = f32x16{}; }
    gemm_k256<2, false, 1, NP3>(st, a, lds + L_X + lane * 16, NoWork{});
    gemm_k256<2, false, 2, NP3>(st, g, lds + L_X + lane * 16, NoWork{});
    STAMP(2, 4);
    LAS char* G = lds + L_Y + w * GSZ;
    lds_barrier();
#pragma unroll
    for (int nb = 0; nb < 2; ++nb) {
      const int row = nb == 0 ? r + 2 : (hc < 2 ? hc : 32 + hc);
      const bool wr = nb == 0 || r < 4;
      const float keep = (nb == 0 || hvalid) ? 1.0f : 0.0f;
#pragma unroll
      for (int q = 0; q < 4; ++q) {
        f32x4 e;
#pragma unroll
        for (int j = 0; j < 4; ++j) e[j] = keep * a[nb][4 * q + j] * sigmoidf_(g[nb][4 * q + j]);
        if (wr) *(LAS f32x4*)(G + row * GROW + (8 * q + 4 * h) * 4) = e;
      }
    }
    asm volatile("s_waitcnt lgkmcnt(0)" ::: "memory");
    f32x16 c;
#pragma unroll
    for (int q = 0; q < 4; ++q) {
      f32x4 e = {0.f, 0.f, 0.f, 0.f};
#pragma unroll
      for (int i = 0; i < 5; ++i)
        e += *(LAS f32x4*)(G + (r + i) * GROW + (8 * q + 4 * h) * 4) * *(LAS f32x4*)(lds + L_DW + i * 1024 + (w * 32 + 8 * q + 4 * h) * 4);
#pragma unroll
      for (int j = 0; j < 4; ++j) c[4 * q + j] = siluf_(e[j]);
    }
#pragma unroll
    for (int s = 0; s < 2; ++s) *(LAS bf16x8*)(lds + L_X + (2 * w + s) * 1024 + lane * 16) = pack8(c, s);
    lds_barrier();
  }
  STAMP(2, 5);
  f32x16 x3[1];
  x3[0] = xv[0];
  gemm_k256<1, false, 3, NP3>(st, x3, lds + L_X + lane * 16, NoWork{});
  STAMP(2, 6);
  ln_to_frags<1>(x3, lds, w, lane);
  STAMP(2, 7);
  f32x16 acc[1];
  ff_block<4, NP3>(st, lds, w, lane, acc);
  x3[0] += 0.5f * acc[0];
  STAMP(2, 8);
  float mean[1], rstd[1];
  ln_stats<1>(x3, lds, w, lane, mean, rstd);
  char* orow = out + (size_t)(tb * 32 + r) * 512 + (w * 32 + 4 * h) * 2;
  const f32x16 n = (x3[0] - mean[0]) * rstd[0];
#pragma unroll
  for (int q = 0; q < 4; ++q) *(u32x2*)(orow + q * 16) = (u32x2){pk2(n[4 * q], n[4 * q + 1]), pk2(n[4 * q + 2], n[4 * q + 3])};
  STAMP(2, 29);
}

extern "C" void kernel_launch(void* const* d_in, const int* in_sizes, int n_in, void* d_out, int out_size, void* d_ws, size_t ws_size,
                              hipStream_t stream) {
  const char* x = (const char*)d_in[0];
  const char* w1a = (const char*)d_in[1];
  const char* w1b = (const char*)d_in[2];
  const char* wqkv = (const char*)d_in[3];
  const char* wo = (const char*)d_in[4];
  const char* pw1 = (const char*)d_in[5];
  const char* dw = (const char*)d_in[6];
  const char* pw2 = (const char*)d_in[7];
  const char* w2a = (const char*)d_in[8];
  const char* w2b = (const char*)d_in[9];
  char* ws = (char*)d_ws;
  (void)hipFuncSetAttribute((const void*)k1_kernel, hipFuncAttributeMaxDynamicSharedMemorySize, L_TOTAL);
  (void)hipFuncSetAttribute((const void*)attn_kernel, hipFuncAttributeMaxDynamicSharedMemorySize, A_LDS);
  (void)hipFuncSetAttribute((const void*)k3_kernel, hipFuncAttributeMaxDynamicSharedMemorySize, L_TOTAL);
  WPtrs wp{w1a, w1b, wqkv, wo, pw1, pw2, w2a, w2b};
  prep_kernel<<<(NP1 + NP3) * 8, 256, 0, stream>>>(wp, ws);
  k1_kernel<<<NTB, 512, L_TOTAL, stream>>>(x, ws);
  attn_kernel<<<256, 256 * A_KG, A_LDS, stream>>>(ws);
  k3_kernel<<<NTB, 512, L_TOTAL, stream>>>(dw, ws, (char*)d_out);
}
```

```cpp
#include <hip/hip_runtime.h>
#include <hip/hip_bf16.h>
#include <stdint.h>

typedef short bf16x8 __attribute__((ext_vector_type(8)));
typedef short s16x4 __attribute__((ext_vector_type(4)));
typedef float f32x16 __attribute__((ext_vector_type(16)));
typedef float f32x2 __attribute__((ext_vector_type(2)));
typedef __bf16 bf16x2_t __attribute__((ext_vector_type(2)));
typedef unsigned u32x4 __attribute__((ext_vector_type(4)));
typedef unsigned u32x2 __attribute__((ext_vector_type(2)));
#define LAS __attribute__((address_space(3)))
#define DI __device__ __forceinline__
#define MFMA(a, b, c) __builtin_amdgcn_mfma_f32_32x32x16_bf16((a), (b), (c), 0, 0, 0)
#define MFMA16(a, b, c) __builtin_amdgcn_mfma_f32_16x16x32_bf16((a), (b), (c), 0, 0, 0)
#define SB() __builtin_amdgcn_sched_barrier(0)
#define STAMP(k, i) do {} while (0)
typedef float f32x4 __attribute__((ext_vector_type(4)));

constexpr int TSEQ = 4000, NBLK = 125, NTB = 250;
constexpr size_t WS_X1F = 0;
constexpr size_t WS_QF = 4u << 20;
constexpr size_t WS_KVF = 8u << 20;
constexpr size_t WS_ATTF = 17u << 20;
constexpr float LN_EPS = 1e-5f;
constexpr float LOG2E = 1.4426950408889634f;
constexpr float QSCALE = 0.125f * LOG2E;

DI unsigned pk2(float a, float b) { f32x2 v = {a, b}; return __builtin_bit_cast(unsigned, __builtin_convertvector(v, bf16x2_t)); }
DI float bflo(unsigned u) { return __uint_as_float(u << 16); }
DI float bfhi(unsigned u) { return __uint_as_float(u & 0xffff0000u); }
DI float rbf(float a) { return bflo(pk2(a, a)); }
DI bf16x8 pack8(const f32x16& v, int s) {
  u32x4 p = {pk2(v[8 * s], v[8 * s + 1]), pk2(v[8 * s + 2], v[8 * s + 3]), pk2(v[8 * s + 4], v[8 * s + 5]), pk2(v[8 * s + 6], v[8 * s + 7])};
  return __builtin_bit_cast(bf16x8, p);
}
DI void unpack8(u32x4 p, f32x16& v, int s) {
#pragma unroll
  for (int i = 0; i < 4; ++i) { v[8 * s + 2 * i] = bflo(p[i]); v[8 * s + 2 * i + 1] = bfhi(p[i]); }
}
DI float halfsum(float x) {
  auto r = __builtin_amdgcn_permlane32_swap(__float_as_uint(x), __float_as_uint(x), false, false);
  return __uint_as_float(r[0]) + __uint_as_float(r[1]);
}
DI float halfmax(float x) {
  auto r = __builtin_amdgcn_permlane32_swap(__float_as_uint(x), __float_as_uint(x), false, false);
  return fmaxf(__uint_as_float(r[0]), __uint_as_float(r[1]));
}
DI float sigmoidf_(float x) { return __builtin_amdgcn_rcpf(1.0f + __builtin_amdgcn_exp2f(-LOG2E * x)); }
DI float siluf_(float x) { return x * sigmoidf_(x); }
DI void lds_barrier() { asm volatile("s_waitcnt lgkmcnt(0)\n\ts_barrier" ::: "memory"); }
typedef short v4i16_t __attribute__((ext_vector_type(4)));
DI s16x4 trread(LAS const char* p) { return __builtin_bit_cast(s16x4, __builtin_amdgcn_ds_read_tr16_b64_v4i16((LAS v4i16_t*)p)); }
DI void stg16(char* p, bf16x8 v) { __builtin_nontemporal_store(v, (bf16x8*)p); }
DI bf16x8 ldsfrag(LAS const char* p) { return *(LAS const bf16x8*)p; }

constexpr size_t WS_WT1 = 24u << 20, WS_WT3 = 26u << 20;
constexpr int NP1 = 11, NP3 = 12;
struct WPtrs { const char *w1a, *w1b, *wqkv, *wo, *pw1, *pw2, *w2a, *w2b; };
DI void ff_panel(const char* wa, const char* wb, int p, int w2, const char*& base, unsigned& ld) {
  const bool isb = (p == 2 || p == 4 || p >= 6);
  const int c = p < 2 ? p : (p == 3 ? 2 : (p == 5 ? 3 : (p == 2 ? 0 : (p == 4 ? 1 : p - 4))));
  if (isb) { base = wb + (size_t)c * 256 * 512 + w2 * 128; ld = 512; } else { base = wa + (size_t)(w2 + 4 * c) * 128; ld = 2048; }
}
__global__ __launch_bounds__(256) void prep_kernel(WPtrs wp, char* __restrict__ ws) {
  __shared__ __attribute__((aligned(16))) char sm[4 * 4096];
  const int lane = threadIdx.x & 63, wv = __builtin_amdgcn_readfirstlane(threadIdx.x >> 6);
  const int g = blockIdx.x * 4 + wv;
  const bool k3 = g >= 4 * NP1 * 8;
  const int gg = k3 ? g - 4 * NP1 * 8 : g, np = k3 ? NP3 : NP1;
  const int w2 = gg / (np * 8), p = (gg >> 3) % np, sl = gg & 7;
  const char* base; unsigned ld;
  if (!k3) {
    if (p < 8) ff_panel(wp.w1a, wp.w1b, p, w2, base, ld);
    else { base = wp.wqkv + (p - 8) * 512 + w2 * 128; ld = 1536; }
  } else {
    if (p == 0) { base = wp.wo + w2 * 128; ld = 512; }
    else if (p < 3) { base = wp.pw1 + (p - 1) * 512 + w2 * 128; ld = 1024; }
    else if (p == 3) { base = wp.pw2 + w2 * 128; ld = 512; }
    else ff_panel(wp.w2a, wp.w2b, p - 4, w2, base, ld);
  }
  LAS char* my = (LAS char*)sm + wv * 4096;
  const char* src = base + (size_t)(sl * 32 + (lane >> 3)) * ld + (((lane & 7) ^ (((lane >> 4) & 1) << 2)) << 4);
  u32x4 t[4];
#pragma unroll
  for (int i = 0; i < 4; ++i) t[i] = *(const u32x4*)(src + (size_t)i * 8 * ld);
#pragma unroll
  for (int i = 0; i < 4; ++i) *(LAS u32x4*)(my + i * 1024 + lane * 16) = t[i];
  asm volatile("s_waitcnt lgkmcnt(0)" ::: "memory");
  const int h = lane >> 5, cb = (lane >> 4) & 1, q = (lane & 15) >> 2, pp = lane & 3;
  char* dst = ws + (k3 ? WS_WT3 : WS_WT1) + (size_t)((2 * w2) * np + p) * 16384 + sl * 2048 + lane * 16;
#pragma unroll
  for (int i = 0; i < 4; ++i) {
    LAS const char* rp = my + (4 * h + q) * 128 + (((i & 1) ^ (q >> 1)) * 64) + cb * 32 + pp * 8 + (i >> 1) * 2048;
    s16x4 l = trread(rp), hh = trread(rp + 1024);
    *(bf16x8*)(dst + (size_t)(i & 1) * np * 16384 + (i >> 1) * 1024) = __builtin_shufflevector(l, hh, 0, 1, 2, 3, 4, 5, 6, 7);
  }
}

struct Stream {
  const char* p;
  bf16x8 wf[2][16];
  DI void start(const char* base) {
    p = base;
#pragma unroll
    for (int i = 0; i < 32; ++i) wf[i >> 4][i & 15] = *(const bf16x8*)(p + i * 1024);
  }
};
struct NoWork { DI void init() {} DI void step(int) {} };
struct SiluWork {
  LAS char* p;
  u32x2 raw; float x[4], e[4];
  DI void init() { raw = *(LAS u32x2*)p; }
  DI void step(int ks) {
    const int g = ks >> 2, i = ks & 3;
    if (i == 0) {
      x[0] = bflo(raw[0]); x[1] = bfhi(raw[0]); x[2] = bflo(raw[1]); x[3] = bfhi(raw[1]);
#pragma unroll
      for (int j = 0; j < 4; ++j) e[j] = -LOG2E * x[j];
      asm volatile("" : "+v"(e[0]), "+v"(e[1]), "+v"(e[2]), "+v"(e[3]), "+v"(x[0]), "+v"(x[1]), "+v"(x[2]), "+v"(x[3]));
      if (g < 3) raw = *(LAS u32x2*)(p + ((g + 1) >> 1) * 1024 + ((g + 1) & 1) * 8);
    } else if (i == 1) {
#pragma unroll
      for (int j = 0; j < 4; ++j) e[j] = __builtin_amdgcn_exp2f(e[j]);
      asm volatile("" : "+v"(e[0]), "+v"(e[1]), "+v"(e[2]), "+v"(e[3]));
    } else if (i == 2) {
#pragma unroll
      for (int j = 0; j < 4; ++j) e[j] = __builtin_amdgcn_rcpf(1.0f + e[j]);
      asm volatile("" : "+v"(e[0]), "+v"(e[1]), "+v"(e[2]), "+v"(e[3]));
    } else {
      *(LAS u32x2*)(p + (g >> 1) * 1024 + (g & 1) * 8) = (u32x2){pk2(x[0] * e[0], x[1] * e[1]), pk2(x[2] * e[2], x[3] * e[3])};
    }
  }
};
template <int NB, bool SWAP, int P, int NP, class W>
DI void gemm_k256(Stream& st, f32x16 (&acc)[NB], LAS const char* bl, W work) {
  constexpr bool REFILL = P + 2 < NP;
  bf16x8 bq[3][NB];
#pragma unroll
  for (int nb = 0; nb < NB; ++nb) { bq[0][nb] = ldsfrag(bl + nb * 16384); bq[1][nb] = ldsfrag(bl + nb * 16384 + 1024); }
  work.init();
#pragma unroll
  for (int ks = 0; ks < 16; ++ks) {
    SB();
#pragma unroll
    for (int nb = 0; nb < NB; ++nb)
      acc[nb] = SWAP ? MFMA(bq[ks % 3][nb], st.wf[P & 1][ks], acc[nb]) : MFMA(st.wf[P & 1][ks], bq[ks % 3][nb], acc[nb]);
    if (ks < 14) {
#pragma unroll
      for (int nb = 0; nb < NB; ++nb) bq[(ks + 2) % 3][nb] = ldsfrag(bl + nb * 16384 + (ks + 2) * 1024);
    }
    if (REFILL && ks > 0) st.wf[P & 1][ks - 1] = *(const bf16x8*)(st.p + (P + 2) * 16384 + (ks - 1) * 1024);
    work.step(ks);
  }
  SB();
  if (REFILL) st.wf[P & 1][15] = *(const bf16x8*)(st.p + (P + 2) * 16384 + 15 * 1024);
}

constexpr int L_X = 0, L_Y = L_X + 16384, L_Z = L_Y + 16384;
constexpr int L_STAT = L_Z + 16384 + 9216;
constexpr int GROW = 144, GSZ = 36 * GROW;
constexpr int L_DW = L_STAT + 4096;
constexpr int L_TOTAL = L_DW + 5120;

DI float sum16(const f32x16& t) {
  return (((t[0] + t[1]) + (t[2] + t[3])) + ((t[4] + t[5]) + (t[6] + t[7]))) + (((t[8] + t[9]) + (t[10] + t[11])) + ((t[12] + t[13]) + (t[14] + t[15])));
}
template <int NB>
DI void ln_stats(const f32x16 (&v)[NB], LAS char* lds, int w, int lane, float (&mean)[NB], float (&rstd)[NB]) {
  const int r = lane & 31;
#pragma unroll
  for (int nb = 0; nb < NB; ++nb) {
    const float mw = halfsum(sum16(v[nb])) * (1.0f / 32.0f);
    const f32x16 d = v[nb] - mw;
    const float m2 = halfsum(sum16(d * d));
    if (lane < 32) *(LAS f32x2*)(lds + L_STAT + ((nb * 32 + r) * 8 + w) * 8) = (f32x2){mw, m2};
  }
  lds_barrier();
#pragma unroll
  for (int nb = 0; nb < NB; ++nb) {
    f32x4 s[4];
#pragma unroll
    for (int i = 0; i < 4; ++i) s[i] = *(LAS f32x4*)(lds + L_STAT + (nb * 32 + r) * 64 + i * 16);
    const float m = 0.125f * (((s[0][0] + s[0][2]) + (s[1][0] + s[1][2])) + ((s[2][0] + s[2][2]) + (s[3][0] + s[3][2])));
    float M2 = 0.f;
#pragma unroll
    for (int i = 0; i < 4; ++i) { const float e0 = s[i][0] - m, e1 = s[i][2] - m; M2 += s[i][1] + s[i][3] + 32.0f * (e0 * e0 + e1 * e1); }
    mean[nb] = m; rstd[nb] = __builtin_amdgcn_rsqf(M2 * (1.0f / 256.0f) + LN_EPS);
  }
}
template <int NB>
DI void ln_to_frags(const f32x16 (&v)[NB], LAS char* lds, int w, int lane) {
  float mean[NB], rstd[NB];
  ln_stats<NB>(v, lds, w, lane, mean, rstd);
  LAS char* fb = lds + L_X + w * 2048 + lane * 16;
#pragma unroll
  for (int nb = 0; nb < NB; ++nb) {
    const f32x16 n = (v[nb] - mean[nb]) * rstd[nb];
#pragma unroll
    for (int s = 0; s < 2; ++s) *(LAS bf16x8*)(fb + nb * 16384 + s * 1024) = pack8(n, s);
  }
  lds_barrier();
}

template <int P0, int NP>
DI void ff_block(Stream& st, LAS char* lds, int w, int lane, f32x16 (&out)[1]) {
  out[0] = f32x16{};
  LAS char* xb = lds + L_X + lane * 16;
  LAS char* yb = lds + L_Y + lane * 16;
  LAS char* zb = lds + L_Z + lane * 16;
  auto put = [&](LAS char* dst, const f32x16& hv) { *(LAS bf16x8*)(dst + w * 2048) = pack8(hv, 0); *(LAS bf16x8*)(dst + w * 2048 + 1024) = pack8(hv, 1); };
  f32x16 hn[1];
  hn[0] = f32x16{}; gemm_k256<1, false, P0 + 0, NP>(st, hn, xb, NoWork{});
  put(yb, hn[0]);
  hn[0] = f32x16{}; gemm_k256<1, false, P0 + 1, NP>(st, hn, xb, SiluWork{yb + w * 2048});
  lds_barrier(); put(zb, hn[0]);
  gemm_k256<1, false, P0 + 2, NP>(st, out, yb, NoWork{});
  hn[0] = f32x16{}; gemm_k256<1, false, P0 + 3, NP>(st, hn, xb, SiluWork{zb + w * 2048});
  lds_barrier(); put(yb, hn[0]);
  gemm_k256<1, false, P0 + 4, NP>(st, out, zb, NoWork{});
  hn[0] = f32x16{}; gemm_k256<1, false, P0 + 5, NP>(st, hn, xb, SiluWork{yb + w * 2048});
  lds_barrier(); put(zb, hn[0]);
  gemm_k256<1, false, P0 + 6, NP>(st, out, yb, SiluWork{zb + w * 2048});
  lds_barrier();
  gemm_k256<1, false, P0 + 7, NP>(st, out, zb, NoWork{});
}

DI void l2_warm(const char* p, int kb_total, int idx, int lane, u32x4& t) {
  const int per = (kb_total + 247) / 248;
  int b = idx * per, e = b + per; if (e > kb_total) e = kb_total;
  for (int i = b; i < e; ++i)
    asm volatile("global_load_dwordx4 %0, %1, %2" : "+v"(t) : "v"(lane * 16 + i * 1024), "s"(p));
}

__global__ __launch_bounds__(512, 2) void k1_kernel(const char* __restrict__ x, char* __restrict__ ws) {
  extern __shared__ __attribute__((aligned(16))) char smem[];
  LAS char* lds = (LAS char*)smem;
  const int lane = threadIdx.x & 63, w = __builtin_amdgcn_readfirstlane(threadIdx.x >> 6), r = lane & 31, h = lane >> 5;
  const int tb = blockIdx.x;
  STAMP(0, 0);
  f32x16 xv[1];
  Stream st;
  u32x4 wt = {0u, 0u, 0u, 0u};
  {
    const char* xrow = x + (size_t)(tb * 32 + r) * 512 + (w * 32 + 4 * h) * 2;
    u32x2 u[4];
#pragma unroll
    for (int g = 0; g < 4; ++g) u[g] = *(const u32x2*)(xrow + g * 16);
    SB();
    st.start(ws + WS_WT1 + (size_t)w * NP1 * 16384 + lane * 16);
    SB();
#pragma unroll
    for (int g = 0; g < 4; ++g) {
      xv[0][4 * g] = bflo(u[g][0]); xv[0][4 * g + 1] = bfhi(u[g][0]); xv[0][4 * g + 2] = bflo(u[g][1]); xv[0][4 * g + 3] = bfhi(u[g][1]);
    }
  }
  STAMP(0, 1);
  ln_to_frags<1>(xv, lds, w, lane);
  STAMP(0, 2);
  f32x16 acc[1];
  ff_block<0, NP1>(st, lds, w, lane, acc);
  asm volatile("" :: "v"(wt));
  STAMP(0, 3);
#pragma unroll
  for (int i = 0; i < 16; ++i) xv[0][i] = rbf(xv[0][i] + 0.5f * acc[0][i]);
#pragma unroll
  for (int s = 0; s < 2; ++s) stg16((char*)(ws + WS_X1F + ((size_t)tb * 16 + 2 * w + s) * 1024 + lane * 16), pack8(xv[0], s));
  ln_to_frags<1>(xv, lds, w, lane);
  STAMP(0, 4);
  const int b = tb / NBLK, blk = tb - b * NBLK, bh = b * 4 + (w >> 1);
  char* kvdst = ws + WS_KVF + (size_t)(bh * NBLK + blk) * 8192 + (w & 1) * 2048 + lane * 16;
  {
    f32x16 a[1]; a[0] = f32x16{};
    gemm_k256<1, false, 8, NP1>(st, a, lds + L_X + lane * 16, NoWork{});
    a[0] = a[0] * QSCALE;
    char* dst = ws + WS_QF + (size_t)(bh * NBLK + blk) * 4096 + (w & 1) * 2048 + lane * 16;
    stg16((char*)(dst), pack8(a[0], 0)); stg16((char*)(dst + 1024), pack8(a[0], 1));
  }
  {
    f32x16 a[1]; a[0] = f32x16{};
    gemm_k256<1, false, 9, NP1>(st, a, lds + L_X + lane * 16, NoWork{});
    stg16((char*)(kvdst), pack8(a[0], 0)); stg16((char*)(kvdst + 1024), pack8(a[0], 1));
  }
  STAMP(0, 5);
  {
    f32x16 a[1]; a[0] = f32x16{};
    gemm_k256<1, true, 10, NP1>(st, a, lds + L_X + lane * 16, NoWork{});
    stg16((char*)(kvdst + 4096), pack8(a[0], 0)); stg16((char*)(kvdst + 5120), pack8(a[0], 1));
  }
  STAMP(0, 29);
}

constexpr int A_KG = 2, A_SBLK = A_KG * 2, A_HST = A_SBLK * 4096, A_NST = (NBLK + A_SBLK - 1) / A_SBLK, A_NFULL = NBLK / A_SBLK;
constexpr int A_VOFF = 3 * A_HST, A_LDS = 6 * A_HST;
__global__ __launch_bounds__(256 * A_KG, A_KG) void attn_kernel(char* __restrict__ ws) {
  extern __shared__ __attribute__((aligned(16))) char smem[];
  LAS char* lds = (LAS char*)smem;
  const int lane = threadIdx.x & 63, w = __builtin_amdgcn_readfirstlane(threadIdx.x >> 6);
  const int bh = blockIdx.x & 7, qg = blockIdx.x >> 3, qi = w & 3, kg = w >> 2;
  const int qb_raw = qg * 4 + qi, qb = qb_raw < NBLK ? qb_raw : NBLK - 1;
  STAMP(1, 0);
  const char* kv = ws + WS_KVF + (size_t)bh * NBLK * 8192;
  const unsigned lds0 = (unsigned)(uintptr_t)lds;
  const unsigned voff = (w >> 1) * 8192 + (w & 1) * 2048 + lane * 16;
  auto dma2 = [&](int st, int isv, unsigned ldsoff) {
    asm volatile("s_mov_b32 m0, %2\n\ts_nop 0\n\tglobal_load_lds_dwordx4 %0, %1\n\tglobal_load_lds_dwordx4 %0, %1 offset:1024"
                 :: "v"(voff), "s"(kv + (size_t)st * (2 * A_HST) + isv * 4096), "s"(lds0 + ldsoff + w * 2048));
  };
  dma2(0, 0, 0); dma2(1, 0, A_HST); dma2(0, 1, A_VOFF); dma2(2, 0, 2 * A_HST); dma2(1, 1, A_VOFF + A_HST);
  bf16x8 qf[4];
#pragma unroll
  for (int f = 0; f < 4; ++f) qf[f] = *(const bf16x8*)(ws + WS_QF + ((size_t)(bh * NBLK + qb) * 4 + f) * 1024 + lane * 16);
#pragma unroll
  for (int f = 0; f < 4; ++f) asm volatile("" : "+v"(qf[f]));
  u32x4 wt = {0u, 0u, 0u, 0u};
  const int rem = NBLK - A_NFULL * A_SBLK - kg * 2;
  const int ntail = rem < 0 ? 0 : (rem > 2 ? 2 : rem);
  LAS const char* base = lds + kg * 8192 + lane * 16;
  f32x16 o0 = f32x16{}, o1 = f32x16{}, negr, sa, sb;
  f32x4 lacc = {0.f, 0.f, 0.f, 0.f};
  bf16x8 sel;
  { const short one = (((lane >> 4) ^ lane) & 1) ? (short)0 : (short)0x3f80;
#pragma unroll
    for (int j = 0; j < 8; ++j) sel[j] = one; }
  bf16x8 kf[4], vf[4], kg2[4], vg2[4]; unsigned pa[8], pb[8];
#pragma unroll
  for (int i = 0; i < 8; ++i) pa[i] = 0u;
  float ref;
  asm volatile("s_waitcnt vmcnt(0)\n\ts_barrier" : "+v"(wt) :: "memory");
  {
#pragma unroll
    for (int f = 0; f < 4; ++f) kf[f] = ldsfrag(base + f * 1024);
    f32x16 z = f32x16{};
    sa = MFMA(kf[0], qf[0], z); sa = MFMA(kf[1], qf[1], sa); sa = MFMA(kf[2], qf[2], sa); sa = MFMA(kf[3], qf[3], sa);
    float mx = sa[0];
#pragma unroll
    for (int i = 1; i < 16; ++i) mx = fmaxf(mx, sa[i]);
    ref = halfmax(mx) + 7.0f;
#pragma unroll
    for (int i = 0; i < 16; ++i) { sa[i] -= ref; negr[i] = -ref; }
#pragma unroll
    for (int f = 0; f < 4; ++f) { kf[f] = ldsfrag(base + 4096 + f * 1024); vf[f] = bf16x8{}; }
  }
#define SLOT3(MF, RF, i) do { MF; RF; e[2*(i)] = __builtin_amdgcn_exp2f(SC_[2*(i)]); e[2*(i)+1] = __builtin_amdgcn_exp2f(SC_[2*(i)+1]); \
    if ((i) > 0) PN_[(i)-1] = pk2(e[2*(i)-2], e[2*(i)-1]); SB(); } while (0)
  auto iter = [&](f32x16& SC_, f32x16& SN_, unsigned (&PP_)[8], unsigned (&PN_)[8], bf16x8 (&KC)[4], bf16x8 (&VC)[4], bf16x8 (&KN)[4], bf16x8 (&VN)[4], LAS const char* kn, LAS const char* vc) {
    float e[16];
    const bf16x8 p0 = __builtin_bit_cast(bf16x8, (u32x4){PP_[0], PP_[1], PP_[2], PP_[3]}), p1 = __builtin_bit_cast(bf16x8, (u32x4){PP_[4], PP_[5], PP_[6], PP_[7]});
    SB();
    SLOT3(SN_ = MFMA(KC[0], qf[0], negr), KN[0] = ldsfrag(kn), 0);
    SLOT3(o0 = MFMA(VC[0], p0, o0), VN[0] = ldsfrag(vc), 1);
    SLOT3(SN_ = MFMA(KC[1], qf[1], SN_), KN[1] = ldsfrag(kn + 1024), 2);
    SLOT3(o1 = MFMA(VC[2], p0, o1); lacc = MFMA16(sel, p0, lacc), VN[2] = ldsfrag(vc + 2048), 3);
    SLOT3(SN_ = MFMA(KC[2], qf[2], SN_), KN[2] = ldsfrag(kn + 2048), 4);
    SLOT3(o0 = MFMA(VC[1], p1, o0), VN[1] = ldsfrag(vc + 1024), 5);
    SLOT3(SN_ = MFMA(KC[3], qf[3], SN_), KN[3] = ldsfrag(kn + 3072), 6);
    SLOT3(o1 = MFMA(VC[3], p1, o1); lacc = MFMA16(sel, p1, lacc), VN[3] = ldsfrag(vc + 3072), 7);
    PN_[7] = pk2(e[14], e[15]);
    const unsigned orall = (PN_[0] | PN_[1] | PN_[2]) | (PN_[3] | PN_[4] | PN_[5]) | (PN_[6] | PN_[7]);
    if (__builtin_expect(__any((orall & 0x40004000u) != 0u), 0)) {
      float mx = SC_[0];
#pragma unroll
      for (int i = 1; i < 16; ++i) mx = fmaxf(mx, SC_[i]);
      mx = halfmax(mx);
      const float delta = (mx >= 0.5f) ? mx + 7.0f : 0.0f;
      const float alpha = __builtin_amdgcn_exp2f(-delta);
      const float alo = __shfl_xor(alpha, 16);
      const float aA = (lane & 16) ? alo : alpha, aB = (lane & 16) ? alpha : alo;
      lacc = lacc * (f32x4){aA, aB, aA, aB};
      o0 = o0 * alpha; o1 = o1 * alpha; negr = negr - delta; SN_ = SN_ - delta;
      asm volatile("" : "+v"(o0), "+v"(o1), "+v"(negr), "+v"(SN_), "+v"(lacc));
      ref = -negr[0];
#pragma unroll
      for (int i = 0; i < 8; ++i) PN_[i] = pk2(__builtin_amdgcn_exp2f(SC_[2*i] - delta), __builtin_amdgcn_exp2f(SC_[2*i+1] - delta));
    }
  };
  STAMP(1, 1);
  int r0 = 0, r1 = A_HST, r2 = 2 * A_HST;
  for (int st = 0; st < A_NFULL; ++st) {
    asm volatile("s_waitcnt vmcnt(4) lgkmcnt(0)\n\ts_barrier" ::: "memory");
    dma2(st + 3 < A_NST ? st + 3 : A_NST - 1, 0, r0); dma2(st + 2 < A_NST ? st + 2 : A_NST - 1, 1, A_VOFF + r2);
    iter(sa, sb, pa, pb, kf, vf, kg2, vg2, base + r1, base + A_VOFF + r0);
    iter(sb, sa, pb, pa, kg2, vg2, kf, vf, base + r1 + 4096, base + A_VOFF + r0 + 4096);
    const int t = r0; r0 = r1; r1 = r2; r2 = t;
  }
  asm volatile("s_waitcnt vmcnt(0) lgkmcnt(0)\n\ts_barrier" ::: "memory");
  if (ntail > 0) {
    iter(sa, sb, pa, pb, kf, vf, kg2, vg2, base + r1, base + A_VOFF + r0);
    if (ntail > 1) iter(sb, sa, pb, pa, kg2, vg2, kf, vf, base + r1 + 4096, base + A_VOFF + r0 + 4096);
    else {
#pragma unroll
      for (int i = 0; i < 8; ++i) pa[i] = pb[i];
#pragma unroll
      for (int i = 0; i < 4; ++i) vf[i] = vg2[i];
    }
  }
  {
    const bf16x8 p0 = __builtin_bit_cast(bf16x8, (u32x4){pa[0], pa[1], pa[2], pa[3]}), p1 = __builtin_bit_cast(bf16x8, (u32x4){pa[4], pa[5], pa[6], pa[7]});
    o0 = MFMA(vf[0], p0, o0); o1 = MFMA(vf[2], p0, o1); o0 = MFMA(vf[1], p1, o0); o1 = MFMA(vf[3], p1, o1);
    lacc = MFMA16(sel, p0, lacc); lacc = MFMA16(sel, p1, lacc);
  }
  float lsum = (lane & 16) ? lacc[1] : lacc[0];
  STAMP(1, 2);
  lds_barrier();
  if (kg > 0) {
    LAS char* cb = lds + ((kg - 1) * 4 + qi) * 9216 + lane * 16;
#pragma unroll
    for (int g = 0; g < 4; ++g) {
      *(LAS f32x4*)(cb + g * 1024) = (f32x4){o0[4 * g], o0[4 * g + 1], o0[4 * g + 2], o0[4 * g + 3]};
      *(LAS f32x4*)(cb + 4096 + g * 1024) = (f32x4){o1[4 * g], o1[4 * g + 1], o1[4 * g + 2], o1[4 * g + 3]};
    }
    *(LAS f32x2*)(lds + ((kg - 1) * 4 + qi) * 9216 + 8192 + lane * 8) = (f32x2){ref, lsum};
  }
  lds_barrier();
  if (kg == 0 && qb_raw < NBLK) {
#pragma unroll
    for (int k = 1; k < A_KG; ++k) {
      LAS char* cb = lds + ((k - 1) * 4 + qi) * 9216 + lane * 16;
      const f32x2 rl = *(LAS f32x2*)(lds + ((k - 1) * 4 + qi) * 9216 + 8192 + lane * 8);
      const float m = fmaxf(ref, rl[0]);
      const float c0 = __builtin_amdgcn_exp2f(ref - m), c1 = __builtin_amdgcn_exp2f(rl[0] - m);
      lsum = lsum * c0 + rl[1] * c1; ref = m;
#pragma unroll
      for (int g = 0; g < 4; ++g) {
        const f32x4 u0 = *(LAS f32x4*)(cb + g * 1024), u1 = *(LAS f32x4*)(cb + 4096 + g * 1024);
#pragma unroll
        for (int j = 0; j < 4; ++j) { o0[4 * g + j] = o0[4 * g + j] * c0 + u0[j] * c1; o1[4 * g + j] = o1[4 * g + j] * c0 + u1[j] * c1; }
      }
    }
    const float inv = 1.0f / lsum;
#pragma unroll
    for (int i = 0; i < 16; ++i) { o0[i] *= inv; o1[i] *= inv; }
    char* dst = ws + WS_ATTF + ((size_t)((bh >> 2) * NBLK + qb) * 16 + (bh & 3) * 4) * 1024 + lane * 16;
    stg16((char*)(dst), pack8(o0, 0)); stg16((char*)(dst + 1024), pack8(o0, 1));
    stg16((char*)(dst + 2048), pack8(o1, 0)); stg16((char*)(dst + 3072), pack8(o1, 1));
  }
  STAMP(1, 29);
}

__global__ __launch_bounds__(512, 2) void k3_kernel(const char* __restrict__ dw, const char* __restrict__ ws, char* __restrict__ out) {
  extern __shared__ __attribute__((aligned(16))) char smem[];
  LAS char* lds = (LAS char*)smem;
  const int lane = threadIdx.x & 63, w = __builtin_amdgcn_readfirstlane(threadIdx.x >> 6), r = lane & 31, h = lane >> 5;
  const int tb = blockIdx.x, b = tb / NBLK, t0 = (tb - b * NBLK) * 32;
  STAMP(2, 0);
  Stream st;
  const int hc = r & 3, ht = (hc < 2) ? t0 - 2 + hc : t0 + 30 + hc;
  const bool hvalid = ht >= 0 && ht < TSEQ;
  const int hg = b * TSEQ + (ht < 0 ? 0 : (ht >= TSEQ ? TSEQ - 1 : ht));
  const unsigned hoff = (unsigned)(hg >> 5) * 16384 + ((hg & 31) + 32 * h) * 16 + w * 2048;
  const unsigned moff = (unsigned)tb * 16384 + lane * 16 + w * 2048;
  {
    const unsigned ly = (unsigned)(uintptr_t)(lds + L_Y + w * 2048);
    asm volatile("s_mov_b32 m0, %3\n\ts_nop 0\n\tglobal_load_lds_dwordx4 %0, %2\n\tglobal_load_lds_dwordx4 %0, %2 offset:1024\n\t"
                 "s_mov_b32 m0, %4\n\ts_nop 0\n\tglobal_load_lds_dwordx4 %1, %2\n\tglobal_load_lds_dwordx4 %1, %2 offset:1024"
                 :: "v"(moff), "v"(hoff), "s"(ws + WS_ATTF), "s"(ly), "s"(ly + 16384));
  }
  u32x4 xr[2][2];
#pragma unroll
  for (int f = 0; f < 2; ++f) {
    xr[0][f] = *(const u32x4*)(ws + WS_X1F + moff + f * 1024);
    xr[1][f] = *(const u32x4*)(ws + WS_X1F + hoff + f * 1024);
  }
  const u32x4 dwv = *(const u32x4*)(dw + (threadIdx.x < 160 ? threadIdx.x : 159) * 16);
  SB();
  st.start(ws + WS_WT3 + (size_t)w * NP3 * 16384 + lane * 16);
  SB();
  if (threadIdx.x < 160) {
    *(LAS f32x4*)(lds + L_DW + threadIdx.x * 32) = (f32x4){bflo(dwv[0]), bfhi(dwv[0]), bflo(dwv[1]), bfhi(dwv[1])};
    *(LAS f32x4*)(lds + L_DW + threadIdx.x * 32 + 16) = (f32x4){bflo(dwv[2]), bfhi(dwv[2]), bflo(dwv[3]), bfhi(dwv[3])};
  }
  asm volatile("s_waitcnt vmcnt(32)" ::: "memory");
  lds_barrier();
  STAMP(2, 1);
  f32x16 xv[2];
#pragma unroll
  for (int nb = 0; nb < 2; ++nb) { unpack8(xr[nb][0], xv[nb], 0); unpack8(xr[nb][1], xv[nb], 1); }
  gemm_k256<2, false, 0, NP3>(st, xv, lds + L_Y + lane * 16, NoWork{});
  STAMP(2, 2);
  ln_to_frags<2>(xv, lds, w, lane);
  STAMP(2, 3);
  {
    f32x16 a[2], g[2];
#pragma unroll
    for (int nb = 0; nb < 2; ++nb) { a[nb] = f32x16{}; g[nb] = f32x16{}; }
    gemm_k256<2, false, 1, NP3>(st, a, lds + L_X + lane * 16, NoWork{});
    gemm_k256<2, false, 2, NP3>(st, g, lds + L_X + lane * 16, NoWork{});
    STAMP(2, 4);
    LAS char* G = lds + L_Y + w * GSZ;
    lds_barrier();
#pragma unroll
    for (int nb = 0; nb < 2; ++nb) {
      const int row = nb == 0 ? r + 2 : (hc < 2 ? hc : 32 + hc);
      const bool wr = nb == 0 || r < 4;
      const float keep = (nb == 0 || hvalid) ? 1.0f : 0.0f;
#pragma unroll
      for (int q = 0; q < 4; ++q) {
        f32x4 e;
#pragma unroll
        for (int j = 0; j < 4; ++j) e[j] = keep * a[nb][4 * q + j] * sigmoidf_(g[nb][4 * q + j]);
        if (wr) *(LAS f32x4*)(G + row * GROW + (8 * q + 4 * h) * 4) = e;
      }
    }
    asm volatile("s_waitcnt lgkmcnt(0)" ::: "memory");
    f32x16 c;
#pragma unroll
    for (int q = 0; q < 4; ++q) {
      f32x4 e = {0.f, 0.f, 0.f, 0.f};
#pragma unroll
      for (int i = 0; i < 5; ++i)
        e += *(LAS f32x4*)(G + (r + i) * GROW + (8 * q + 4 * h) * 4) * *(LAS f32x4*)(lds + L_DW + i * 1024 + (w * 32 + 8 * q + 4 * h) * 4);
#pragma unroll
      for (int j = 0; j < 4; ++j) c[4 * q + j] = siluf_(e[j]);
    }
#pragma unroll
    for (int s = 0; s < 2; ++s) *(LAS bf16x8*)(lds + L_X + (2 * w + s) * 1024 + lane * 16) = pack8(c, s);
    lds_barrier();
  }
  STAMP(2, 5);
  f32x16 x3[1];
  x3[0] = xv[0];
  gemm_k256<1, false, 3, NP3>(st, x3, lds + L_X + lane * 16, NoWork{});
  STAMP(2, 6);
  ln_to_frags<1>(x3, lds, w, lane);
  STAMP(2, 7);
  f32x16 acc[1];
  ff_block<4, NP3>(st, lds, w, lane, acc);
  x3[0] += 0.5f * acc[0];
  STAMP(2, 8);
  float mean[1], rstd[1];
  ln_stats<1>(x3, lds, w, lane, mean, rstd);
  char* orow = out + (size_t)(tb * 32 + r) * 512 + (w * 32 + 4 * h) * 2;
  const f32x16 n = (x3[0] - mean[0]) * rstd[0];
#pragma unroll
  for (int q = 0; q < 4; ++q) *(u32x2*)(orow + q * 16) = (u32x2){pk2(n[4 * q], n[4 * q + 1]), pk2(n[4 * q + 2], n[4 * q + 3])};
  STAMP(2, 29);
}

extern "C" void kernel_launch(void* const* d_in, const int* in_sizes, int n_in, void* d_out, int out_size, void* d_ws, size_t ws_size,
                              hipStream_t stream) {
  const char* x = (const char*)d_in[0];
  const char* w1a = (const char*)d_in[1];
  const char* w1b = (const char*)d_in[2];
  const char* wqkv = (const char*)d_in[3];
  const char* wo = (const char*)d_in[4];
  const char* pw1 = (const char*)d_in[5];
  const char* dw = (const char*)d_in[6];
  const char* pw2 = (const char*)d_in[7];
  const char* w2a = (const char*)d_in[8];
  const char* w2b = (const char*)d_in[9];
  char* ws = (char*)d_ws;
  (void)hipFuncSetAttribute((const void*)k1_kernel, hipFuncAttributeMaxDynamicSharedMemorySize, L_TOTAL);
  (void)hipFuncSetAttribute((const void*)attn_kernel, hipFuncAttributeMaxDynamicSharedMemorySize, A_LDS);
  (void)hipFuncSetAttribute((const void*)k3_kernel, hipFuncAttributeMaxDynamicSharedMemorySize, L_TOTAL);
  WPtrs wp{w1a, w1b, wqkv, wo, pw1, pw2, w2a, w2b};
  prep_kernel<<<(NP1 + NP3) * 8, 256, 0, stream>>>(wp, ws);
  k1_kernel<<<NTB, 512, L_TOTAL, stream>>>(x, ws);
  attn_kernel<<<256, 256 * A_KG, A_LDS, stream>>>(ws);
  k3_kernel<<<NTB, 512, L_TOTAL, stream>>>(dw, ws, (char*)d_out);
}
```

```cpp
#include <hip/hip_runtime.h>
#include <hip/hip_bf16.h>
#include <stdint.h>

typedef short bf16x8 __attribute__((ext_vector_type(8)));
typedef short s16x4 __attribute__((ext_vector_type(4)));
typedef float f32x16 __attribute__((ext_vector_type(16)));
typedef float f32x2 __attribute__((ext_vector_type(2)));
typedef __bf16 bf16x2_t __attribute__((ext_vector_type(2)));
typedef unsigned u32x4 __attribute__((ext_vector_type(4)));
typedef unsigned u32x2 __attribute__((ext_vector_type(2)));
#define LAS __attribute__((address_space(3)))
#define DI __device__ __forceinline__
#define MFMA(a, b, c) __builtin_amdgcn_mfma_f32_32x32x16_bf16((a), (b), (c), 0, 0, 0)
#define MFMA16(a, b, c) __builtin_amdgcn_mfma_f32_16x16x32_bf16((a), (b), (c), 0, 0, 0)
#define SB() __builtin_amdgcn_sched_barrier(0)
#define STAMP(k, i) do {} while (0)
typedef float f32x4 __attribute__((ext_vector_type(4)));

constexpr int TSEQ = 4000, NBLK = 125, NTB = 250;
constexpr size_t WS_X1F = 0;
constexpr size_t WS_QF = 4u << 20;
constexpr size_t WS_KVF = 8u << 20;
constexpr size_t WS_ATTF = 17u << 20;
constexpr float LN_EPS = 1e-5f;
constexpr float LOG2E = 1.4426950408889634f;
constexpr float QSCALE = 0.125f * LOG2E;

DI unsigned pk2(float a, float b) { f32x2 v = {a, b}; return __builtin_bit_cast(unsigned, __builtin_convertvector(v, bf16x2_t)); }
DI float bflo(unsigned u) { return __uint_as_float(u << 16); }
DI float bfhi(unsigned u) { return __uint_as_float(u & 0xffff0000u); }
DI float rbf(float a) { return bflo(pk2(a, a)); }
DI bf16x8 pack8(const f32x16& v, int s) {
  u32x4 p = {pk2(v[8 * s], v[8 * s + 1]), pk2(v[8 * s + 2], v[8 * s + 3]), pk2(v[8 * s + 4], v[8 * s + 5]), pk2(v[8 * s + 6], v[8 * s + 7])};
  return __builtin_bit_cast(bf16x8, p);
}
DI void unpack8(u32x4 p, f32x16& v, int s) {
#pragma unroll
  for (int i = 0; i < 4; ++i) { v[8 * s + 2 * i] = bflo(p[i]); v[8 * s + 2 * i + 1] = bfhi(p[i]); }
}
DI float halfsum(float x) {
  auto r = __builtin_amdgcn_permlane32_swap(__float_as_uint(x), __float_as_uint(x), false, false);
  return __uint_as_float(r[0]) + __uint_as_float(r[1]);
}
DI float halfmax(float x) {
  auto r = __builtin_amdgcn_permlane32_swap(__float_as_uint(x), __float_as_uint(x), false, false);
  return fmaxf(__uint_as_float(r[0]), __uint_as_float(r[1]));
}
DI float sigmoidf_(float x) { return __builtin_amdgcn_rcpf(1.0f + __builtin_amdgcn_exp2f(-LOG2E * x)); }
DI float siluf_(float x) { return x * sigmoidf_(x); }
DI void lds_barrier() { asm volatile("s_waitcnt lgkmcnt(0)\n\ts_barrier" ::: "memory"); }
typedef short v4i16_t __attribute__((ext_vector_type(4)));
DI s16x4 trread(LAS const char* p) { return __builtin_bit_cast(s16x4, __builtin_amdgcn_ds_read_tr16_b64_v4i16((LAS v4i16_t*)p)); }
DI void stg16(char* p, bf16x8 v) { __builtin_nontemporal_store(v, (bf16x8*)p); }
DI bf16x8 ldsfrag(LAS const char* p) { return *(LAS const bf16x8*)p; }

constexpr size_t WS_WT1 = 24u << 20, WS_WT3 = 26u << 20;
constexpr int NP1 = 11, NP3 = 12;
struct WPtrs { const char *w1a, *w1b, *wqkv, *wo, *pw1, *pw2, *w2a, *w2b; };
DI void ff_panel(const char* wa, const char* wb, int p, int w2, const char*& base, unsigned& ld) {
  const bool isb = (p == 2 || p == 4 || p >= 6);
  const int c = p < 2 ? p : (p == 3 ? 2 : (p == 5 ? 3 : (p == 2 ? 0 : (p == 4 ? 1 : p - 4))));
  if (isb) { base = wb + (size_t)c * 256 * 512 + w2 * 128; ld = 512; } else { base = wa + (size_t)(w2 + 4 * c) * 128; ld = 2048; }
}
__global__ __launch_bounds__(256) void prep_kernel(WPtrs wp, char* __restrict__ ws) {
  __shared__ __attribute__((aligned(16))) char sm[4 * 4096];
  const int lane = threadIdx.x & 63, wv = __builtin_amdgcn_readfirstlane(threadIdx.x >> 6);
  const int g = blockIdx.x * 4 + wv;
  const bool k3 = g >= 4 * NP1 * 8;
  const int gg = k3 ? g - 4 * NP1 * 8 : g, np = k3 ? NP3 : NP1;
  const int w2 = gg / (np * 8), p = (gg >> 3) % np, sl = gg & 7;
  const char* base; unsigned ld;
  if (!k3) {
    if (p < 8) ff_panel(wp.w1a, wp.w1b, p, w2, base, ld);
    else { base = wp.wqkv + (p - 8) * 512 + w2 * 128; ld = 1536; }
  } else {
    if (p == 0) { base = wp.wo + w2 * 128; ld = 512; }
    else if (p < 3) { base = wp.pw1 + (p - 1) * 512 + w2 * 128; ld = 1024; }
    else if (p == 3) { base = wp.pw2 + w2 * 128; ld = 512; }
    else ff_panel(wp.w2a, wp.w2b, p - 4, w2, base, ld);
  }
  u32x4 ct = {0u, 0u, 0u, 0u};
  {
    unsigned long long pc; int db, len;
    asm volatile("s_getpc_b64 %0\nzf_here:\n\ts_mov_b32 %1, zf_code_begin-zf_here\n\ts_mov_b32 %2, zf_code_end-zf_code_begin" : "=s"(pc), "=s"(db), "=s"(len));
    const unsigned long long cb = pc + (long long)db;
    const int off = ((blockIdx.x >> 3) * 4 + wv) * 1024 + lane * 16;
    if (len > 0 && len < 92 * 1024 && off + 16 <= len) asm volatile("global_load_dwordx4 %0, %1, %2" : "+v"(ct) : "v"(off & ~3), "s"(cb & ~3ull));
  }
  LAS char* my = (LAS char*)sm + wv * 4096;
  const char* src = base + (size_t)(sl * 32 + (lane >> 3)) * ld + (((lane & 7) ^ (((lane >> 4) & 1) << 2)) << 4);
  u32x4 t[4];
#pragma unroll
  for (int i = 0; i < 4; ++i) t[i] = *(const u32x4*)(src + (size_t)i * 8 * ld);
#pragma unroll
  for (int i = 0; i < 4; ++i) *(LAS u32x4*)(my + i * 1024 + lane * 16) = t[i];
  asm volatile("s_waitcnt lgkmcnt(0)" ::: "memory");
  const int h = lane >> 5, cb = (lane >> 4) & 1, q = (lane & 15) >> 2, pp = lane & 3;
  char* dst = ws + (k3 ? WS_WT3 : WS_WT1) + (size_t)((2 * w2) * np + p) * 16384 + sl * 2048 + lane * 16;
#pragma unroll
  for (int i = 0; i < 4; ++i) {
    LAS const char* rp = my + (4 * h + q) * 128 + (((i & 1) ^ (q >> 1)) * 64) + cb * 32 + pp * 8 + (i >> 1) * 2048;
    s16x4 l = trread(rp), hh = trread(rp + 1024);
    *(bf16x8*)(dst + (size_t)(i & 1) * np * 16384 + (i >> 1) * 1024) = __builtin_shufflevector(l, hh, 0, 1, 2, 3, 4, 5, 6, 7);
  }
  asm volatile("s_waitcnt vmcnt(0)" : "+v"(ct));
}

struct Stream {
  const char* p;
  bf16x8 wf[2][16];
  DI void start(const char* base) {
    p = base;
#pragma unroll
    for (int i = 0; i < 32; ++i) wf[i >> 4][i & 15] = *(const bf16x8*)(p + i * 1024);
  }
};
struct NoWork { DI void init() {} DI void step(int) {} };
struct SiluWork {
  LAS char* p;
  u32x2 raw; float x[4], e[4];
  DI void init() { raw = *(LAS u32x2*)p; }
  DI void step(int ks) {
    const int g = ks >> 2, i = ks & 3;
    if (i == 0) {
      x[0] = bflo(raw[0]); x[1] = bfhi(raw[0]); x[2] = bflo(raw[1]); x[3] = bfhi(raw[1]);
#pragma unroll
      for (int j = 0; j < 4; ++j) e[j] = -LOG2E * x[j];
      asm volatile("" : "+v"(e[0]), "+v"(e[1]), "+v"(e[2]), "+v"(e[3]), "+v"(x[0]), "+v"(x[1]), "+v"(x[2]), "+v"(x[3]));
      if (g < 3) raw = *(LAS u32x2*)(p + ((g + 1) >> 1) * 1024 + ((g + 1) & 1) * 8);
    } else if (i == 1) {
#pragma unroll
      for (int j = 0; j < 4; ++j) e[j] = __builtin_amdgcn_exp2f(e[j]);
      asm volatile("" : "+v"(e[0]), "+v"(e[1]), "+v"(e[2]), "+v"(e[3]));
    } else if (i == 2) {
#pragma unroll
      for (int j = 0; j < 4; ++j) e[j] = __builtin_amdgcn_rcpf(1.0f + e[j]);
      asm volatile("" : "+v"(e[0]), "+v"(e[1]), "+v"(e[2]), "+v"(e[3]));
    } else {
      *(LAS u32x2*)(p + (g >> 1) * 1024 + (g & 1) * 8) = (u32x2){pk2(x[0] * e[0], x[1] * e[1]), pk2(x[2] * e[2], x[3] * e[3])};
    }
  }
};
template <int NB, bool SWAP, int P, int NP, class W>
DI void gemm_k256(Stream& st, f32x16 (&acc)[NB], LAS const char* bl, W work) {
  constexpr bool REFILL = P + 2 < NP;
  bf16x8 bq[3][NB];
#pragma unroll
  for (int nb = 0; nb < NB; ++nb) { bq[0][nb] = ldsfrag(bl + nb * 16384); bq[1][nb] = ldsfrag(bl + nb * 16384 + 1024); }
  work.init();
#pragma unroll
  for (int ks = 0; ks < 16; ++ks) {
    SB();
#pragma unroll
    for (int nb = 0; nb < NB; ++nb)
      acc[nb] = SWAP ? MFMA(bq[ks % 3][nb], st.wf[P & 1][ks], acc[nb]) : MFMA(st.wf[P & 1][ks], bq[ks % 3][nb], acc[nb]);
    if (ks < 14) {
#pragma unroll
      for (int nb = 0; nb < NB; ++nb) bq[(ks + 2) % 3][nb] = ldsfrag(bl + nb * 16384 + (ks + 2) * 1024);
    }
    if (REFILL && ks > 0) st.wf[P & 1][ks - 1] = *(const bf16x8*)(st.p + (P + 2) * 16384 + (ks - 1) * 1024);
    work.step(ks);
  }
  SB();
  if (REFILL) st.wf[P & 1][15] = *(const bf16x8*)(st.p + (P + 2) * 16384 + 15 * 1024);
}

constexpr int L_X = 0, L_Y = L_X + 16384, L_Z = L_Y + 16384;
constexpr int L_STAT = L_Z + 16384 + 9216;
constexpr int GROW = 144, GSZ = 36 * GROW;
constexpr int L_DW = L_STAT + 4096;
constexpr int L_TOTAL = L_DW + 5120;

DI float sum16(const f32x16& t) {
  return (((t[0] + t[1]) + (t[2] + t[3])) + ((t[4] + t[5]) + (t[6] + t[7]))) + (((t[8] + t[9]) + (t[10] + t[11])) + ((t[12] + t[13]) + (t[14] + t[15])));
}
template <int NB>
DI void ln_stats(const f32x16 (&v)[NB], LAS char* lds, int w, int lane, float (&mean)[NB], float (&rstd)[NB]) {
  const int r = lane & 31;
#pragma unroll
  for (int nb = 0; nb < NB; ++nb) {
    const float mw = halfsum(sum16(v[nb])) * (1.0f / 32.0f);
    const f32x16 d = v[nb] - mw;
    const float m2 = halfsum(sum16(d * d));
    if (lane < 32) *(LAS f32x2*)(lds + L_STAT + ((nb * 32 + r) * 8 + w) * 8) = (f32x2){mw, m2};
  }
  lds_barrier();
#pragma unroll
  for (int nb = 0; nb < NB; ++nb) {
    f32x4 s[4];
#pragma unroll
    for (int i = 0; i < 4; ++i) s[i] = *(LAS f32x4*)(lds + L_STAT + (nb * 32 + r) * 64 + i * 16);
    const float m = 0.125f * (((s[0][0] + s[0][2]) + (s[1][0] + s[1][2])) + ((s[2][0] + s[2][2]) + (s[3][0] + s[3][2])));
    float M2 = 0.f;
#pragma unroll
    for (int i = 0; i < 4; ++i) { const float e0 = s[i][0] - m, e1 = s[i][2] - m; M2 += s[i][1] + s[i][3] + 32.0f * (e0 * e0 + e1 * e1); }
    mean[nb] = m; rstd[nb] = __builtin_amdgcn_rsqf(M2 * (1.0f / 256.0f) + LN_EPS);
  }
}
template <int NB>
DI void ln_to_frags(const f32x16 (&v)[NB], LAS char* lds, int w, int lane) {
  float mean[NB], rstd[NB];
  ln_stats<NB>(v, lds, w, lane, mean, rstd);
  LAS char* fb = lds + L_X + w * 2048 + lane * 16;
#pragma unroll
  for (int nb = 0; nb < NB; ++nb) {
    const f32x16 n = (v[nb] - mean[nb]) * rstd[nb];
#pragma unroll
    for (int s = 0; s < 2; ++s) *(LAS bf16x8*)(fb + nb * 16384 + s * 1024) = pack8(n, s);
  }
  lds_barrier();
}

template <int P0, int NP>
DI void ff_block(Stream& st, LAS char* lds, int w, int lane, f32x16 (&out)[1]) {
  out[0] = f32x16{};
  LAS char* xb = lds + L_X + lane * 16;
  LAS char* yb = lds + L_Y + lane * 16;
  LAS char* zb = lds + L_Z + lane * 16;
  auto put = [&](LAS char* dst, const f32x16& hv) { *(LAS bf16x8*)(dst + w * 2048) = pack8(hv, 0); *(LAS bf16x8*)(dst + w * 2048 + 1024) = pack8(hv, 1); };
  f32x16 hn[1];
  hn[0] = f32x16{}; gemm_k256<1, false, P0 + 0, NP>(st, hn, xb, NoWork{});
  put(yb, hn[0]);
  hn[0] = f32x16{}; gemm_k256<1, false, P0 + 1, NP>(st, hn, xb, SiluWork{yb + w * 2048});
  lds_barrier(); put(zb, hn[0]);
  gemm_k256<1, false, P0 + 2, NP>(st, out, yb, NoWork{});
  hn[0] = f32x16{}; gemm_k256<1, false, P0 + 3, NP>(st, hn, xb, SiluWork{zb + w * 2048});
  lds_barrier(); put(yb, hn[0]);
  gemm_k256<1, false, P0 + 4, NP>(st, out, zb, NoWork{});
  hn[0] = f32x16{}; gemm_k256<1, false, P0 + 5, NP>(st, hn, xb, SiluWork{yb + w * 2048});
  lds_barrier(); put(zb, hn[0]);
  gemm_k256<1, false, P0 + 6, NP>(st, out, yb, SiluWork{zb + w * 2048});
  lds_barrier();
  gemm_k256<1, false, P0 + 7, NP>(st, out, zb, NoWork{});
}

DI void l2_warm(const char* p, int kb_total, int idx, int lane, u32x4& t) {
  const int per = (kb_total + 247) / 248;
  int b = idx * per, e = b + per; if (e > kb_total) e = kb_total;
  for (int i = b; i < e; ++i)
    asm volatile("global_load_dwordx4 %0, %1, %2" : "+v"(t) : "v"(lane * 16 + i * 1024), "s"(p));
}

__global__ __launch_bounds__(512, 2) void k1_kernel(const char* __restrict__ x, char* __restrict__ ws) {
  extern __shared__ __attribute__((aligned(16))) char smem[];
  LAS char* lds = (LAS char*)smem;
  const int lane = threadIdx.x & 63, w = __builtin_amdgcn_readfirstlane(threadIdx.x >> 6), r = lane & 31, h = lane >> 5;
  const int tb = blockIdx.x;
  asm volatile("zf_code_begin:");
  STAMP(0, 0);
  f32x16 xv[1];
  Stream st;
  u32x4 wt = {0u, 0u, 0u, 0u};
  {
    const char* xrow = x + (size_t)(tb * 32 + r) * 512 + (w * 32 + 4 * h) * 2;
    u32x2 u[4];
#pragma unroll
    for (int g = 0; g < 4; ++g) u[g] = *(const u32x2*)(xrow + g * 16);
    SB();
    st.start(ws + WS_WT1 + (size_t)w * NP1 * 16384 + lane * 16);
    SB();
#pragma unroll
    for (int g = 0; g < 4; ++g) {
      xv[0][4 * g] = bflo(u[g][0]); xv[0][4 * g + 1] = bfhi(u[g][0]); xv[0][4 * g + 2] = bflo(u[g][1]); xv[0][4 * g + 3] = bfhi(u[g][1]);
    }
  }
  STAMP(0, 1);
  ln_to_frags<1>(xv, lds, w, lane);
  STAMP(0, 2);
  f32x16 acc[1];
  ff_block<0, NP1>(st, lds, w, lane, acc);
  asm volatile("" :: "v"(wt));
  STAMP(0, 3);
#pragma unroll
  for (int i = 0; i < 16; ++i) xv[0][i] = rbf(xv[0][i] + 0.5f * acc[0][i]);
#pragma unroll
  for (int s = 0; s < 2; ++s) stg16((char*)(ws + WS_X1F + ((size_t)tb * 16 + 2 * w + s) * 1024 + lane * 16), pack8(xv[0], s));
  ln_to_frags<1>(xv, lds, w, lane);
  STAMP(0, 4);
  const int b = tb / NBLK, blk = tb - b * NBLK, bh = b * 4 + (w >> 1);
  char* kvdst = ws + WS_KVF + (size_t)(bh * NBLK + blk) * 8192 + (w & 1) * 2048 + lane * 16;
  {
    f32x16 a[1]; a[0] = f32x16{};
    gemm_k256<1, false, 8, NP1>(st, a, lds + L_X + lane * 16, NoWork{});
    a[0] = a[0] * QSCALE;
    char* dst = ws + WS_QF + (size_t)(bh * NBLK + blk) * 4096 + (w & 1) * 2048 + lane * 16;
    stg16((char*)(dst), pack8(a[0], 0)); stg16((char*)(dst + 1024), pack8(a[0], 1));
  }
  {
    f32x16 a[1]; a[0] = f32x16{};
    gemm_k256<1, false, 9, NP1>(st, a, lds + L_X + lane * 16, NoWork{});
    stg16((char*)(kvdst), pack8(a[0], 0)); stg16((char*)(kvdst + 1024), pack8(a[0], 1));
  }
  STAMP(0, 5);
  {
    f32x16 a[1]; a[0] = f32x16{};
    gemm_k256<1, true, 10, NP1>(st, a, lds + L_X + lane * 16, NoWork{});
    stg16((char*)(kvdst + 4096), pack8(a[0], 0)); stg16((char*)(kvdst + 5120), pack8(a[0], 1));
  }
  STAMP(0, 29);
}

constexpr int A_KG = 2, A_SBLK = A_KG * 2, A_HST = A_SBLK * 4096, A_NST = (NBLK + A_SBLK - 1) / A_SBLK, A_NFULL = NBLK / A_SBLK;
constexpr int A_VOFF = 3 * A_HST, A_LDS = 6 * A_HST;
__global__ __launch_bounds__(256 * A_KG, A_KG) void attn_kernel(char* __restrict__ ws) {
  extern __shared__ __attribute__((aligned(16))) char smem[];
  LAS char* lds = (LAS char*)smem;
  const int lane = threadIdx.x & 63, w = __builtin_amdgcn_readfirstlane(threadIdx.x >> 6);
  const int bh = blockIdx.x & 7, qg = blockIdx.x >> 3, qi = w & 3, kg = w >> 2;
  const int qb_raw = qg * 4 + qi, qb = qb_raw < NBLK ? qb_raw : NBLK - 1;
  STAMP(1, 0);
  const char* kv = ws + WS_KVF + (size_t)bh * NBLK * 8192;
  const unsigned lds0 = (unsigned)(uintptr_t)lds;
  const unsigned voff = (w >> 1) * 8192 + (w & 1) * 2048 + lane * 16;
  auto dma2 = [&](int st, int isv, unsigned ldsoff) {
    asm volatile("s_mov_b32 m0, %2\n\ts_nop 0\n\tglobal_load_lds_dwordx4 %0, %1\n\tglobal_load_lds_dwordx4 %0, %1 offset:1024"
                 :: "v"(voff), "s"(kv + (size_t)st * (2 * A_HST) + isv * 4096), "s"(lds0 + ldsoff + w * 2048));
  };
  dma2(0, 0, 0); dma2(1, 0, A_HST); dma2(0, 1, A_VOFF); dma2(2, 0, 2 * A_HST); dma2(1, 1, A_VOFF + A_HST);
  bf16x8 qf[4];
#pragma unroll
  for (int f = 0; f < 4; ++f) qf[f] = *(const bf16x8*)(ws + WS_QF + ((size_t)(bh * NBLK + qb) * 4 + f) * 1024 + lane * 16);
#pragma unroll
  for (int f = 0; f < 4; ++f) asm volatile("" : "+v"(qf[f]));
  u32x4 wt = {0u, 0u, 0u, 0u};
  const int rem = NBLK - A_NFULL * A_SBLK - kg * 2;
  const int ntail = rem < 0 ? 0 : (rem > 2 ? 2 : rem);
  LAS const char* base = lds + kg * 8192 + lane * 16;
  f32x16 o0 = f32x16{}, o1 = f32x16{}, negr, sa, sb;
  f32x4 lacc = {0.f, 0.f, 0.f, 0.f};
  bf16x8 sel;
  { const short one = (((lane >> 4) ^ lane) & 1) ? (short)0 : (short)0x3f80;
#pragma unroll
    for (int j = 0; j < 8; ++j) sel[j] = one; }
  bf16x8 kf[4], vf[4], kg2[4], vg2[4]; unsigned pa[8], pb[8];
#pragma unroll
  for (int i = 0; i < 8; ++i) pa[i] = 0u;
  float ref;
  asm volatile("s_waitcnt vmcnt(0)\n\ts_barrier" : "+v"(wt) :: "memory");
  {
#pragma unroll
    for (int f = 0; f < 4; ++f) kf[f] = ldsfrag(base + f * 1024);
    f32x16 z = f32x16{};
    sa = MFMA(kf[0], qf[0], z); sa = MFMA(kf[1], qf[1], sa); sa = MFMA(kf[2], qf[2], sa); sa = MFMA(kf[3], qf[3], sa);
    float mx = sa[0];
#pragma unroll
    for (int i = 1; i < 16; ++i) mx = fmaxf(mx, sa[i]);
    ref = halfmax(mx) + 7.0f;
#pragma unroll
    for (int i = 0; i < 16; ++i) { sa[i] -= ref; negr[i] = -ref; }
#pragma unroll
    for (int f = 0; f < 4; ++f) { kf[f] = ldsfrag(base + 4096 + f * 1024); vf[f] = bf16x8{}; }
  }
#define SLOT3(MF, RF, i) do { MF; RF; e[2*(i)] = __builtin_amdgcn_exp2f(SC_[2*(i)]); e[2*(i)+1] = __builtin_amdgcn_exp2f(SC_[2*(i)+1]); \
    if ((i) > 0) PN_[(i)-1] = pk2(e[2*(i)-2], e[2*(i)-1]); SB(); } while (0)
  auto iter = [&](f32x16& SC_, f32x16& SN_, unsigned (&PP_)[8], unsigned (&PN_)[8], bf16x8 (&KC)[4], bf16x8 (&VC)[4], bf16x8 (&KN)[4], bf16x8 (&VN)[4], LAS const char* kn, LAS const char* vc) {
    float e[16];
    const bf16x8 p0 = __builtin_bit_cast(bf16x8, (u32x4){PP_[0], PP_[1], PP_[2], PP_[3]}), p1 = __builtin_bit_cast(bf16x8, (u32x4){PP_[4], PP_[5], PP_[6], PP_[7]});
    SB();
    SLOT3(SN_ = MFMA(KC[0], qf[0], negr), KN[0] = ldsfrag(kn), 0);
    SLOT3(o0 = MFMA(VC[0], p0, o0), VN[0] = ldsfrag(vc), 1);
    SLOT3(SN_ = MFMA(KC[1], qf[1], SN_), KN[1] = ldsfrag(kn + 1024), 2);
    SLOT3(o1 = MFMA(VC[2], p0, o1); lacc = MFMA16(sel, p0, lacc), VN[2] = ldsfrag(vc + 2048), 3);
    SLOT3(SN_ = MFMA(KC[2], qf[2], SN_), KN[2] = ldsfrag(kn + 2048), 4);
    SLOT3(o0 = MFMA(VC[1], p1, o0), VN[1] = ldsfrag(vc + 1024), 5);
    SLOT3(SN_ = MFMA(KC[3], qf[3], SN_), KN[3] = ldsfrag(kn + 3072), 6);
    SLOT3(o1 = MFMA(VC[3], p1, o1); lacc = MFMA16(sel, p1, lacc), VN[3] = ldsfrag(vc + 3072), 7);
    PN_[7] = pk2(e[14], e[15]);
    const unsigned orall = (PN_[0] | PN_[1] | PN_[2]) | (PN_[3] | PN_[4] | PN_[5]) | (PN_[6] | PN_[7]);
    if (__builtin_expect(__any((orall & 0x40004000u) != 0u), 0)) {
      float mx = SC_[0];
#pragma unroll
      for (int i = 1; i < 16; ++i) mx = fmaxf(mx, SC_[i]);
      mx = halfmax(mx);
      const float delta = (mx >= 0.5f) ? mx + 7.0f : 0.0f;
      const float alpha = __builtin_amdgcn_exp2f(-delta);
      const float alo = __shfl_xor(alpha, 16);
      const float aA = (lane & 16) ? alo : alpha, aB = (lane & 16) ? alpha : alo;
      lacc = lacc * (f32x4){aA, aB, aA, aB};
      o0 = o0 * alpha; o1 = o1 * alpha; negr = negr - delta; SN_ = SN_ - delta;
      asm volatile("" : "+v"(o0), "+v"(o1), "+v"(negr), "+v"(SN_), "+v"(lacc));
      ref = -negr[0];
#pragma unroll
      for (int i = 0; i < 8; ++i) PN_[i] = pk2(__builtin_amdgcn_exp2f(SC_[2*i] - delta), __builtin_amdgcn_exp2f(SC_[2*i+1] - delta));
    }
  };
  STAMP(1, 1);
  int r0 = 0, r1 = A_HST, r2 = 2 * A_HST;
  for (int st = 0; st < A_NFULL; ++st) {
    asm volatile("s_waitcnt vmcnt(4) lgkmcnt(0)\n\ts_barrier" ::: "memory");
    dma2(st + 3 < A_NST ? st + 3 : A_NST - 1, 0, r0); dma2(st + 2 < A_NST ? st + 2 : A_NST - 1, 1, A_VOFF + r2);
    iter(sa, sb, pa, pb, kf, vf, kg2, vg2, base + r1, base + A_VOFF + r0);
    iter(sb, sa, pb, pa, kg2, vg2, kf, vf, base + r1 + 4096, base + A_VOFF + r0 + 4096);
    const int t = r0; r0 = r1; r1 = r2; r2 = t;
  }
  asm volatile("s_waitcnt vmcnt(0) lgkmcnt(0)\n\ts_barrier" ::: "memory");
  if (ntail > 0) {
    iter(sa, sb, pa, pb, kf, vf, kg2, vg2, base + r1, base + A_VOFF + r0);
    if (ntail > 1) iter(sb, sa, pb, pa, kg2, vg2, kf, vf, base + r1 + 4096, base + A_VOFF + r0 + 4096);
    else {
#pragma unroll
      for (int i = 0; i < 8; ++i) pa[i] = pb[i];
#pragma unroll
      for (int i = 0; i < 4; ++i) vf[i] = vg2[i];
    }
  }
  {
    const bf16x8 p0 = __builtin_bit_cast(bf16x8, (u32x4){pa[0], pa[1], pa[2], pa[3]}), p1 = __builtin_bit_cast(bf16x8, (u32x4){pa[4], pa[5], pa[6], pa[7]});
    o0 = MFMA(vf[0], p0, o0); o1 = MFMA(vf[2], p0, o1); o0 = MFMA(vf[1], p1, o0); o1 = MFMA(vf[3], p1, o1);
    lacc = MFMA16(sel, p0, lacc); lacc = MFMA16(sel, p1, lacc);
  }
  float lsum = (lane & 16) ? lacc[1] : lacc[0];
  STAMP(1, 2);
  lds_barrier();
  if (kg > 0) {
    LAS char* cb = lds + ((kg - 1) * 4 + qi) * 9216 + lane * 16;
#pragma unroll
    for (int g = 0; g < 4; ++g) {
      *(LAS f32x4*)(cb + g * 1024) = (f32x4){o0[4 * g], o0[4 * g + 1], o0[4 * g + 2], o0[4 * g + 3]};
      *(LAS f32x4*)(cb + 4096 + g * 1024) = (f32x4){o1[4 * g], o1[4 * g + 1], o1[4 * g + 2], o1[4 * g + 3]};
    }
    *(LAS f32x2*)(lds + ((kg - 1) * 4 + qi) * 9216 + 8192 + lane * 8) = (f32x2){ref, lsum};
  }
  lds_barrier();
  if (kg == 0 && qb_raw < NBLK) {
#pragma unroll
    for (int k = 1; k < A_KG; ++k) {
      LAS char* cb = lds + ((k - 1) * 4 + qi) * 9216 + lane * 16;
      const f32x2 rl = *(LAS f32x2*)(lds + ((k - 1) * 4 + qi) * 9216 + 8192 + lane * 8);
      const float m = fmaxf(ref, rl[0]);
      const float c0 = __builtin_amdgcn_exp2f(ref - m), c1 = __builtin_amdgcn_exp2f(rl[0] - m);
      lsum = lsum * c0 + rl[1] * c1; ref = m;
#pragma unroll
      for (int g = 0; g < 4; ++g) {
        const f32x4 u0 = *(LAS f32x4*)(cb + g * 1024), u1 = *(LAS f32x4*)(cb + 4096 + g * 1024);
#pragma unroll
        for (int j = 0; j < 4; ++j) { o0[4 * g + j] = o0[4 * g + j] * c0 + u0[j] * c1; o1[4 * g + j] = o1[4 * g + j] * c0 + u1[j] * c1; }
      }
    }
    const float inv = 1.0f / lsum;
#pragma unroll
    for (int i = 0; i < 16; ++i) { o0[i] *= inv; o1[i] *= inv; }
    char* dst = ws + WS_ATTF + ((size_t)((bh >> 2) * NBLK + qb) * 16 + (bh & 3) * 4) * 1024 + lane * 16;
    stg16((char*)(dst), pack8(o0, 0)); stg16((char*)(dst + 1024), pack8(o0, 1));
    stg16((char*)(dst + 2048), pack8(o1, 0)); stg16((char*)(dst + 3072), pack8(o1, 1));
  }
  STAMP(1, 29);
}

__global__ __launch_bounds__(512, 2) void k3_kernel(const char* __restrict__ dw, const char* __restrict__ ws, char* __restrict__ out) {
  extern __shared__ __attribute__((aligned(16))) char smem[];
  LAS char* lds = (LAS char*)smem;
  const int lane = threadIdx.x & 63, w = __builtin_amdgcn_readfirstlane(threadIdx.x >> 6), r = lane & 31, h = lane >> 5;
  const int tb = blockIdx.x, b = tb / NBLK, t0 = (tb - b * NBLK) * 32;
  STAMP(2, 0);
  Stream st;
  const int hc = r & 3, ht = (hc < 2) ? t0 - 2 + hc : t0 + 30 + hc;
  const bool hvalid = ht >= 0 && ht < TSEQ;
  const int hg = b * TSEQ + (ht < 0 ? 0 : (ht >= TSEQ ? TSEQ - 1 : ht));
  const unsigned hoff = (unsigned)(hg >> 5) * 16384 + ((hg & 31) + 32 * h) * 16 + w * 2048;
  const unsigned moff = (unsigned)tb * 16384 + lane * 16 + w * 2048;
  {
    const unsigned ly = (unsigned)(uintptr_t)(lds + L_Y + w * 2048);
    asm volatile("s_mov_b32 m0, %3\n\ts_nop 0\n\tglobal_load_lds_dwordx4 %0, %2\n\tglobal_load_lds_dwordx4 %0, %2 offset:1024\n\t"
                 "s_mov_b32 m0, %4\n\ts_nop 0\n\tglobal_load_lds_dwordx4 %1, %2\n\tglobal_load_lds_dwordx4 %1, %2 offset:1024"
                 :: "v"(moff), "v"(hoff), "s"(ws + WS_ATTF), "s"(ly), "s"(ly + 16384));
  }
  u32x4 xr[2][2];
#pragma unroll
  for (int f = 0; f < 2; ++f) {
    xr[0][f] = *(const u32x4*)(ws + WS_X1F + moff + f * 1024);
    xr[1][f] = *(const u32x4*)(ws + WS_X1F + hoff + f * 1024);
  }
  const u32x4 dwv = *(const u32x4*)(dw + (threadIdx.x < 160 ? threadIdx.x : 159) * 16);
  SB();
  st.start(ws + WS_WT3 + (size_t)w * NP3 * 16384 + lane * 16);
  SB();
  if (threadIdx.x < 160) {
    *(LAS f32x4*)(lds + L_DW + threadIdx.x * 32) = (f32x4){bflo(dwv[0]), bfhi(dwv[0]), bflo(dwv[1]), bfhi(dwv[1])};
    *(LAS f32x4*)(lds + L_DW + threadIdx.x * 32 + 16) = (f32x4){bflo(dwv[2]), bfhi(dwv[2]), bflo(dwv[3]), bfhi(dwv[3])};
  }
  asm volatile("s_waitcnt vmcnt(32)" ::: "memory");
  lds_barrier();
  STAMP(2, 1);
  f32x16 xv[2];
#pragma unroll
  for (int nb = 0; nb < 2; ++nb) { unpack8(xr[nb][0], xv[nb], 0); unpack8(xr[nb][1], xv[nb], 1); }
  gemm_k256<2, false, 0, NP3>(st, xv, lds + L_Y + lane * 16, NoWork{});
  STAMP(2, 2);
  ln_to_frags<2>(xv, lds, w, lane);
  STAMP(2, 3);
  {
    f32x16 a[2], g[2];
#pragma unroll
    for (int nb = 0; nb < 2; ++nb) { a[nb] = f32x16{}; g[nb] = f32x16{}; }
    gemm_k256<2, false, 1, NP3>(st, a, lds + L_X + lane * 16, NoWork{});
    gemm_k256<2, false, 2, NP3>(st, g, lds + L_X + lane * 16, NoWork{});
    STAMP(2, 4);
    LAS char* G = lds + L_Y + w * GSZ;
    lds_barrier();
#pragma unroll
    for (int nb = 0; nb < 2; ++nb) {
      const int row = nb == 0 ? r + 2 : (hc < 2 ? hc : 32 + hc);
      const bool wr = nb == 0 || r < 4;
      const float keep = (nb == 0 || hvalid) ? 1.0f : 0.0f;
#pragma unroll
      for (int q = 0; q < 4; ++q) {
        f32x4 e;
#pragma unroll
        for (int j = 0; j < 4; ++j) e[j] = keep * a[nb][4 * q + j] * sigmoidf_(g[nb][4 * q + j]);
        if (wr) *(LAS f32x4*)(G + row * GROW + (8 * q + 4 * h) * 4) = e;
      }
    }
    asm volatile("s_waitcnt lgkmcnt(0)" ::: "memory");
    f32x16 c;
#pragma unroll
    for (int q = 0; q < 4; ++q) {
      f32x4 e = {0.f, 0.f, 0.f, 0.f};
#pragma unroll
      for (int i = 0; i < 5; ++i)
        e += *(LAS f32x4*)(G + (r + i) * GROW + (8 * q + 4 * h) * 4) * *(LAS f32x4*)(lds + L_DW + i * 1024 + (w * 32 + 8 * q + 4 * h) * 4);
#pragma unroll
      for (int j = 0; j < 4; ++j) c[4 * q + j] = siluf_(e[j]);
    }
#pragma unroll
    for (int s = 0; s < 2; ++s) *(LAS bf16x8*)(lds + L_X + (2 * w + s) * 1024 + lane * 16) = pack8(c, s);
    lds_barrier();
  }
  STAMP(2, 5);
  f32x16 x3[1];
  x3[0] = xv[0];
  gemm_k256<1, false, 3, NP3>(st, x3, lds + L_X + lane * 16, NoWork{});
  STAMP(2, 6);
  ln_to_frags<1>(x3, lds, w, lane);
  STAMP(2, 7);
  f32x16 acc[1];
  ff_block<4, NP3>(st, lds, w, lane, acc);
  x3[0] += 0.5f * acc[0];
  STAMP(2, 8);
  float mean[1], rstd[1];
  ln_stats<1>(x3, lds, w, lane, mean, rstd);
  char* orow = out + (size_t)(tb * 32 + r) * 512 + (w * 32 + 4 * h) * 2;
  const f32x16 n = (x3[0] - mean[0]) * rstd[0];
#pragma unroll
  for (int q = 0; q < 4; ++q) *(u32x2*)(orow + q * 16) = (u32x2){pk2(n[4 * q], n[4 * q + 1]), pk2(n[4 * q + 2], n[4 * q + 3])};
  STAMP(2, 29);
  asm volatile("zf_code_end:");
}

extern "C" void kernel_launch(void* const* d_in, const int* in_sizes, int n_in, void* d_out, int out_size, void* d_ws, size_t ws_size,
                              hipStream_t stream) {
  const char* x = (const char*)d_in[0];
  const char* w1a = (const char*)d_in[1];
  const char* w1b = (const char*)d_in[2];
  const char* wqkv = (const char*)d_in[3];
  const char* wo = (const char*)d_in[4];
  const char* pw1 = (const char*)d_in[5];
  const char* dw = (const char*)d_in[6];
  const char* pw2 = (const char*)d_in[7];
  const char* w2a = (const char*)d_in[8];
  const char* w2b = (const char*)d_in[9];
  char* ws = (char*)d_ws;
  (void)hipFuncSetAttribute((const void*)k1_kernel, hipFuncAttributeMaxDynamicSharedMemorySize, L_TOTAL);
  (void)hipFuncSetAttribute((const void*)attn_kernel, hipFuncAttributeMaxDynamicSharedMemorySize, A_LDS);
  (void)hipFuncSetAttribute((const void*)k3_kernel, hipFuncAttributeMaxDynamicSharedMemorySize, L_TOTAL);
  WPtrs wp{w1a, w1b, wqkv, wo, pw1, pw2, w2a, w2b};
  prep_kernel<<<(NP1 + NP3) * 8, 256, 0, stream>>>(wp, ws);
  k1_kernel<<<NTB, 512, L_TOTAL, stream>>>(x, ws);
  attn_kernel<<<256, 256 * A_KG, A_LDS, stream>>>(ws);
  k3_kernel<<<NTB, 512, L_TOTAL, stream>>>(dw, ws, (char*)d_out);
}
```

```cpp
#include <hip/hip_runtime.h>
#include <hip/hip_bf16.h>
#include <stdint.h>

typedef short bf16x8 __attribute__((ext_vector_type(8)));
typedef short s16x4 __attribute__((ext_vector_type(4)));
typedef float f32x16 __attribute__((ext_vector_type(16)));
typedef float f32x2 __attribute__((ext_vector_type(2)));
typedef __bf16 bf16x2_t __attribute__((ext_vector_type(2)));
typedef unsigned u32x4 __attribute__((ext_vector_type(4)));
typedef unsigned u32x2 __attribute__((ext_vector_type(2)));
#define LAS __attribute__((address_space(3)))
#define DI __device__ __forceinline__
#define MFMA(a, b, c) __builtin_amdgcn_mfma_f32_32x32x16_bf16((a), (b), (c), 0, 0, 0)
#define MFMA16(a, b, c) __builtin_amdgcn_mfma_f32_16x16x32_bf16((a), (b), (c), 0, 0, 0)
#define SB() __builtin_amdgcn_sched_barrier(0)
#define STAMP(k, i) do {} while (0)
typedef float f32x4 __attribute__((ext_vector_type(4)));

constexpr int TSEQ = 4000, NBLK = 125, NTB = 250;
constexpr size_t WS_X1F = 0;
constexpr size_t WS_QF = 4u << 20;
constexpr size_t WS_KVF = 8u << 20;
constexpr size_t WS_ATTF = 17u << 20;
constexpr size_t WS_FLAG = 22u << 20;
constexpr float LN_EPS = 1e-5f;
constexpr float LOG2E = 1.4426950408889634f;
constexpr float QSCALE = 0.125f * LOG2E;

DI unsigned pk2(float a, float b) { f32x2 v = {a, b}; return __builtin_bit_cast(unsigned, __builtin_convertvector(v, bf16x2_t)); }
DI float bflo(unsigned u) { return __uint_as_float(u << 16); }
DI float bfhi(unsigned u) { return __uint_as_float(u & 0xffff0000u); }
DI float rbf(float a) { return bflo(pk2(a, a)); }
DI bf16x8 pack8(const f32x16& v, int s) {
  u32x4 p = {pk2(v[8 * s], v[8 * s + 1]), pk2(v[8 * s + 2], v[8 * s + 3]), pk2(v[8 * s + 4], v[8 * s + 5]), pk2(v[8 * s + 6], v[8 * s + 7])};
  return __builtin_bit_cast(bf16x8, p);
}
DI void unpack8(u32x4 p, f32x16& v, int s) {
#pragma unroll
  for (int i = 0; i < 4; ++i) { v[8 * s + 2 * i] = bflo(p[i]); v[8 * s + 2 * i + 1] = bfhi(p[i]); }
}
DI float halfsum(float x) {
  auto r = __builtin_amdgcn_permlane32_swap(__float_as_uint(x), __float_as_uint(x), false, false);
  return __uint_as_float(r[0]) + __uint_as_float(r[1]);
}
DI float halfmax(float x) {
  auto r = __builtin_amdgcn_permlane32_swap(__float_as_uint(x), __float_as_uint(x), false, false);
  return fmaxf(__uint_as_float(r[0]), __uint_as_float(r[1]));
}
DI float sigmoidf_(float x) { return __builtin_amdgcn_rcpf(1.0f + __builtin_amdgcn_exp2f(-LOG2E * x)); }
DI float siluf_(float x) { return x * sigmoidf_(x); }
DI void lds_barrier() { asm volatile("s_waitcnt lgkmcnt(0)\n\ts_barrier" ::: "memory"); }
typedef short v4i16_t __attribute__((ext_vector_type(4)));
DI s16x4 trread(LAS const char* p) { return __builtin_bit_cast(s16x4, __builtin_amdgcn_ds_read_tr16_b64_v4i16((LAS v4i16_t*)p)); }
DI void stg16(char* p, bf16x8 v) { __builtin_nontemporal_store(v, (bf16x8*)p); }
DI bf16x8 ldsfrag(LAS const char* p) { return *(LAS const bf16x8*)p; }

constexpr size_t WS_WT1 = 24u << 20, WS_WT3 = 26u << 20;
constexpr int NP1 = 11, NP3 = 12;
struct WPtrs { const char *w1a, *w1b, *wqkv, *wo, *pw1, *pw2, *w2a, *w2b; };
DI void ff_panel(const char* wa, const char* wb, int p, int w2, const char*& base, unsigned& ld) {
  const bool isb = (p == 2 || p == 4 || p >= 6);
  const int c = p < 2 ? p : (p == 3 ? 2 : (p == 5 ? 3 : (p == 2 ? 0 : (p == 4 ? 1 : p - 4))));
  if (isb) { base = wb + (size_t)c * 256 * 512 + w2 * 128; ld = 512; } else { base = wa + (size_t)(w2 + 4 * c) * 128; ld = 2048; }
}
__global__ __launch_bounds__(256) void prep_kernel(WPtrs wp, char* __restrict__ ws) {
  __shared__ __attribute__((aligned(16))) char sm[4 * 4096];
  const int lane = threadIdx.x & 63, wv = __builtin_amdgcn_readfirstlane(threadIdx.x >> 6);
  const int g = blockIdx.x * 4 + wv;
  const bool k3 = g >= 4 * NP1 * 8;
  const int gg = k3 ? g - 4 * NP1 * 8 : g, np = k3 ? NP3 : NP1;
  const int w2 = gg / (np * 8), p = (gg >> 3) % np, sl = gg & 7;
  const char* base; unsigned ld;
  if (!k3) {
    if (p < 8) ff_panel(wp.w1a, wp.w1b, p, w2, base, ld);
    else { base = wp.wqkv + (p - 8) * 512 + w2 * 128; ld = 1536; }
  } else {
    if (p == 0) { base = wp.wo + w2 * 128; ld = 512; }
    else if (p < 3) { base = wp.pw1 + (p - 1) * 512 + w2 * 128; ld = 1024; }
    else if (p == 3) { base = wp.pw2 + w2 * 128; ld = 512; }
    else ff_panel(wp.w2a, wp.w2b, p - 4, w2, base, ld);
  }
  LAS char* my = (LAS char*)sm + wv * 4096;
  const char* src = base + (size_t)(sl * 32 + (lane >> 3)) * ld + (((lane & 7) ^ (((lane >> 4) & 1) << 2)) << 4);
  u32x4 t[4];
#pragma unroll
  for (int i = 0; i < 4; ++i) t[i] = *(const u32x4*)(src + (size_t)i * 8 * ld);
#pragma unroll
  for (int i = 0; i < 4; ++i) *(LAS u32x4*)(my + i * 1024 + lane * 16) = t[i];
  asm volatile("s_waitcnt lgkmcnt(0)" ::: "memory");
  const int h = lane >> 5, cb = (lane >> 4) & 1, q = (lane & 15) >> 2, pp = lane & 3;
  char* dst = ws + (k3 ? WS_WT3 : WS_WT1) + (size_t)((2 * w2) * np + p) * 16384 + sl * 2048 + lane * 16;
#pragma unroll
  for (int i = 0; i < 4; ++i) {
    LAS const char* rp = my + (4 * h + q) * 128 + (((i & 1) ^ (q >> 1)) * 64) + cb * 32 + pp * 8 + (i >> 1) * 2048;
    s16x4 l = trread(rp), hh = trread(rp + 1024);
    *(bf16x8*)(dst + (size_t)(i & 1) * np * 16384 + (i >> 1) * 1024) = __builtin_shufflevector(l, hh, 0, 1, 2, 3, 4, 5, 6, 7);
  }
}

struct Stream {
  const char* p;
  bf16x8 wf[2][16];
  DI void start(const char* base) {
    p = base;
#pragma unroll
    for (int i = 0; i < 16; ++i) wf[0][i] = *(const bf16x8*)(p + i * 1024);
  }
};
struct NoWork { DI void init() {} DI void step(int) {} };
struct SiluWork {
  LAS char* p;
  u32x2 raw; float x[4], e[4];
  DI void init() { raw = *(LAS u32x2*)p; }
  DI void step(int ks) {
    const int g = ks >> 2, i = ks & 3;
    if (i == 0) {
      x[0] = bflo(raw[0]); x[1] = bfhi(raw[0]); x[2] = bflo(raw[1]); x[3] = bfhi(raw[1]);
#pragma unroll
      for (int j = 0; j < 4; ++j) e[j] = -LOG2E * x[j];
      asm volatile("" : "+v"(e[0]), "+v"(e[1]), "+v"(e[2]), "+v"(e[3]), "+v"(x[0]), "+v"(x[1]), "+v"(x[2]), "+v"(x[3]));
      if (g < 3) raw = *(LAS u32x2*)(p + ((g + 1) >> 1) * 1024 + ((g + 1) & 1) * 8);
    } else if (i == 1) {
#pragma unroll
      for (int j = 0; j < 4; ++j) e[j] = __builtin_amdgcn_exp2f(e[j]);
      asm volatile("" : "+v"(e[0]), "+v"(e[1]), "+v"(e[2]), "+v"(e[3]));
    } else if (i == 2) {
#pragma unroll
      for (int j = 0; j < 4; ++j) e[j] = __builtin_amdgcn_rcpf(1.0f + e[j]);
      asm volatile("" : "+v"(e[0]), "+v"(e[1]), "+v"(e[2]), "+v"(e[3]));
    } else {
      *(LAS u32x2*)(p + (g >> 1) * 1024 + (g & 1) * 8) = (u32x2){pk2(x[0] * e[0], x[1] * e[1]), pk2(x[2] * e[2], x[3] * e[3])};
    }
  }
};
template <int NB, bool SWAP, int P, int NP, class W>
DI void gemm_k256(Stream& st, f32x16 (&acc)[NB], LAS const char* bl, W work) {
  constexpr bool REFILL = P + 2 < NP;
  bf16x8 bq[3][NB];
#pragma unroll
  for (int nb = 0; nb < NB; ++nb) { bq[0][nb] = ldsfrag(bl + nb * 16384); bq[1][nb] = ldsfrag(bl + nb * 16384 + 1024); }
  work.init();
#pragma unroll
  for (int ks = 0; ks < 16; ++ks) {
    SB();
#pragma unroll
    for (int nb = 0; nb < NB; ++nb)
      acc[nb] = SWAP ? MFMA(bq[ks % 3][nb], st.wf[P & 1][ks], acc[nb]) : MFMA(st.wf[P & 1][ks], bq[ks % 3][nb], acc[nb]);
    if (ks < 14) {
#pragma unroll
      for (int nb = 0; nb < NB; ++nb) bq[(ks + 2) % 3][nb] = ldsfrag(bl + nb * 16384 + (ks + 2) * 1024);
    }
    if (P == 0) st.wf[1][ks] = *(const bf16x8*)(st.p + 16384 + ks * 1024);
    if (REFILL && ks > 0) st.wf[P & 1][ks - 1] = *(const bf16x8*)(st.p + (P + 2) * 16384 + (ks - 1) * 1024);
    work.step(ks);
  }
  SB();
  if (REFILL) st.wf[P & 1][15] = *(const bf16x8*)(st.p + (P + 2) * 16384 + 15 * 1024);
}

constexpr int L_X = 0, L_Y = L_X + 16384, L_Z = L_Y + 16384;
constexpr int L_STAT = L_Z + 16384 + 9216;
constexpr int GROW = 144, GSZ = 36 * GROW;
constexpr int L_DW = L_STAT + 4096;
constexpr int L_TOTAL = L_DW + 5120;

DI float sum16(const f32x16& t) {
  return (((t[0] + t[1]) + (t[2] + t[3])) + ((t[4] + t[5]) + (t[6] + t[7]))) + (((t[8] + t[9]) + (t[10] + t[11])) + ((t[12] + t[13]) + (t[14] + t[15])));
}
template <int NB>
DI void ln_stats(const f32x16 (&v)[NB], LAS char* lds, int w, int lane, float (&mean)[NB], float (&rstd)[NB]) {
  const int r = lane & 31;
#pragma unroll
  for (int nb = 0; nb < NB; ++nb) {
    const float mw = halfsum(sum16(v[nb])) * (1.0f / 32.0f);
    const f32x16 d = v[nb] - mw;
    const float m2 = halfsum(sum16(d * d));
    if (lane < 32) *(LAS f32x2*)(lds + L_STAT + ((nb * 32 + r) * 8 + w) * 8) = (f32x2){mw, m2};
  }
  lds_barrier();
#pragma unroll
  for (int nb = 0; nb < NB; ++nb) {
    f32x4 s[4];
#pragma unroll
    for (int i = 0; i < 4; ++i) s[i] = *(LAS f32x4*)(lds + L_STAT + (nb * 32 + r) * 64 + i * 16);
    const float m = 0.125f * (((s[0][0] + s[0][2]) + (s[1][0] + s[1][2])) + ((s[2][0] + s[2][2]) + (s[3][0] + s[3][2])));
    float M2 = 0.f;
#pragma unroll
    for (int i = 0; i < 4; ++i) { const float e0 = s[i][0] - m, e1 = s[i][2] - m; M2 += s[i][1] + s[i][3] + 32.0f * (e0 * e0 + e1 * e1); }
    mean[nb] = m; rstd[nb] = __builtin_amdgcn_rsqf(M2 * (1.0f / 256.0f) + LN_EPS);
  }
}
template <int NB>
DI void ln_to_frags(const f32x16 (&v)[NB], LAS char* lds, int w, int lane) {
  float mean[NB], rstd[NB];
  ln_stats<NB>(v, lds, w, lane, mean, rstd);
  LAS char* fb = lds + L_X + w * 2048 + lane * 16;
#pragma unroll
  for (int nb = 0; nb < NB; ++nb) {
    const f32x16 n = (v[nb] - mean[nb]) * rstd[nb];
#pragma unroll
    for (int s = 0; s < 2; ++s) *(LAS bf16x8*)(fb + nb * 16384 + s * 1024) = pack8(n, s);
  }
  lds_barrier();
}

template <int P0, int NP>
DI void ff_block(Stream& st, LAS char* lds, int w, int lane, f32x16 (&out)[1]) {
  out[0] = f32x16{};
  LAS char* xb = lds + L_X + lane * 16;
  LAS char* yb = lds + L_Y + lane * 16;
  LAS char* zb = lds + L_Z + lane * 16;
  auto put = [&](LAS char* dst, const f32x16& hv) { *(LAS bf16x8*)(dst + w * 2048) = pack8(hv, 0); *(LAS bf16x8*)(dst + w * 2048 + 1024) = pack8(hv, 1); };
  f32x16 hn[1];
  hn[0] = f32x16{}; gemm_k256<1, false, P0 + 0, NP>(st, hn, xb, NoWork{});
  put(yb, hn[0]);
  hn[0] = f32x16{}; gemm_k256<1, false, P0 + 1, NP>(st, hn, xb, SiluWork{yb + w * 2048});
  lds_barrier(); put(zb, hn[0]);
  gemm_k256<1, false, P0 + 2, NP>(st, out, yb, NoWork{});
  hn[0] = f32x16{}; gemm_k256<1, false, P0 + 3, NP>(st, hn, xb, SiluWork{zb + w * 2048});
  lds_barrier(); put(yb, hn[0]);
  gemm_k256<1, false, P0 + 4, NP>(st, out, zb, NoWork{});
  hn[0] = f32x16{}; gemm_k256<1, false, P0 + 5, NP>(st, hn, xb, SiluWork{yb + w * 2048});
  lds_barrier(); put(zb, hn[0]);
  gemm_k256<1, false, P0 + 6, NP>(st, out, yb, SiluWork{zb + w * 2048});
  lds_barrier();
  gemm_k256<1, false, P0 + 7, NP>(st, out, zb, NoWork{});
}

DI void l2_warm(const char* p, int kb_total, int idx, int lane, u32x4& t) {
  const int per = (kb_total + 247) / 248;
  int b = idx * per, e = b + per; if (e > kb_total) e = kb_total;
  for (int i = b; i < e; ++i)
    asm volatile("global_load_dwordx4 %0, %1, %2" : "+v"(t) : "v"(lane * 16 + i * 1024), "s"(p));
}

__global__ __launch_bounds__(512, 2) void k1_kernel(const char* __restrict__ x, char* __restrict__ ws) {
  extern __shared__ __attribute__((aligned(16))) char smem[];
  LAS char* lds = (LAS char*)smem;
  const int lane = threadIdx.x & 63, w = __builtin_amdgcn_readfirstlane(threadIdx.x >> 6), r = lane & 31, h = lane >> 5;
  const int tb = blockIdx.x;
  if (tb == 0 && threadIdx.x < 256) *(unsigned*)(ws + WS_FLAG + threadIdx.x * 64) = 0u;
  STAMP(0, 0);
  f32x16 xv[1];
  Stream st;
  u32x4 wt = {0u, 0u, 0u, 0u};
  {
    const char* xrow = x + (size_t)(tb * 32 + r) * 512 + (w * 32 + 4 * h) * 2;
    u32x2 u[4];
#pragma unroll
    for (int g = 0; g < 4; ++g) u[g] = *(const u32x2*)(xrow + g * 16);
    asm volatile("s_barrier" ::: "memory");
    SB();
    st.start(ws + WS_WT1 + (size_t)w * NP1 * 16384 + lane * 16);
    SB();
#pragma unroll
    for (int g = 0; g < 4; ++g) {
      xv[0][4 * g] = bflo(u[g][0]); xv[0][4 * g + 1] = bfhi(u[g][0]); xv[0][4 * g + 2] = bflo(u[g][1]); xv[0][4 * g + 3] = bfhi(u[g][1]);
    }
  }
  STAMP(0, 1);
  ln_to_frags<1>(xv, lds, w, lane);
  STAMP(0, 2);
  f32x16 acc[1];
  ff_block<0, NP1>(st, lds, w, lane, acc);
  asm volatile("" :: "v"(wt));
  STAMP(0, 3);
#pragma unroll
  for (int i = 0; i < 16; ++i) xv[0][i] = rbf(xv[0][i] + 0.5f * acc[0][i]);
#pragma unroll
  for (int s = 0; s < 2; ++s) stg16((char*)(ws + WS_X1F + ((size_t)tb * 16 + 2 * w + s) * 1024 + lane * 16), pack8(xv[0], s));
  ln_to_frags<1>(xv, lds, w, lane);
  STAMP(0, 4);
  const int b = tb / NBLK, blk = tb - b * NBLK, bh = b * 4 + (w >> 1);
  char* kvdst = ws + WS_KVF + (size_t)(bh * NBLK + blk) * 8192 + (w & 1) * 2048 + lane * 16;
  {
    f32x16 a[1]; a[0] = f32x16{};
    gemm_k256<1, false, 8, NP1>(st, a, lds + L_X + lane * 16, NoWork{});
    a[0] = a[0] * QSCALE;
    char* dst = ws + WS_QF + (size_t)(bh * NBLK + blk) * 4096 + (w & 1) * 2048 + lane * 16;
    stg16((char*)(dst), pack8(a[0], 0)); stg16((char*)(dst + 1024), pack8(a[0], 1));
  }
  {
    f32x16 a[1]; a[0] = f32x16{};
    gemm_k256<1, false, 9, NP1>(st, a, lds + L_X + lane * 16, NoWork{});
    stg16((char*)(kvdst), pack8(a[0], 0)); stg16((char*)(kvdst + 1024), pack8(a[0], 1));
  }
  STAMP(0, 5);
  {
    f32x16 a[1]; a[0] = f32x16{};
    gemm_k256<1, true, 10, NP1>(st, a, lds + L_X + lane * 16, NoWork{});
    stg16((char*)(kvdst + 4096), pack8(a[0], 0)); stg16((char*)(kvdst + 5120), pack8(a[0], 1));
  }
  STAMP(0, 29);
}

constexpr int A_KG = 2, A_SBLK = A_KG * 2, A_HST = A_SBLK * 4096, A_NST = (NBLK + A_SBLK - 1) / A_SBLK, A_NFULL = NBLK / A_SBLK;
constexpr int A_VOFF = 3 * A_HST, A_LDS = 6 * A_HST;
DI void attn_body(char* __restrict__ ws, LAS char* lds) {
  const int lane = threadIdx.x & 63, w = __builtin_amdgcn_readfirstlane(threadIdx.x >> 6);
  const int bh = blockIdx.x & 7, qg = blockIdx.x >> 3, qi = w & 3, kg = w >> 2;
  const int qb_raw = qg * 4 + qi, qb = qb_raw < NBLK ? qb_raw : NBLK - 1;
  STAMP(1, 0);
  const char* kv = ws + WS_KVF + (size_t)bh * NBLK * 8192;
  const unsigned lds0 = (unsigned)(uintptr_t)lds;
  const unsigned voff = (w >> 1) * 8192 + (w & 1) * 2048 + lane * 16;
  auto dma2 = [&](int st, int isv, unsigned ldsoff) {
    asm volatile("s_mov_b32 m0, %2\n\ts_nop 0\n\tglobal_load_lds_dwordx4 %0, %1\n\tglobal_load_lds_dwordx4 %0, %1 offset:1024"
                 :: "v"(voff), "s"(kv + (size_t)st * (2 * A_HST) + isv * 4096), "s"(lds0 + ldsoff + w * 2048));
  };
  dma2(0, 0, 0); dma2(1, 0, A_HST); dma2(0, 1, A_VOFF); dma2(2, 0, 2 * A_HST); dma2(1, 1, A_VOFF + A_HST);
  bf16x8 qf[4];
#pragma unroll
  for (int f = 0; f < 4; ++f) qf[f] = *(const bf16x8*)(ws + WS_QF + ((size_t)(bh * NBLK + qb) * 4 + f) * 1024 + lane * 16);
#pragma unroll
  for (int f = 0; f < 4; ++f) asm volatile("" : "+v"(qf[f]));
  u32x4 wt = {0u, 0u, 0u, 0u};
  const int rem = NBLK - A_NFULL * A_SBLK - kg * 2;
  const int ntail = rem < 0 ? 0 : (rem > 2 ? 2 : rem);
  LAS const char* base = lds + kg * 8192 + lane * 16;
  f32x16 o0 = f32x16{}, o1 = f32x16{}, negr, sa, sb;
  f32x4 lacc = {0.f, 0.f, 0.f, 0.f};
  bf16x8 sel;
  { const short one = (((lane >> 4) ^ lane) & 1) ? (short)0 : (short)0x3f80;
#pragma unroll
    for (int j = 0; j < 8; ++j) sel[j] = one; }
  bf16x8 kf[4], vf[4], kg2[4], vg2[4]; unsigned pa[8], pb[8];
#pragma unroll
  for (int i = 0; i < 8; ++i) pa[i] = 0u;
  float ref;
  asm volatile("s_waitcnt vmcnt(0)\n\ts_barrier" : "+v"(wt) :: "memory");
  {
#pragma unroll
    for (int f = 0; f < 4; ++f) kf[f] = ldsfrag(base + f * 1024);
    f32x16 z = f32x16{};
    sa = MFMA(kf[0], qf[0], z); sa = MFMA(kf[1], qf[1], sa); sa = MFMA(kf[2], qf[2], sa); sa = MFMA(kf[3], qf[3], sa);
    float mx = sa[0];
#pragma unroll
    for (int i = 1; i < 16; ++i) mx = fmaxf(mx, sa[i]);
    ref = halfmax(mx) + 7.0f;
#pragma unroll
    for (int i = 0; i < 16; ++i) { sa[i] -= ref; negr[i] = -ref; }
#pragma unroll
    for (int f = 0; f < 4; ++f) { kf[f] = ldsfrag(base + 4096 + f * 1024); vf[f] = bf16x8{}; }
  }
#define SLOT3(MF, RF, i) do { MF; RF; e[2*(i)] = __builtin_amdgcn_exp2f(SC_[2*(i)]); e[2*(i)+1] = __builtin_amdgcn_exp2f(SC_[2*(i)+1]); \
    if ((i) > 0) PN_[(i)-1] = pk2(e[2*(i)-2], e[2*(i)-1]); SB(); } while (0)
  auto iter = [&](f32x16& SC_, f32x16& SN_, unsigned (&PP_)[8], unsigned (&PN_)[8], bf16x8 (&KC)[4], bf16x8 (&VC)[4], bf16x8 (&KN)[4], bf16x8 (&VN)[4], LAS const char* kn, LAS const char* vc) {
    float e[16];
    const bf16x8 p0 = __builtin_bit_cast(bf16x8, (u32x4){PP_[0], PP_[1], PP_[2], PP_[3]}), p1 = __builtin_bit_cast(bf16x8, (u32x4){PP_[4], PP_[5], PP_[6], PP_[7]});
    SB();
    SLOT3(SN_ = MFMA(KC[0], qf[0], negr), KN[0] = ldsfrag(kn), 0);
    SLOT3(o0 = MFMA(VC[0], p0, o0), VN[0] = ldsfrag(vc), 1);
    SLOT3(SN_ = MFMA(KC[1], qf[1], SN_), KN[1] = ldsfrag(kn + 1024), 2);
    SLOT3(o1 = MFMA(VC[2], p0, o1); lacc = MFMA16(sel, p0, lacc), VN[2] = ldsfrag(vc + 2048), 3);
    SLOT3(SN_ = MFMA(KC[2], qf[2], SN_), KN[2] = ldsfrag(kn + 2048), 4);
    SLOT3(o0 = MFMA(VC[1], p1, o0), VN[1] = ldsfrag(vc + 1024), 5);
    SLOT3(SN_ = MFMA(KC[3], qf[3], SN_), KN[3] = ldsfrag(kn + 3072), 6);
    SLOT3(o1 = MFMA(VC[3], p1, o1); lacc = MFMA16(sel, p1, lacc), VN[3] = ldsfrag(vc + 3072), 7);
    PN_[7] = pk2(e[14], e[15]);
    const unsigned orall = (PN_[0] | PN_[1] | PN_[2]) | (PN_[3] | PN_[4] | PN_[5]) | (PN_[6] | PN_[7]);
    if (__builtin_expect(__any((orall & 0x40004000u) != 0u), 0)) {
      float mx = SC_[0];
#pragma unroll
      for (int i = 1; i < 16; ++i) mx = fmaxf(mx, SC_[i]);
      mx = halfmax(mx);
      const float delta = (mx >= 0.5f) ? mx + 7.0f : 0.0f;
      const float alpha = __builtin_amdgcn_exp2f(-delta);
      const float alo = __shfl_xor(alpha, 16);
      const float aA = (lane & 16) ? alo : alpha, aB = (lane & 16) ? alpha : alo;
      lacc = lacc * (f32x4){aA, aB, aA, aB};
      o0 = o0 * alpha; o1 = o1 * alpha; negr = negr - delta; SN_ = SN_ - delta;
      asm volatile("" : "+v"(o0), "+v"(o1), "+v"(negr), "+v"(SN_), "+v"(lacc));
      ref = -negr[0];
#pragma unroll
      for (int i = 0; i < 8; ++i) PN_[i] = pk2(__builtin_amdgcn_exp2f(SC_[2*i] - delta), __builtin_amdgcn_exp2f(SC_[2*i+1] - delta));
    }
  };
  STAMP(1, 1);
  int r0 = 0, r1 = A_HST, r2 = 2 * A_HST;
  for (int st = 0; st < A_NFULL; ++st) {
    asm volatile("s_waitcnt vmcnt(4) lgkmcnt(0)\n\ts_barrier" ::: "memory");
    dma2(st + 3 < A_NST ? st + 3 : A_NST - 1, 0, r0); dma2(st + 2 < A_NST ? st + 2 : A_NST - 1, 1, A_VOFF + r2);
    iter(sa, sb, pa, pb, kf, vf, kg2, vg2, base + r1, base + A_VOFF + r0);
    iter(sb, sa, pb, pa, kg2, vg2, kf, vf, base + r1 + 4096, base + A_VOFF + r0 + 4096);
    const int t = r0; r0 = r1; r1 = r2; r2 = t;
  }
  asm volatile("s_waitcnt vmcnt(0) lgkmcnt(0)\n\ts_barrier" ::: "memory");
  if (ntail > 0) {
    iter(sa, sb, pa, pb, kf, vf, kg2, vg2, base + r1, base + A_VOFF + r0);
    if (ntail > 1) iter(sb, sa, pb, pa, kg2, vg2, kf, vf, base + r1 + 4096, base + A_VOFF + r0 + 4096);
    else {
#pragma unroll
      for (int i = 0; i < 8; ++i) pa[i] = pb[i];
#pragma unroll
      for (int i = 0; i < 4; ++i) vf[i] = vg2[i];
    }
  }
  {
    const bf16x8 p0 = __builtin_bit_cast(bf16x8, (u32x4){pa[0], pa[1], pa[2], pa[3]}), p1 = __builtin_bit_cast(bf16x8, (u32x4){pa[4], pa[5], pa[6], pa[7]});
    o0 = MFMA(vf[0], p0, o0); o1 = MFMA(vf[2], p0, o1); o0 = MFMA(vf[1], p1, o0); o1 = MFMA(vf[3], p1, o1);
    lacc = MFMA16(sel, p0, lacc); lacc = MFMA16(sel, p1, lacc);
  }
  float lsum = (lane & 16) ? lacc[1] : lacc[0];
  STAMP(1, 2);
  lds_barrier();
  if (kg > 0) {
    LAS char* cb = lds + ((kg - 1) * 4 + qi) * 9216 + lane * 16;
#pragma unroll
    for (int g = 0; g < 4; ++g) {
      *(LAS f32x4*)(cb + g * 1024) = (f32x4){o0[4 * g], o0[4 * g + 1], o0[4 * g + 2], o0[4 * g + 3]};
      *(LAS f32x4*)(cb + 4096 + g * 1024) = (f32x4){o1[4 * g], o1[4 * g + 1], o1[4 * g + 2], o1[4 * g + 3]};
    }
    *(LAS f32x2*)(lds + ((kg - 1) * 4 + qi) * 9216 + 8192 + lane * 8) = (f32x2){ref, lsum};
  }
  lds_barrier();
  if (kg == 0 && qb_raw < NBLK) {
#pragma unroll
    for (int k = 1; k < A_KG; ++k) {
      LAS char* cb = lds + ((k - 1) * 4 + qi) * 9216 + lane * 16;
      const f32x2 rl = *(LAS f32x2*)(lds + ((k - 1) * 4 + qi) * 9216 + 8192 + lane * 8);
      const float m = fmaxf(ref, rl[0]);
      const float c0 = __builtin_amdgcn_exp2f(ref - m), c1 = __builtin_amdgcn_exp2f(rl[0] - m);
      lsum = lsum * c0 + rl[1] * c1; ref = m;
#pragma unroll
      for (int g = 0; g < 4; ++g) {
        const f32x4 u0 = *(LAS f32x4*)(cb + g * 1024), u1 = *(LAS f32x4*)(cb + 4096 + g * 1024);
#pragma unroll
        for (int j = 0; j < 4; ++j) { o0[4 * g + j] = o0[4 * g + j] * c0 + u0[j] * c1; o1[4 * g + j] = o1[4 * g + j] * c0 + u1[j] * c1; }
      }
    }
    const float inv = 1.0f / lsum;
#pragma unroll
    for (int i = 0; i < 16; ++i) { o0[i] *= inv; o1[i] *= inv; }
    char* dst = ws + WS_ATTF + ((size_t)((bh >> 2) * NBLK + qb) * 16 + (bh & 3) * 4) * 1024 + lane * 16;
    const bf16x8 f0 = pack8(o0, 0), f1 = pack8(o0, 1), f2 = pack8(o1, 0), f3 = pack8(o1, 1);
    asm volatile("global_store_dwordx4 %0, %1, off sc1\n\tglobal_store_dwordx4 %0, %2, off offset:1024 sc1\n\t"
                 "global_store_dwordx4 %0, %3, off offset:2048 sc1\n\tglobal_store_dwordx4 %0, %4, off offset:3072 sc1\n\ts_nop 1"
                 :: "v"(dst), "v"(f0), "v"(f1), "v"(f2), "v"(f3) : "memory");
  }
  asm volatile("s_waitcnt vmcnt(0) lgkmcnt(0)\n\ts_barrier" ::: "memory");
  if (threadIdx.x == 0) __hip_atomic_store((unsigned*)(ws + WS_FLAG + blockIdx.x * 64), 1u, __ATOMIC_RELAXED, __HIP_MEMORY_SCOPE_AGENT);
  STAMP(1, 29);
}

__global__ __launch_bounds__(512, 2) void k23_kernel(const char* __restrict__ dw, char* __restrict__ ws, char* __restrict__ out) {
  extern __shared__ __attribute__((aligned(16))) char smem[];
  LAS char* lds = (LAS char*)smem;
  attn_body(ws, lds);
  if (blockIdx.x >= NTB) return;
  const int lane = threadIdx.x & 63, w = __builtin_amdgcn_readfirstlane(threadIdx.x >> 6), r = lane & 31, h = lane >> 5;
  const int tb = blockIdx.x, b = tb / NBLK, t0 = (tb - b * NBLK) * 32;
  STAMP(2, 0);
  Stream st;
  const int hc = r & 3, ht = (hc < 2) ? t0 - 2 + hc : t0 + 30 + hc;
  const bool hvalid = ht >= 0 && ht < TSEQ;
  const int hg = b * TSEQ + (ht < 0 ? 0 : (ht >= TSEQ ? TSEQ - 1 : ht));
  const unsigned hoff = (unsigned)(hg >> 5) * 16384 + ((hg & 31) + 32 * h) * 16 + w * 2048;
  const unsigned moff = (unsigned)tb * 16384 + lane * 16 + w * 2048;
  u32x4 xr[2][2];
#pragma unroll
  for (int f = 0; f < 2; ++f) {
    xr[0][f] = *(const u32x4*)(ws + WS_X1F + moff + f * 1024);
    xr[1][f] = *(const u32x4*)(ws + WS_X1F + hoff + f * 1024);
  }
  const u32x4 dwv = *(const u32x4*)(dw + (threadIdx.x < 160 ? threadIdx.x : 159) * 16);
  SB();
  st.start(ws + WS_WT3 + (size_t)w * NP3 * 16384 + lane * 16);
  SB();
  if (threadIdx.x < 160) {
    *(LAS f32x4*)(lds + L_DW + threadIdx.x * 32) = (f32x4){bflo(dwv[0]), bfhi(dwv[0]), bflo(dwv[1]), bfhi(dwv[1])};
    *(LAS f32x4*)(lds + L_DW + threadIdx.x * 32 + 16) = (f32x4){bflo(dwv[2]), bfhi(dwv[2]), bflo(dwv[3]), bfhi(dwv[3])};
  }
  if (w == 0) {
    const int blk = tb - b * NBLK;
    int q = blk + (lane >> 2) - 1; q = q < 0 ? 0 : (q >= NBLK ? NBLK - 1 : q);
    const unsigned* fp = (const unsigned*)(ws + WS_FLAG + (size_t)((q >> 2) * 8 + b * 4 + (lane & 3)) * 64);
    for (int spin = 0; spin < (1 << 22); ++spin) {
      const unsigned v = (lane < 12) ? __hip_atomic_load(fp, __ATOMIC_RELAXED, __HIP_MEMORY_SCOPE_AGENT) : 1u;
      if (__all(v == 1u)) break;
      __builtin_amdgcn_s_sleep(2);
    }
    __builtin_amdgcn_fence(__ATOMIC_ACQUIRE, "agent");
    asm volatile("s_waitcnt vmcnt(0)" ::: "memory");
  }
  lds_barrier();
  {
    const unsigned ly = (unsigned)(uintptr_t)(lds + L_Y + w * 2048);
    asm volatile("s_mov_b32 m0, %3\n\ts_nop 0\n\tglobal_load_lds_dwordx4 %0, %2 sc1\n\tglobal_load_lds_dwordx4 %0, %2 offset:1024 sc1\n\t"
                 "s_mov_b32 m0, %4\n\ts_nop 0\n\tglobal_load_lds_dwordx4 %1, %2 sc1\n\tglobal_load_lds_dwordx4 %1, %2 offset:1024 sc1\n\t"
                 "s_waitcnt vmcnt(0)"
                 :: "v"(moff), "v"(hoff), "s"(ws + WS_ATTF), "s"(ly), "s"(ly + 16384) : "memory");
  }
  lds_barrier();
  STAMP(2, 1);
  f32x16 xv[2];
#pragma unroll
  for (int nb = 0; nb < 2; ++nb) { unpack8(xr[nb][0], xv[nb], 0); unpack8(xr[nb][1], xv[nb], 1); }
  gemm_k256<2, false, 0, NP3>(st, xv, lds + L_Y + lane * 16, NoWork{});
  STAMP(2, 2);
  ln_to_frags<2>(xv, lds, w, lane);
  STAMP(2, 3);
  {
    f32x16 a[2], g[2];
#pragma unroll
    for (int nb = 0; nb < 2; ++nb) { a[nb] = f32x16{}; g[nb] = f32x16{}; }
    gemm_k256<2, false, 1, NP3>(st, a, lds + L_X + lane * 16, NoWork{});
    gemm_k256<2, false, 2, NP3>(st, g, lds + L_X + lane * 16, NoWork{});
    STAMP(2, 4);
    LAS char* G = lds + L_Y + w * GSZ;
    lds_barrier();
#pragma unroll
    for (int nb = 0; nb < 2; ++nb) {
      const int row = nb == 0 ? r + 2 : (hc < 2 ? hc : 32 + hc);
      const bool wr = nb == 0 || r < 4;
      const float keep = (nb == 0 || hvalid) ? 1.0f : 0.0f;
#pragma unroll
      for (int q = 0; q < 4; ++q) {
        f32x4 e;
#pragma unroll
        for (int j = 0; j < 4; ++j) e[j] = keep * a[nb][4 * q + j] * sigmoidf_(g[nb][4 * q + j]);
        if (wr) *(LAS f32x4*)(G + row * GROW + (8 * q + 4 * h) * 4) = e;
      }
    }
    asm volatile("s_waitcnt lgkmcnt(0)" ::: "memory");
    f32x16 c;
#pragma unroll
    for (int q = 0; q < 4; ++q) {
      f32x4 e = {0.f, 0.f, 0.f, 0.f};
#pragma unroll
      for (int i = 0; i < 5; ++i)
        e += *(LAS f32x4*)(G + (r + i) * GROW + (8 * q + 4 * h) * 4) * *(LAS f32x4*)(lds + L_DW + i * 1024 + (w * 32 + 8 * q + 4 * h) * 4);
#pragma unroll
      for (int j = 0; j < 4; ++j) c[4 * q + j] = siluf_(e[j]);
    }
#pragma unroll
    for (int s = 0; s < 2; ++s) *(LAS bf16x8*)(lds + L_X + (2 * w + s) * 1024 + lane * 16) = pack8(c, s);
    lds_barrier();
  }
  STAMP(2, 5);
  f32x16 x3[1];
  x3[0] = xv[0];
  gemm_k256<1, false, 3, NP3>(st, x3, lds + L_X + lane * 16, NoWork{});
  STAMP(2, 6);
  ln_to_frags<1>(x3, lds, w, lane);
  STAMP(2, 7);
  f32x16 acc[1];
  ff_block<4, NP3>(st, lds, w, lane, acc);
  x3[0] += 0.5f * acc[0];
  STAMP(2, 8);
  float mean[1], rstd[1];
  ln_stats<1>(x3, lds, w, lane, mean, rstd);
  char* orow = out + (size_t)(tb * 32 + r) * 512 + (w * 32 + 4 * h) * 2;
  const f32x16 n = (x3[0] - mean[0]) * rstd[0];
#pragma unroll
  for (int q = 0; q < 4; ++q) *(u32x2*)(orow + q * 16) = (u32x2){pk2(n[4 * q], n[4 * q + 1]), pk2(n[4 * q + 2], n[4 * q + 3])};
  STAMP(2, 29);
}

extern "C" void kernel_launch(void* const* d_in, const int* in_sizes, int n_in, void* d_out, int out_size, void* d_ws, size_t ws_size,
                              hipStream_t stream) {
  const char* x = (const char*)d_in[0];
  const char* w1a = (const char*)d_in[1];
  const char* w1b = (const char*)d_in[2];
  const char* wqkv = (const char*)d_in[3];
  const char* wo = (const char*)d_in[4];
  const char* pw1 = (const char*)d_in[5];
  const char* dw = (const char*)d_in[6];
  const char* pw2 = (const char*)d_in[7];
  const char* w2a = (const char*)d_in[8];
  const char* w2b = (const char*)d_in[9];
  char* ws = (char*)d_ws;
  (void)hipFuncSetAttribute((const void*)k1_kernel, hipFuncAttributeMaxDynamicSharedMemorySize, L_TOTAL);
  (void)hipFuncSetAttribute((const void*)k23_kernel, hipFuncAttributeMaxDynamicSharedMemorySize, A_LDS);
  WPtrs wp{w1a, w1b, wqkv, wo, pw1, pw2, w2a, w2b};
  prep_kernel<<<(NP1 + NP3) * 8, 256, 0, stream>>>(wp, ws);
  k1_kernel<<<NTB, 512, L_TOTAL, stream>>>(x, ws);
  k23_kernel<<<256, 512, A_LDS, stream>>>(dw, ws, (char*)d_out);
}
```

```cpp
#include <hip/hip_runtime.h>
#include <hip/hip_bf16.h>
#include <stdint.h>

typedef short bf16x8 __attribute__((ext_vector_type(8)));
typedef short s16x4 __attribute__((ext_vector_type(4)));
typedef float f32x16 __attribute__((ext_vector_type(16)));
typedef float f32x2 __attribute__((ext_vector_type(2)));
typedef __bf16 bf16x2_t __attribute__((ext_vector_type(2)));
typedef unsigned u32x4 __attribute__((ext_vector_type(4)));
typedef unsigned u32x2 __attribute__((ext_vector_type(2)));
#define LAS __attribute__((address_space(3)))
#define DI __device__ __forceinline__
#define MFMA(a, b, c) __builtin_amdgcn_mfma_f32_32x32x16_bf16((a), (b), (c), 0, 0, 0)
#define MFMA16(a, b, c) __builtin_amdgcn_mfma_f32_16x16x32_bf16((a), (b), (c), 0, 0, 0)
#define SB() __builtin_amdgcn_sched_barrier(0)
#define STAMP(k, i) do {} while (0)
typedef float f32x4 __attribute__((ext_vector_type(4)));

constexpr int TSEQ = 4000, NBLK = 125, NTB = 250;
constexpr size_t WS_X1F = 0;
constexpr size_t WS_QF = 4u << 20;
constexpr size_t WS_KVF = 8u << 20;
constexpr size_t WS_ATTF = 17u << 20;
constexpr size_t WS_FLAG = 22u << 20;
constexpr float LN_EPS = 1e-5f;
constexpr float LOG2E = 1.4426950408889634f;
constexpr float QSCALE = 0.125f * LOG2E;

DI unsigned pk2(float a, float b) { f32x2 v = {a, b}; return __builtin_bit_cast(unsigned, __builtin_convertvector(v, bf16x2_t)); }
DI float bflo(unsigned u) { return __uint_as_float(u << 16); }
DI float bfhi(unsigned u) { return __uint_as_float(u & 0xffff0000u); }
DI float rbf(float a) { return bflo(pk2(a, a)); }
DI bf16x8 pack8(const f32x16& v, int s) {
  u32x4 p = {pk2(v[8 * s], v[8 * s + 1]), pk2(v[8 * s + 2], v[8 * s + 3]), pk2(v[8 * s + 4], v[8 * s + 5]), pk2(v[8 * s + 6], v[8 * s + 7])};
  return __builtin_bit_cast(bf16x8, p);
}
DI void unpack8(u32x4 p, f32x16& v, int s) {
#pragma unroll
  for (int i = 0; i < 4; ++i) { v[8 * s + 2 * i] = bflo(p[i]); v[8 * s + 2 * i + 1] = bfhi(p[i]); }
}
DI float halfsum(float x) {
  auto r = __builtin_amdgcn_permlane32_swap(__float_as_uint(x), __float_as_uint(x), false, false);
  return __uint_as_float(r[0]) + __uint_as_float(r[1]);
}
DI float halfmax(float x) {
  auto r = __builtin_amdgcn_permlane32_swap(__float_as_uint(x), __float_as_uint(x), false, false);
  return fmaxf(__uint_as_float(r[0]), __uint_as_float(r[1]));
}
DI float sigmoidf_(float x) { return __builtin_amdgcn_rcpf(1.0f + __builtin_amdgcn_exp2f(-LOG2E * x)); }
DI float siluf_(float x) { return x * sigmoidf_(x); }
DI void lds_barrier() { asm volatile("s_waitcnt lgkmcnt(0)\n\ts_barrier" ::: "memory"); }
typedef short v4i16_t __attribute__((ext_vector_type(4)));
DI s16x4 trread(LAS const char* p) { return __builtin_bit_cast(s16x4, __builtin_amdgcn_ds_read_tr16_b64_v4i16((LAS v4i16_t*)p)); }
DI void stg16(char* p, bf16x8 v) { __builtin_nontemporal_store(v, (bf16x8*)p); }
DI bf16x8 ldsfrag(LAS const char* p) { return *(LAS const bf16x8*)p; }

constexpr size_t WS_WT1 = 24u << 20, WS_WT3 = 26u << 20;
constexpr int NP1 = 11, NP3 = 12;
struct WPtrs { const char *w1a, *w1b, *wqkv, *wo, *pw1, *pw2, *w2a, *w2b; };
DI void ff_panel(const char* wa, const char* wb, int p, int w2, const char*& base, unsigned& ld) {
  const bool isb = (p == 2 || p == 4 || p >= 6);
  const int c = p < 2 ? p : (p == 3 ? 2 : (p == 5 ? 3 : (p == 2 ? 0 : (p == 4 ? 1 : p - 4))));
  if (isb) { base = wb + (size_t)c * 256 * 512 + w2 * 128; ld = 512; } else { base = wa + (size_t)(w2 + 4 * c) * 128; ld = 2048; }
}
__global__ __launch_bounds__(256) void prep_kernel(WPtrs wp, char* __restrict__ ws) {
  __shared__ __attribute__((aligned(16))) char sm[4 * 4096];
  const int lane = threadIdx.x & 63, wv = __builtin_amdgcn_readfirstlane(threadIdx.x >> 6);
  const int g = blockIdx.x * 4 + wv;
  const bool k3 = g >= 4 * NP1 * 8;
  const int gg = k3 ? g - 4 * NP1 * 8 : g, np = k3 ? NP3 : NP1;
  const int w2 = gg / (np * 8), p = (gg >> 3) % np, sl = gg & 7;
  const char* base; unsigned ld;
  if (!k3) {
    if (p < 8) ff_panel(wp.w1a, wp.w1b, p, w2, base, ld);
    else { base = wp.wqkv + (p - 8) * 512 + w2 * 128; ld = 1536; }
  } else {
    if (p == 0) { base = wp.wo + w2 * 128; ld = 512; }
    else if (p < 3) { base = wp.pw1 + (p - 1) * 512 + w2 * 128; ld = 1024; }
    else if (p == 3) { base = wp.pw2 + w2 * 128; ld = 512; }
    else ff_panel(wp.w2a, wp.w2b, p - 4, w2, base, ld);
  }
  LAS char* my = (LAS char*)sm + wv * 4096;
  const char* src = base + (size_t)(sl * 32 + (lane >> 3)) * ld + (((lane & 7) ^ (((lane >> 4) & 1) << 2)) << 4);
  u32x4 t[4];
#pragma unroll
  for (int i = 0; i < 4; ++i) t[i] = *(const u32x4*)(src + (size_t)i * 8 * ld);
#pragma unroll
  for (int i = 0; i < 4; ++i) *(LAS u32x4*)(my + i * 1024 + lane * 16) = t[i];
  asm volatile("s_waitcnt lgkmcnt(0)" ::: "memory");
  const int h = lane >> 5, cb = (lane >> 4) & 1, q = (lane & 15) >> 2, pp = lane & 3;
  char* dst = ws + (k3 ? WS_WT3 : WS_WT1) + (size_t)((2 * w2) * np + p) * 16384 + sl * 2048 + lane * 16;
#pragma unroll
  for (int i = 0; i < 4; ++i) {
    LAS const char* rp = my + (4 * h + q) * 128 + (((i & 1) ^ (q >> 1)) * 64) + cb * 32 + pp * 8 + (i >> 1) * 2048;
    s16x4 l = trread(rp), hh = trread(rp + 1024);
    *(bf16x8*)(dst + (size_t)(i & 1) * np * 16384 + (i >> 1) * 1024) = __builtin_shufflevector(l, hh, 0, 1, 2, 3, 4, 5, 6, 7);
  }
}

struct Stream {
  const char* p;
  bf16x8 wf[2][16];
  DI void start(const char* base) {
    p = base;
#pragma unroll
    for (int i = 0; i < 16; ++i) wf[0][i] = *(const bf16x8*)(p + i * 1024);
  }
};
struct NoWork { DI void init() {} DI void step(int) {} };
struct SiluWork {
  LAS char* p;
  u32x2 raw; float x[4], e[4];
  DI void init() { raw = *(LAS u32x2*)p; }
  DI void step(int ks) {
    const int g = ks >> 2, i = ks & 3;
    if (i == 0) {
      x[0] = bflo(raw[0]); x[1] = bfhi(raw[0]); x[2] = bflo(raw[1]); x[3] = bfhi(raw[1]);
#pragma unroll
      for (int j = 0; j < 4; ++j) e[j] = -LOG2E * x[j];
      asm volatile("" : "+v"(e[0]), "+v"(e[1]), "+v"(e[2]), "+v"(e[3]), "+v"(x[0]), "+v"(x[1]), "+v"(x[2]), "+v"(x[3]));
      if (g < 3) raw = *(LAS u32x2*)(p + ((g + 1) >> 1) * 1024 + ((g + 1) & 1) * 8);
    } else if (i == 1) {
#pragma unroll
      for (int j = 0; j < 4; ++j) e[j] = __builtin_amdgcn_exp2f(e[j]);
      asm volatile("" : "+v"(e[0]), "+v"(e[1]), "+v"(e[2]), "+v"(e[3]));
    } else if (i == 2) {
#pragma unroll
      for (int j = 0; j < 4; ++j) e[j] = __builtin_amdgcn_rcpf(1.0f + e[j]);
      asm volatile("" : "+v"(e[0]), "+v"(e[1]), "+v"(e[2]), "+v"(e[3]));
    } else {
      *(LAS u32x2*)(p + (g >> 1) * 1024 + (g & 1) * 8) = (u32x2){pk2(x[0] * e[0], x[1] * e[1]), pk2(x[2] * e[2], x[3] * e[3])};
    }
  }
};
template <int NB, bool SWAP, int P, int NP, class W>
DI void gemm_k256(Stream& st, f32x16 (&acc)[NB], LAS const char* bl, W work) {
  constexpr bool REFILL = P + 2 < NP;
  bf16x8 bq[3][NB];
#pragma unroll
  for (int nb = 0; nb < NB; ++nb) { bq[0][nb] = ldsfrag(bl + nb * 16384); bq[1][nb] = ldsfrag(bl + nb * 16384 + 1024); }
  work.init();
#pragma unroll
  for (int ks = 0; ks < 16; ++ks) {
    SB();
#pragma unroll
    for (int nb = 0; nb < NB; ++nb)
      acc[nb] = SWAP ? MFMA(bq[ks % 3][nb], st.wf[P & 1][ks], acc[nb]) : MFMA(st.wf[P & 1][ks], bq[ks % 3][nb], acc[nb]);
    if (ks < 14) {
#pragma unroll
      for (int nb = 0; nb < NB; ++nb) bq[(ks + 2) % 3][nb] = ldsfrag(bl + nb * 16384 + (ks + 2) * 1024);
    }
    if (P == 0) st.wf[1][ks] = *(const bf16x8*)(st.p + 16384 + ks * 1024);
    if (REFILL && ks > 0) st.wf[P & 1][ks - 1] = *(const bf16x8*)(st.p + (P + 2) * 16384 + (ks - 1) * 1024);
    work.step(ks);
  }
  SB();
  if (REFILL) st.wf[P & 1][15] = *(const bf16x8*)(st.p + (P + 2) * 16384 + 15 * 1024);
}

constexpr int L_X = 0, L_Y = L_X + 16384, L_Z = L_Y + 16384;
constexpr int L_STAT = L_Z + 16384 + 9216;
constexpr int GROW = 144, GSZ = 36 * GROW;
constexpr int L_DW = L_STAT + 4096;
constexpr int L_TOTAL = L_DW + 5120;

DI float sum16(const f32x16& t) {
  return (((t[0] + t[1]) + (t[2] + t[3])) + ((t[4] + t[5]) + (t[6] + t[7]))) + (((t[8] + t[9]) + (t[10] + t[11])) + ((t[12] + t[13]) + (t[14] + t[15])));
}
template <int NB>
DI void ln_stats(const f32x16 (&v)[NB], LAS char* lds, int w, int lane, float (&mean)[NB], float (&rstd)[NB]) {
  const int r = lane & 31;
#pragma unroll
  for (int nb = 0; nb < NB; ++nb) {
    const float mw = halfsum(sum16(v[nb])) * (1.0f / 32.0f);
    const f32x16 d = v[nb] - mw;
    const float m2 = halfsum(sum16(d * d));
    if (lane < 32) *(LAS f32x2*)(lds + L_STAT + ((nb * 32 + r) * 8 + w) * 8) = (f32x2){mw, m2};
  }
  lds_barrier();
#pragma unroll
  for (int nb = 0; nb < NB; ++nb) {
    f32x4 s[4];
#pragma unroll
    for (int i = 0; i < 4; ++i) s[i] = *(LAS f32x4*)(lds + L_STAT + (nb * 32 + r) * 64 + i * 16);
    const float m = 0.125f * (((s[0][0] + s[0][2]) + (s[1][0] + s[1][2])) + ((s[2][0] + s[2][2]) + (s[3][0] + s[3][2])));
    float M2 = 0.f;
#pragma unroll
    for (int i = 0; i < 4; ++i) { const float e0 = s[i][0] - m, e1 = s[i][2] - m; M2 += s[i][1] + s[i][3] + 32.0f * (e0 * e0 + e1 * e1); }
    mean[nb] = m; rstd[nb] = __builtin_amdgcn_rsqf(M2 * (1.0f / 256.0f) + LN_EPS);
  }
}
template <int NB>
DI void ln_to_frags(const f32x16 (&v)[NB], LAS char* lds, int w, int lane) {
  float mean[NB], rstd[NB];
  ln_stats<NB>(v, lds, w, lane, mean, rstd);
  LAS char* fb = lds + L_X + w * 2048 + lane * 16;
#pragma unroll
  for (int nb = 0; nb < NB; ++nb) {
    const f32x16 n = (v[nb] - mean[nb]) * rstd[nb];
#pragma unroll
    for (int s = 0; s < 2; ++s) *(LAS bf16x8*)(fb + nb * 16384 + s * 1024) = pack8(n, s);
  }
  lds_barrier();
}

template <int P0, int NP>
DI void ff_block(Stream& st, LAS char* lds, int w, int lane, f32x16 (&out)[1]) {
  out[0] = f32x16{};
  LAS char* xb = lds + L_X + lane * 16;
  LAS char* yb = lds + L_Y + lane * 16;
  LAS char* zb = lds + L_Z + lane * 16;
  auto put = [&](LAS char* dst, const f32x16& hv) { *(LAS bf16x8*)(dst + w * 2048) = pack8(hv, 0); *(LAS bf16x8*)(dst + w * 2048 + 1024) = pack8(hv, 1); };
  f32x16 hn[1];
  hn[0] = f32x16{}; gemm_k256<1, false, P0 + 0, NP>(st, hn, xb, NoWork{});
  put(yb, hn[0]);
  hn[0] = f32x16{}; gemm_k256<1, false, P0 + 1, NP>(st, hn, xb, SiluWork{yb + w * 2048});
  lds_barrier(); put(zb, hn[0]);
  gemm_k256<1, false, P0 + 2, NP>(st, out, yb, NoWork{});
  hn[0] = f32x16{}; gemm_k256<1, false, P0 + 3, NP>(st, hn, xb, SiluWork{zb + w * 2048});
  lds_barrier(); put(yb, hn[0]);
  gemm_k256<1, false, P0 + 4, NP>(st, out, zb, NoWork{});
  hn[0] = f32x16{}; gemm_k256<1, false, P0 + 5, NP>(st, hn, xb, SiluWork{yb + w * 2048});
  lds_barrier(); put(zb, hn[0]);
  gemm_k256<1, false, P0 + 6, NP>(st, out, yb, SiluWork{zb + w * 2048});
  lds_barrier();
  gemm_k256<1, false, P0 + 7, NP>(st, out, zb, NoWork{});
}

DI void l2_warm(const char* p, int kb_total, int idx, int lane, u32x4& t) {
  const int per = (kb_total + 247) / 248;
  int b = idx * per, e = b + per; if (e > kb_total) e = kb_total;
  for (int i = b; i < e; ++i)
    asm volatile("global_load_dwordx4 %0, %1, %2" : "+v"(t) : "v"(lane * 16 + i * 1024), "s"(p));
}

__global__ __launch_bounds__(512, 2) void k1_kernel(const char* __restrict__ x, char* __restrict__ ws) {
  extern __shared__ __attribute__((aligned(16))) char smem[];
  LAS char* lds = (LAS char*)smem;
  const int lane = threadIdx.x & 63, w = __builtin_amdgcn_readfirstlane(threadIdx.x >> 6), r = lane & 31, h = lane >> 5;
  const int tb = blockIdx.x;
  if (tb == 0 && threadIdx.x < 256) *(unsigned*)(ws + WS_FLAG + threadIdx.x * 64) = 0u;
  STAMP(0, 0);
  f32x16 xv[1];
  Stream st;
  u32x4 wt = {0u, 0u, 0u, 0u};
  {
    const char* xrow = x + (size_t)(tb * 32 + r) * 512 + (w * 32 + 4 * h) * 2;
    u32x2 u[4];
#pragma unroll
    for (int g = 0; g < 4; ++g) u[g] = *(const u32x2*)(xrow + g * 16);
    asm volatile("s_barrier" ::: "memory");
    SB();
    st.start(ws + WS_WT1 + (size_t)w * NP1 * 16384 + lane * 16);
    SB();
#pragma unroll
    for (int g = 0; g < 4; ++g) {
      xv[0][4 * g] = bflo(u[g][0]); xv[0][4 * g + 1] = bfhi(u[g][0]); xv[0][4 * g + 2] = bflo(u[g][1]); xv[0][4 * g + 3] = bfhi(u[g][1]);
    }
  }
  STAMP(0, 1);
  ln_to_frags<1>(xv, lds, w, lane);
  STAMP(0, 2);
  f32x16 acc[1];
  ff_block<0, NP1>(st, lds, w, lane, acc);
  asm volatile("" :: "v"(wt));
  STAMP(0, 3);
#pragma unroll
  for (int i = 0; i < 16; ++i) xv[0][i] = rbf(xv[0][i] + 0.5f * acc[0][i]);
#pragma unroll
  for (int s = 0; s < 2; ++s) stg16((char*)(ws + WS_X1F + ((size_t)tb * 16 + 2 * w + s) * 1024 + lane * 16), pack8(xv[0], s));
  ln_to_frags<1>(xv, lds, w, lane);
  STAMP(0, 4);
  const int b = tb / NBLK, blk = tb - b * NBLK, bh = b * 4 + (w >> 1);
  char* kvdst = ws + WS_KVF + (size_t)(bh * NBLK + blk) * 8192 + (w & 1) * 2048 + lane * 16;
  {
    f32x16 a[1]; a[0] = f32x16{};
    gemm_k256<1, false, 8, NP1>(st, a, lds + L_X + lane * 16, NoWork{});
    a[0] = a[0] * QSCALE;
    char* dst = ws + WS_QF + (size_t)(bh * NBLK + blk) * 4096 + (w & 1) * 2048 + lane * 16;
    stg16((char*)(dst), pack8(a[0], 0)); stg16((char*)(dst + 1024), pack8(a[0], 1));
  }
  {
    f32x16 a[1]; a[0] = f32x16{};
    gemm_k256<1, false, 9, NP1>(st, a, lds + L_X + lane * 16, NoWork{});
    stg16((char*)(kvdst), pack8(a[0], 0)); stg16((char*)(kvdst + 1024), pack8(a[0], 1));
  }
  STAMP(0, 5);
  {
    f32x16 a[1]; a[0] = f32x16{};
    gemm_k256<1, true, 10, NP1>(st, a, lds + L_X + lane * 16, NoWork{});
    stg16((char*)(kvdst + 4096), pack8(a[0], 0)); stg16((char*)(kvdst + 5120), pack8(a[0], 1));
  }
  STAMP(0, 29);
}

constexpr int A_KG = 2, A_SBLK = A_KG * 2, A_HST = A_SBLK * 4096, A_NST = (NBLK + A_SBLK - 1) / A_SBLK, A_NFULL = NBLK / A_SBLK;
constexpr int A_VOFF = 3 * A_HST, A_LDS = 6 * A_HST;
DI void attn_body(char* __restrict__ ws, LAS char* lds) {
  const int lane = threadIdx.x & 63, w = __builtin_amdgcn_readfirstlane(threadIdx.x >> 6);
  const int bh = blockIdx.x & 7, qg = blockIdx.x >> 3, qi = w & 3, kg = w >> 2;
  const int qb_raw = qg * 4 + qi, qb = qb_raw < NBLK ? qb_raw : NBLK - 1;
  STAMP(1, 0);
  const char* kv = ws + WS_KVF + (size_t)bh * NBLK * 8192;
  const unsigned lds0 = (unsigned)(uintptr_t)lds;
  const unsigned voff = (w >> 1) * 8192 + (w & 1) * 2048 + lane * 16;
  auto dma2 = [&](int st, int isv, unsigned ldsoff) {
    asm volatile("s_mov_b32 m0, %2\n\ts_nop 0\n\tglobal_load_lds_dwordx4 %0, %1\n\tglobal_load_lds_dwordx4 %0, %1 offset:1024"
                 :: "v"(voff), "s"(kv + (size_t)st * (2 * A_HST) + isv * 4096), "s"(lds0 + ldsoff + w * 2048));
  };
  dma2(0, 0, 0); dma2(1, 0, A_HST); dma2(0, 1, A_VOFF); dma2(2, 0, 2 * A_HST); dma2(1, 1, A_VOFF + A_HST);
  bf16x8 qf[4];
#pragma unroll
  for (int f = 0; f < 4; ++f) qf[f] = *(const bf16x8*)(ws + WS_QF + ((size_t)(bh * NBLK + qb) * 4 + f) * 1024 + lane * 16);
#pragma unroll
  for (int f = 0; f < 4; ++f) asm volatile("" : "+v"(qf[f]));
  u32x4 wt = {0u, 0u, 0u, 0u};
  const int rem = NBLK - A_NFULL * A_SBLK - kg * 2;
  const int ntail = rem < 0 ? 0 : (rem > 2 ? 2 : rem);
  LAS const char* base = lds + kg * 8192 + lane * 16;
  f32x16 o0 = f32x16{}, o1 = f32x16{}, negr, sa, sb;
  f32x4 lacc = {0.f, 0.f, 0.f, 0.f};
  bf16x8 sel;
  { const short one = (((lane >> 4) ^ lane) & 1) ? (short)0 : (short)0x3f80;
#pragma unroll
    for (int j = 0; j < 8; ++j) sel[j] = one; }
  bf16x8 kf[4], vf[4], kg2[4], vg2[4]; unsigned pa[8], pb[8];
#pragma unroll
  for (int i = 0; i < 8; ++i) pa[i] = 0u;
  float ref;
  asm volatile("s_waitcnt vmcnt(0)\n\ts_barrier" : "+v"(wt) :: "memory");
  {
#pragma unroll
    for (int f = 0; f < 4; ++f) kf[f] = ldsfrag(base + f * 1024);
    f32x16 z = f32x16{};
    sa = MFMA(kf[0], qf[0], z); sa = MFMA(kf[1], qf[1], sa); sa = MFMA(kf[2], qf[2], sa); sa = MFMA(kf[3], qf[3], sa);
    float mx = sa[0];
#pragma unroll
    for (int i = 1; i < 16; ++i) mx = fmaxf(mx, sa[i]);
    ref = halfmax(mx) + 7.0f;
#pragma unroll
    for (int i = 0; i < 16; ++i) { sa[i] -= ref; negr[i] = -ref; }
#pragma unroll
    for (int f = 0; f < 4; ++f) { kf[f] = ldsfrag(base + 4096 + f * 1024); vf[f] = bf16x8{}; }
  }
#define SLOT3(MF, RF, i) do { MF; RF; e[2*(i)] = __builtin_amdgcn_exp2f(SC_[2*(i)]); e[2*(i)+1] = __builtin_amdgcn_exp2f(SC_[2*(i)+1]); \
    if ((i) > 0) PN_[(i)-1] = pk2(e[2*(i)-2], e[2*(i)-1]); SB(); } while (0)
  auto iter = [&](f32x16& SC_, f32x16& SN_, unsigned (&PP_)[8], unsigned (&PN_)[8], bf16x8 (&KC)[4], bf16x8 (&VC)[4], bf16x8 (&KN)[4], bf16x8 (&VN)[4], LAS const char* kn, LAS const char* vc) {
    float e[16];
    const bf16x8 p0 = __builtin_bit_cast(bf16x8, (u32x4){PP_[0], PP_[1], PP_[2], PP_[3]}), p1 = __builtin_bit_cast(bf16x8, (u32x4){PP_[4], PP_[5], PP_[6], PP_[7]});
    SB();
    SLOT3(SN_ = MFMA(KC[0], qf[0], negr), KN[0] = ldsfrag(kn), 0);
    SLOT3(o0 = MFMA(VC[0], p0, o0), VN[0] = ldsfrag(vc), 1);
    SLOT3(SN_ = MFMA(KC[1], qf[1], SN_), KN[1] = ldsfrag(kn + 1024), 2);
    SLOT3(o1 = MFMA(VC[2], p0, o1); lacc = MFMA16(sel, p0, lacc), VN[2] = ldsfrag(vc + 2048), 3);
    SLOT3(SN_ = MFMA(KC[2], qf[2], SN_), KN[2] = ldsfrag(kn + 2048), 4);
    SLOT3(o0 = MFMA(VC[1], p1, o0), VN[1] = ldsfrag(vc + 1024), 5);
    SLOT3(SN_ = MFMA(KC[3], qf[3], SN_), KN[3] = ldsfrag(kn + 3072), 6);
    SLOT3(o1 = MFMA(VC[3], p1, o1); lacc = MFMA16(sel, p1, lacc), VN[3] = ldsfrag(vc + 3072), 7);
    PN_[7] = pk2(e[14], e[15]);
    const unsigned orall = (PN_[0] | PN_[1] | PN_[2]) | (PN_[3] | PN_[4] | PN_[5]) | (PN_[6] | PN_[7]);
    if (__builtin_expect(__any((orall & 0x40004000u) != 0u), 0)) {
      float mx = SC_[0];
#pragma unroll
      for (int i = 1; i < 16; ++i) mx = fmaxf(mx, SC_[i]);
      mx = halfmax(mx);
      const float delta = (mx >= 0.5f) ? mx + 7.0f : 0.0f;
      const float alpha = __builtin_amdgcn_exp2f(-delta);
      const float alo = __shfl_xor(alpha, 16);
      const float aA = (lane & 16) ? alo : alpha, aB = (lane & 16) ? alpha : alo;
      lacc = lacc * (f32x4){aA, aB, aA, aB};
      o0 = o0 * alpha; o1 = o1 * alpha; negr = negr - delta; SN_ = SN_ - delta;
      asm volatile("" : "+v"(o0), "+v"(o1), "+v"(negr), "+v"(SN_), "+v"(lacc));
      ref = -negr[0];
#pragma unroll
      for (int i = 0; i < 8; ++i) PN_[i] = pk2(__builtin_amdgcn_exp2f(SC_[2*i] - delta), __builtin_amdgcn_exp2f(SC_[2*i+1] - delta));
    }
  };
  STAMP(1, 1);
  int r0 = 0, r1 = A_HST, r2 = 2 * A_HST;
  for (int st = 0; st < A_NFULL; ++st) {
    asm volatile("s_waitcnt vmcnt(4) lgkmcnt(0)\n\ts_barrier" ::: "memory");
    dma2(st + 3 < A_NST ? st + 3 : A_NST - 1, 0, r0); dma2(st + 2 < A_NST ? st + 2 : A_NST - 1, 1, A_VOFF + r2);
    iter(sa, sb, pa, pb, kf, vf, kg2, vg2, base + r1, base + A_VOFF + r0);
    iter(sb, sa, pb, pa, kg2, vg2, kf, vf, base + r1 + 4096, base + A_VOFF + r0 + 4096);
    const int t = r0; r0 = r1; r1 = r2; r2 = t;
  }
  asm volatile("s_waitcnt vmcnt(0) lgkmcnt(0)\n\ts_barrier" ::: "memory");
  if (ntail > 0) {
    iter(sa, sb, pa, pb, kf, vf, kg2, vg2, base + r1, base + A_VOFF + r0);
    if (ntail > 1) iter(sb, sa, pb, pa, kg2, vg2, kf, vf, base + r1 + 4096, base + A_VOFF + r0 + 4096);
    else {
#pragma unroll
      for (int i = 0; i < 8; ++i) pa[i] = pb[i];
#pragma unroll
      for (int i = 0; i < 4; ++i) vf[i] = vg2[i];
    }
  }
  {
    const bf16x8 p0 = __builtin_bit_cast(bf16x8, (u32x4){pa[0], pa[1], pa[2], pa[3]}), p1 = __builtin_bit_cast(bf16x8, (u32x4){pa[4], pa[5], pa[6], pa[7]});
    o0 = MFMA(vf[0], p0, o0); o1 = MFMA(vf[2], p0, o1); o0 = MFMA(vf[1], p1, o0); o1 = MFMA(vf[3], p1, o1);
    lacc = MFMA16(sel, p0, lacc); lacc = MFMA16(sel, p1, lacc);
  }
  float lsum = (lane & 16) ? lacc[1] : lacc[0];
  STAMP(1, 2);
  lds_barrier();
  if (kg > 0) {
    LAS char* cb = lds + ((kg - 1) * 4 + qi) * 9216 + lane * 16;
#pragma unroll
    for (int g = 0; g < 4; ++g) {
      *(LAS f32x4*)(cb + g * 1024) = (f32x4){o0[4 * g], o0[4 * g + 1], o0[4 * g + 2], o0[4 * g + 3]};
      *(LAS f32x4*)(cb + 4096 + g * 1024) = (f32x4){o1[4 * g], o1[4 * g + 1], o1[4 * g + 2], o1[4 * g + 3]};
    }
    *(LAS f32x2*)(lds + ((kg - 1) * 4 + qi) * 9216 + 8192 + lane * 8) = (f32x2){ref, lsum};
  }
  lds_barrier();
  if (kg == 0 && qb_raw < NBLK) {
#pragma unroll
    for (int k = 1; k < A_KG; ++k) {
      LAS char* cb = lds + ((k - 1) * 4 + qi) * 9216 + lane * 16;
      const f32x2 rl = *(LAS f32x2*)(lds + ((k - 1) * 4 + qi) * 9216 + 8192 + lane * 8);
      const float m = fmaxf(ref, rl[0]);
      const float c0 = __builtin_amdgcn_exp2f(ref - m), c1 = __builtin_amdgcn_exp2f(rl[0] - m);
      lsum = lsum * c0 + rl[1] * c1; ref = m;
#pragma unroll
      for (int g = 0; g < 4; ++g) {
        const f32x4 u0 = *(LAS f32x4*)(cb + g * 1024), u1 = *(LAS f32x4*)(cb + 4096 + g * 1024);
#pragma unroll
        for (int j = 0; j < 4; ++j) { o0[4 * g + j] = o0[4 * g + j] * c0 + u0[j] * c1; o1[4 * g + j] = o1[4 * g + j] * c0 + u1[j] * c1; }
      }
    }
    const float inv = 1.0f / lsum;
#pragma unroll
    for (int i = 0; i < 16; ++i) { o0[i] *= inv; o1[i] *= inv; }
    char* dst = ws + WS_ATTF + ((size_t)((bh >> 2) * NBLK + qb) * 16 + (bh & 3) * 4) * 1024 + lane * 16;
    const bf16x8 f0 = pack8(o0, 0), f1 = pack8(o0, 1), f2 = pack8(o1, 0), f3 = pack8(o1, 1);
    asm volatile("global_store_dwordx4 %0, %1, off sc1\n\tglobal_store_dwordx4 %0, %2, off offset:1024 sc1\n\t"
                 "global_store_dwordx4 %0, %3, off offset:2048 sc1\n\tglobal_store_dwordx4 %0, %4, off offset:3072 sc1\n\ts_nop 1"
                 :: "v"(dst), "v"(f0), "v"(f1), "v"(f2), "v"(f3) : "memory");
  }
  asm volatile("s_waitcnt vmcnt(0) lgkmcnt(0)\n\ts_barrier" ::: "memory");
  if (threadIdx.x == 0) __hip_atomic_store((unsigned*)(ws + WS_FLAG + blockIdx.x * 64), 1u, __ATOMIC_RELAXED, __HIP_MEMORY_SCOPE_AGENT);
  STAMP(1, 29);
}

__global__ __launch_bounds__(512, 2) void k23_kernel(const char* __restrict__ dw, char* __restrict__ ws, char* __restrict__ out) {
  extern __shared__ __attribute__((aligned(16))) char smem[];
  LAS char* lds = (LAS char*)smem;
  attn_body(ws, lds);
  if (blockIdx.x >= NTB) return;
  const int lane = threadIdx.x & 63, w = __builtin_amdgcn_readfirstlane(threadIdx.x >> 6), r = lane & 31, h = lane >> 5;
  const int tb = blockIdx.x, b = tb / NBLK, t0 = (tb - b * NBLK) * 32;
  STAMP(2, 0);
  Stream st;
  const int hc = r & 3, ht = (hc < 2) ? t0 - 2 + hc : t0 + 30 + hc;
  const bool hvalid = ht >= 0 && ht < TSEQ;
  const int hg = b * TSEQ + (ht < 0 ? 0 : (ht >= TSEQ ? TSEQ - 1 : ht));
  const unsigned hoff = (unsigned)(hg >> 5) * 16384 + ((hg & 31) + 32 * h) * 16 + w * 2048;
  const unsigned moff = (unsigned)tb * 16384 + lane * 16 + w * 2048;
  u32x4 xr[2][2];
#pragma unroll
  for (int f = 0; f < 2; ++f) {
    xr[0][f] = *(const u32x4*)(ws + WS_X1F + moff + f * 1024);
    xr[1][f] = *(const u32x4*)(ws + WS_X1F + hoff + f * 1024);
  }
  const u32x4 dwv = *(const u32x4*)(dw + (threadIdx.x < 160 ? threadIdx.x : 159) * 16);
  SB();
  st.start(ws + WS_WT3 + (size_t)w * NP3 * 16384 + lane * 16);
  SB();
  if (threadIdx.x < 160) {
    *(LAS f32x4*)(lds + L_DW + threadIdx.x * 32) = (f32x4){bflo(dwv[0]), bfhi(dwv[0]), bflo(dwv[1]), bfhi(dwv[1])};
    *(LAS f32x4*)(lds + L_DW + threadIdx.x * 32 + 16) = (f32x4){bflo(dwv[2]), bfhi(dwv[2]), bflo(dwv[3]), bfhi(dwv[3])};
  }
  {
    const int blk = tb - b * NBLK;
    int q = blk + (lane & 3) - 1; q = q < 0 ? 0 : (q >= NBLK ? NBLK - 1 : q);
    const unsigned* fp = (const unsigned*)(ws + WS_FLAG + (size_t)((q >> 2) * 8 + b * 4 + (w >> 1)) * 64);
    for (int spin = 0; spin < (1 << 16); ++spin) {
      const unsigned v = (lane < 3) ? __hip_atomic_load(fp, __ATOMIC_RELAXED, __HIP_MEMORY_SCOPE_AGENT) : 1u;
      if (__all(v == 1u)) break;
      __builtin_amdgcn_s_sleep(1);
    }
    __builtin_amdgcn_fence(__ATOMIC_ACQUIRE, "wavefront");
  }
  {
    const unsigned ly = (unsigned)(uintptr_t)(lds + L_Y + w * 2048);
    asm volatile("s_mov_b32 m0, %3\n\ts_nop 0\n\tglobal_load_lds_dwordx4 %0, %2 sc1\n\tglobal_load_lds_dwordx4 %0, %2 offset:1024 sc1\n\t"
                 "s_mov_b32 m0, %4\n\ts_nop 0\n\tglobal_load_lds_dwordx4 %1, %2 sc1\n\tglobal_load_lds_dwordx4 %1, %2 offset:1024 sc1\n\t"
                 "s_waitcnt vmcnt(0)"
                 :: "v"(moff), "v"(hoff), "s"(ws + WS_ATTF), "s"(ly), "s"(ly + 16384) : "memory");
  }
  lds_barrier();
  STAMP(2, 1);
  f32x16 xv[2];
#pragma unroll
  for (int nb = 0; nb < 2; ++nb) { unpack8(xr[nb][0], xv[nb], 0); unpack8(xr[nb][1], xv[nb], 1); }
  gemm_k256<2, false, 0, NP3>(st, xv, lds + L_Y + lane * 16, NoWork{});
  STAMP(2, 2);
  ln_to_frags<2>(xv, lds, w, lane);
  STAMP(2, 3);
  {
    f32x16 a[2], g[2];
#pragma unroll
    for (int nb = 0; nb < 2; ++nb) { a[nb] = f32x16{}; g[nb] = f32x16{}; }
    gemm_k256<2, false, 1, NP3>(st, a, lds + L_X + lane * 16, NoWork{});
    gemm_k256<2, false, 2, NP3>(st, g, lds + L_X + lane * 16, NoWork{});
    STAMP(2, 4);
    LAS char* G = lds + L_Y + w * GSZ;
    lds_barrier();
#pragma unroll
    for (int nb = 0; nb < 2; ++nb) {
      const int row = nb == 0 ? r + 2 : (hc < 2 ? hc : 32 + hc);
      const bool wr = nb == 0 || r < 4;
      const float keep = (nb == 0 || hvalid) ? 1.0f : 0.0f;
#pragma unroll
      for (int q = 0; q < 4; ++q) {
        f32x4 e;
#pragma unroll
        for (int j = 0; j < 4; ++j) e[j] = keep * a[nb][4 * q + j] * sigmoidf_(g[nb][4 * q + j]);
        if (wr) *(LAS f32x4*)(G + row * GROW + (8 * q + 4 * h) * 4) = e;
      }
    }
    asm volatile("s_waitcnt lgkmcnt(0)" ::: "memory");
    f32x16 c;
#pragma unroll
    for (int q = 0; q < 4; ++q) {
      f32x4 e = {0.f, 0.f, 0.f, 0.f};
#pragma unroll
      for (int i = 0; i < 5; ++i)
        e += *(LAS f32x4*)(G + (r + i) * GROW + (8 * q + 4 * h) * 4) * *(LAS f32x4*)(lds + L_DW + i * 1024 + (w * 32 + 8 * q + 4 * h) * 4);
#pragma unroll
      for (int j = 0; j < 4; ++j) c[4 * q + j] = siluf_(e[j]);
    }
#pragma unroll
    for (int s = 0; s < 2; ++s) *(LAS bf16x8*)(lds + L_X + (2 * w + s) * 1024 + lane * 16) = pack8(c, s);
    lds_barrier();
  }
  STAMP(2, 5);
  f32x16 x3[1];
  x3[0] = xv[0];
  gemm_k256<1, false, 3, NP3>(st, x3, lds + L_X + lane * 16, NoWork{});
  STAMP(2, 6);
  ln_to_frags<1>(x3, lds, w, lane);
  STAMP(2, 7);
  f32x16 acc[1];
  ff_block<4, NP3>(st, lds, w, lane, acc);
  x3[0] += 0.5f * acc[0];
  STAMP(2, 8);
  float mean[1], rstd[1];
  ln_stats<1>(x3, lds, w, lane, mean, rstd);
  char* orow = out + (size_t)(tb * 32 + r) * 512 + (w * 32 + 4 * h) * 2;
  const f32x16 n = (x3[0] - mean[0]) * rstd[0];
#pragma unroll
  for (int q = 0; q < 4; ++q) *(u32x2*)(orow + q * 16) = (u32x2){pk2(n[4 * q], n[4 * q + 1]), pk2(n[4 * q + 2], n[4 * q + 3])};
  STAMP(2, 29);
}

extern "C" void kernel_launch(void* const* d_in, const int* in_sizes, int n_in, void* d_out, int out_size, void* d_ws, size_t ws_size,
                              hipStream_t stream) {
  const char* x = (const char*)d_in[0];
  const char* w1a = (const char*)d_in[1];
  const char* w1b = (const char*)d_in[2];
  const char* wqkv = (const char*)d_in[3];
  const char* wo = (const char*)d_in[4];
  const char* pw1 = (const char*)d_in[5];
  const char* dw = (const char*)d_in[6];
  const char* pw2 = (const char*)d_in[7];
  const char* w2a = (const char*)d_in[8];
  const char* w2b = (const char*)d_in[9];
  char* ws = (char*)d_ws;
  (void)hipFuncSetAttribute((const void*)k1_kernel, hipFuncAttributeMaxDynamicSharedMemorySize, L_TOTAL);
  (void)hipFuncSetAttribute((const void*)k23_kernel, hipFuncAttributeMaxDynamicSharedMemorySize, A_LDS);
  WPtrs wp{w1a, w1b, wqkv, wo, pw1, pw2, w2a, w2b};
  prep_kernel<<<(NP1 + NP3) * 8, 256, 0, stream>>>(wp, ws);
  k1_kernel<<<NTB, 512, L_TOTAL, stream>>>(x, ws);
  k23_kernel<<<256, 512, A_LDS, stream>>>(dw, ws, (char*)d_out);
}
```

```cpp
#include <hip/hip_runtime.h>
#include <hip/hip_bf16.h>
#include <stdint.h>

typedef short bf16x8 __attribute__((ext_vector_type(8)));
typedef short s16x4 __attribute__((ext_vector_type(4)));
typedef float f32x16 __attribute__((ext_vector_type(16)));
typedef float f32x2 __attribute__((ext_vector_type(2)));
typedef __bf16 bf16x2_t __attribute__((ext_vector_type(2)));
typedef unsigned u32x4 __attribute__((ext_vector_type(4)));
typedef unsigned u32x2 __attribute__((ext_vector_type(2)));
#define LAS __attribute__((address_space(3)))
#define DI __device__ __forceinline__
#define MFMA(a, b, c) __builtin_amdgcn_mfma_f32_32x32x16_bf16((a), (b), (c), 0, 0, 0)
#define MFMA16(a, b, c) __builtin_amdgcn_mfma_f32_16x16x32_bf16((a), (b), (c), 0, 0, 0)
#define SB() __builtin_amdgcn_sched_barrier(0)
#define STAMP(k, i) do {} while (0)
typedef float f32x4 __attribute__((ext_vector_type(4)));

constexpr int TSEQ = 4000, NBLK = 125, NTB = 250;
constexpr size_t WS_X1F = 0;
constexpr size_t WS_QF = 4u << 20;
constexpr size_t WS_KVF = 8u << 20;
constexpr size_t WS_ATTF = 17u << 20;
constexpr size_t WS_XNF = 28u << 20;
constexpr size_t WS_FLAG = 22u << 20;
constexpr float LN_EPS = 1e-5f;
constexpr float LOG2E = 1.4426950408889634f;
constexpr float QSCALE = 0.125f * LOG2E;

DI unsigned pk2(float a, float b) { f32x2 v = {a, b}; return __builtin_bit_cast(unsigned, __builtin_convertvector(v, bf16x2_t)); }
DI float bflo(unsigned u) { return __uint_as_float(u << 16); }
DI float bfhi(unsigned u) { return __uint_as_float(u & 0xffff0000u); }
DI float rbf(float a) { return bflo(pk2(a, a)); }
DI bf16x8 pack8(const f32x16& v, int s) {
  u32x4 p = {pk2(v[8 * s], v[8 * s + 1]), pk2(v[8 * s + 2], v[8 * s + 3]), pk2(v[8 * s + 4], v[8 * s + 5]), pk2(v[8 * s + 6], v[8 * s + 7])};
  return __builtin_bit_cast(bf16x8, p);
}
DI void unpack8(u32x4 p, f32x16& v, int s) {
#pragma unroll
  for (int i = 0; i < 4; ++i) { v[8 * s + 2 * i] = bflo(p[i]); v[8 * s + 2 * i + 1] = bfhi(p[i]); }
}
DI float halfsum(float x) {
  auto r = __builtin_amdgcn_permlane32_swap(__float_as_uint(x), __float_as_uint(x), false, false);
  return __uint_as_float(r[0]) + __uint_as_float(r[1]);
}
DI float halfmax(float x) {
  auto r = __builtin_amdgcn_permlane32_swap(__float_as_uint(x), __float_as_uint(x), false, false);
  return fmaxf(__uint_as_float(r[0]), __uint_as_float(r[1]));
}
DI float sigmoidf_(float x) { return __builtin_amdgcn_rcpf(1.0f + __builtin_amdgcn_exp2f(-LOG2E * x)); }
DI float siluf_(float x) { return x * sigmoidf_(x); }
DI void lds_barrier() { asm volatile("s_waitcnt lgkmcnt(0)\n\ts_barrier" ::: "memory"); }
typedef short v4i16_t __attribute__((ext_vector_type(4)));
DI s16x4 trread(LAS const char* p) { return __builtin_bit_cast(s16x4, __builtin_amdgcn_ds_read_tr16_b64_v4i16((LAS v4i16_t*)p)); }
DI void stg16(char* p, bf16x8 v) { __builtin_nontemporal_store(v, (bf16x8*)p); }
DI bf16x8 ldsfrag(LAS const char* p) { return *(LAS const bf16x8*)p; }

constexpr size_t WS_WT1 = 24u << 20, WS_WT3 = 26u << 20;
constexpr int NP1 = 11, NP3 = 12;
struct WPtrs { const char *w1a, *w1b, *wqkv, *wo, *pw1, *pw2, *w2a, *w2b; };
DI void ff_panel(const char* wa, const char* wb, int p, int w2, const char*& base, unsigned& ld) {
  const bool isb = (p == 2 || p == 4 || p >= 6);
  const int c = p < 2 ? p : (p == 3 ? 2 : (p == 5 ? 3 : (p == 2 ? 0 : (p == 4 ? 1 : p - 4))));
  if (isb) { base = wb + (size_t)c * 256 * 512 + w2 * 128; ld = 512; } else { base = wa + (size_t)(w2 + 4 * c) * 128; ld = 2048; }
}
struct Stream {
  const char* p;
  bf16x8 wf[2][16];
  DI void start(const char* base) {
    p = base;
#pragma unroll
    for (int i = 0; i < 16; ++i) wf[0][i] = *(const bf16x8*)(p + i * 1024);
  }
};
struct NoWork { DI void init() {} DI void step(int) {} };
struct SiluWork {
  LAS char* p;
  u32x2 raw; float x[4], e[4];
  DI void init() { raw = *(LAS u32x2*)p; }
  DI void step(int ks) {
    const int g = ks >> 2, i = ks & 3;
    if (i == 0) {
      x[0] = bflo(raw[0]); x[1] = bfhi(raw[0]); x[2] = bflo(raw[1]); x[3] = bfhi(raw[1]);
#pragma unroll
      for (int j = 0; j < 4; ++j) e[j] = -LOG2E * x[j];
      asm volatile("" : "+v"(e[0]), "+v"(e[1]), "+v"(e[2]), "+v"(e[3]), "+v"(x[0]), "+v"(x[1]), "+v"(x[2]), "+v"(x[3]));
      if (g < 3) raw = *(LAS u32x2*)(p + ((g + 1) >> 1) * 1024 + ((g + 1) & 1) * 8);
    } else if (i == 1) {
#pragma unroll
      for (int j = 0; j < 4; ++j) e[j] = __builtin_amdgcn_exp2f(e[j]);
      asm volatile("" : "+v"(e[0]), "+v"(e[1]), "+v"(e[2]), "+v"(e[3]));
    } else if (i == 2) {
#pragma unroll
      for (int j = 0; j < 4; ++j) e[j] = __builtin_amdgcn_rcpf(1.0f + e[j]);
      asm volatile("" : "+v"(e[0]), "+v"(e[1]), "+v"(e[2]), "+v"(e[3]));
    } else {
      *(LAS u32x2*)(p + (g >> 1) * 1024 + (g & 1) * 8) = (u32x2){pk2(x[0] * e[0], x[1] * e[1]), pk2(x[2] * e[2], x[3] * e[3])};
    }
  }
};
template <int NB, bool SWAP, int P, int NP, class W>
DI void gemm_k256(Stream& st, f32x16 (&acc)[NB], LAS const char* bl, W work) {
  constexpr bool REFILL = P + 2 < NP;
  asm volatile("" ::: "memory");
  bf16x8 bq[3][NB];
#pragma unroll
  for (int nb = 0; nb < NB; ++nb) { bq[0][nb] = ldsfrag(bl + nb * 16384); bq[1][nb] = ldsfrag(bl + nb * 16384 + 1024); }
  work.init();
#pragma unroll
  for (int ks = 0; ks < 16; ++ks) {
    SB();
#pragma unroll
    for (int nb = 0; nb < NB; ++nb)
      acc[nb] = SWAP ? MFMA(bq[ks % 3][nb], st.wf[P & 1][ks], acc[nb]) : MFMA(st.wf[P & 1][ks], bq[ks % 3][nb], acc[nb]);
    if (ks < 14) {
#pragma unroll
      for (int nb = 0; nb < NB; ++nb) bq[(ks + 2) % 3][nb] = ldsfrag(bl + nb * 16384 + (ks + 2) * 1024);
    }
    if (P == 0) st.wf[1][ks] = *(const bf16x8*)(st.p + 16384 + ks * 1024);
    if (REFILL && ks > 0) st.wf[P & 1][ks - 1] = *(const bf16x8*)(st.p + (P + 2) * 16384 + (ks - 1) * 1024);
    work.step(ks);
  }
#pragma unroll
  for (int nb = 0; nb < NB; ++nb) asm volatile("" : "+v"(acc[nb]));
  SB();
  if (REFILL) st.wf[P & 1][15] = *(const bf16x8*)(st.p + (P + 2) * 16384 + 15 * 1024);
}

constexpr int L_X = 0, L_Y = L_X + 16384, L_Z = L_Y + 16384;
constexpr int L_STAT = L_Z + 16384 + 9216;
constexpr int GROW = 144, GSZ = 36 * GROW;
constexpr int L_DW = L_STAT + 4096;
constexpr int L_TOTAL = L_DW + 5120;

DI float sum16(const f32x16& t) {
  return (((t[0] + t[1]) + (t[2] + t[3])) + ((t[4] + t[5]) + (t[6] + t[7]))) + (((t[8] + t[9]) + (t[10] + t[11])) + ((t[12] + t[13]) + (t[14] + t[15])));
}
template <int NB>
DI void ln_stats(const f32x16 (&v)[NB], LAS char* lds, int w, int lane, float (&mean)[NB], float (&rstd)[NB]) {
  const int r = lane & 31;
#pragma unroll
  for (int nb = 0; nb < NB; ++nb) {
    const float mw = halfsum(sum16(v[nb])) * (1.0f / 32.0f);
    const f32x16 d = v[nb] - mw;
    const float m2 = halfsum(sum16(d * d));
    if (lane < 32) *(LAS f32x2*)(lds + L_STAT + ((nb * 32 + r) * 8 + w) * 8) = (f32x2){mw, m2};
  }
  lds_barrier();
#pragma unroll
  for (int nb = 0; nb < NB; ++nb) {
    f32x4 s[4];
#pragma unroll
    for (int i = 0; i < 4; ++i) s[i] = *(LAS f32x4*)(lds + L_STAT + (nb * 32 + r) * 64 + i * 16);
    const float m = 0.125f * (((s[0][0] + s[0][2]) + (s[1][0] + s[1][2])) + ((s[2][0] + s[2][2]) + (s[3][0] + s[3][2])));
    float M2 = 0.f;
#pragma unroll
    for (int i = 0; i < 4; ++i) { const float e0 = s[i][0] - m, e1 = s[i][2] - m; M2 += s[i][1] + s[i][3] + 32.0f * (e0 * e0 + e1 * e1); }
    mean[nb] = m; rstd[nb] = __builtin_amdgcn_rsqf(M2 * (1.0f / 256.0f) + LN_EPS);
  }
}
template <int NB>
DI void ln_to_frags(const f32x16 (&v)[NB], LAS char* lds, int w, int lane) {
  float mean[NB], rstd[NB];
  ln_stats<NB>(v, lds, w, lane, mean, rstd);
  LAS char* fb = lds + L_X + w * 2048 + lane * 16;
#pragma unroll
  for (int nb = 0; nb < NB; ++nb) {
    const f32x16 n = (v[nb] - mean[nb]) * rstd[nb];
#pragma unroll
    for (int s = 0; s < 2; ++s) *(LAS bf16x8*)(fb + nb * 16384 + s * 1024) = pack8(n, s);
  }
  lds_barrier();
}

constexpr int NPREP = (NP1 + NP3) * 4;
__global__ __launch_bounds__(512) void prep_kernel(WPtrs wp, const char* __restrict__ x, char* __restrict__ ws) {
  extern __shared__ __attribute__((aligned(16))) char sm[];
  const int lane = threadIdx.x & 63, wv = __builtin_amdgcn_readfirstlane(threadIdx.x >> 6);
  if (blockIdx.x >= NPREP) {
    const int tb = blockIdx.x - NPREP;
    if (tb == 0 && threadIdx.x < 256) *(unsigned*)(ws + WS_FLAG + threadIdx.x * 64) = 0u;
    const char* xrow = x + (size_t)(tb * 32 + (lane & 31)) * 512 + (wv * 32 + 4 * (lane >> 5)) * 2;
    f32x16 xv[1];
#pragma unroll
    for (int g = 0; g < 4; ++g) {
      const u32x2 u = *(const u32x2*)(xrow + g * 16);
      xv[0][4 * g] = bflo(u[0]); xv[0][4 * g + 1] = bfhi(u[0]); xv[0][4 * g + 2] = bflo(u[1]); xv[0][4 * g + 3] = bfhi(u[1]);
    }
    float mean[1], rstd[1];
    ln_stats<1>(xv, (LAS char*)sm, wv, lane, mean, rstd);
    const f32x16 n = (xv[0] - mean[0]) * rstd[0];
    char* dst = ws + WS_XNF + ((size_t)tb * 16 + 2 * wv) * 1024 + lane * 16;
    *(bf16x8*)dst = pack8(n, 0); *(bf16x8*)(dst + 1024) = pack8(n, 1);
    return;
  }
  const int g = blockIdx.x * 8 + wv;
  const bool k3 = g >= 4 * NP1 * 8;
  const int gg = k3 ? g - 4 * NP1 * 8 : g, np = k3 ? NP3 : NP1;
  const int w2 = gg / (np * 8), p = (gg >> 3) % np, sl = gg & 7;
  const char* base; unsigned ld;
  if (!k3) {
    if (p < 8) ff_panel(wp.w1a, wp.w1b, p, w2, base, ld);
    else { base = wp.wqkv + (p - 8) * 512 + w2 * 128; ld = 1536; }
  } else {
    if (p == 0) { base = wp.wo + w2 * 128; ld = 512; }
    else if (p < 3) { base = wp.pw1 + (p - 1) * 512 + w2 * 128; ld = 1024; }
    else if (p == 3) { base = wp.pw2 + w2 * 128; ld = 512; }
    else ff_panel(wp.w2a, wp.w2b, p - 4, w2, base, ld);
  }
  LAS char* my = (LAS char*)sm + wv * 4096;
  const char* src = base + (size_t)(sl * 32 + (lane >> 3)) * ld + (((lane & 7) ^ (((lane >> 4) & 1) << 2)) << 4);
  u32x4 t[4];
#pragma unroll
  for (int i = 0; i < 4; ++i) t[i] = *(const u32x4*)(src + (size_t)i * 8 * ld);
#pragma unroll
  for (int i = 0; i < 4; ++i) *(LAS u32x4*)(my + i * 1024 + lane * 16) = t[i];
  asm volatile("s_waitcnt lgkmcnt(0)" ::: "memory");
  const int h = lane >> 5, cb = (lane >> 4) & 1, q = (lane & 15) >> 2, pp = lane & 3;
  char* dst = ws + (k3 ? WS_WT3 : WS_WT1) + (size_t)((2 * w2) * np + p) * 16384 + sl * 2048 + lane * 16;
#pragma unroll
  for (int i = 0; i < 4; ++i) {
    LAS const char* rp = my + (4 * h + q) * 128 + (((i & 1) ^ (q >> 1)) * 64) + cb * 32 + pp * 8 + (i >> 1) * 2048;
    s16x4 l = trread(rp), hh = trread(rp + 1024);
    *(bf16x8*)(dst + (size_t)(i & 1) * np * 16384 + (i >> 1) * 1024) = __builtin_shufflevector(l, hh, 0, 1, 2, 3, 4, 5, 6, 7);
  }
}

template <int P0, int NP>
DI void ff_block(Stream& st, LAS char* lds, int w, int lane, f32x16 (&out)[1]) {
  out[0] = f32x16{};
  LAS char* xb = lds + L_X + lane * 16;
  LAS char* yb = lds + L_Y + lane * 16;
  LAS char* zb = lds + L_Z + lane * 16;
  auto put = [&](LAS char* dst, const f32x16& hv) { *(LAS bf16x8*)(dst + w * 2048) = pack8(hv, 0); *(LAS bf16x8*)(dst + w * 2048 + 1024) = pack8(hv, 1); };
  f32x16 hn[1];
  hn[0] = f32x16{}; gemm_k256<1, false, P0 + 0, NP>(st, hn, xb, NoWork{});
  put(yb, hn[0]);
  hn[0] = f32x16{}; gemm_k256<1, false, P0 + 1, NP>(st, hn, xb, SiluWork{yb + w * 2048});
  lds_barrier(); put(zb, hn[0]);
  gemm_k256<1, false, P0 + 2, NP>(st, out, yb, NoWork{});
  hn[0] = f32x16{}; gemm_k256<1, false, P0 + 3, NP>(st, hn, xb, SiluWork{zb + w * 2048});
  lds_barrier(); put(yb, hn[0]);
  gemm_k256<1, false, P0 + 4, NP>(st, out, zb, NoWork{});
  hn[0] = f32x16{}; gemm_k256<1, false, P0 + 5, NP>(st, hn, xb, SiluWork{yb + w * 2048});
  lds_barrier(); put(zb, hn[0]);
  gemm_k256<1, false, P0 + 6, NP>(st, out, yb, SiluWork{zb + w * 2048});
  lds_barrier();
  gemm_k256<1, false, P0 + 7, NP>(st, out, zb, NoWork{});
}

__global__ __launch_bounds__(512, 2) void k1_kernel(const char* __restrict__ x, char* __restrict__ ws) {
  extern __shared__ __attribute__((aligned(16))) char smem[];
  LAS char* lds = (LAS char*)smem;
  const int lane = threadIdx.x & 63, w = __builtin_amdgcn_readfirstlane(threadIdx.x >> 6), r = lane & 31, h = lane >> 5;
  const int tb = blockIdx.x;
  STAMP(0, 0);
  asm volatile("s_mov_b32 m0, %2\n\ts_nop 0\n\tglobal_load_lds_dwordx4 %0, %1\n\tglobal_load_lds_dwordx4 %0, %1 offset:1024"
               :: "v"(tb * 16384 + w * 2048 + lane * 16), "s"(ws + WS_XNF), "s"((unsigned)(uintptr_t)(lds + L_X + w * 2048)));
  u32x2 xu[4];
  {
    const char* xrow = x + (size_t)(tb * 32 + r) * 512 + (w * 32 + 4 * h) * 2;
#pragma unroll
    for (int g = 0; g < 4; ++g) xu[g] = *(const u32x2*)(xrow + g * 16);
  }
  asm volatile("s_barrier" ::: "memory");
  Stream st;
  SB();
  st.start(ws + WS_WT1 + (size_t)w * NP1 * 16384 + lane * 16);
  SB();
  asm volatile("s_waitcnt vmcnt(16)\n\ts_barrier" : "+v"(xu[0]), "+v"(xu[1]), "+v"(xu[2]), "+v"(xu[3]) :: "memory");
  STAMP(0, 2);
  f32x16 acc[1];
  ff_block<0, NP1>(st, lds, w, lane, acc);
  STAMP(0, 3);
  f32x16 xv[1];
#pragma unroll
  for (int g = 0; g < 4; ++g) {
    xv[0][4 * g] = bflo(xu[g][0]); xv[0][4 * g + 1] = bfhi(xu[g][0]); xv[0][4 * g + 2] = bflo(xu[g][1]); xv[0][4 * g + 3] = bfhi(xu[g][1]);
  }
#pragma unroll
  for (int i = 0; i < 16; ++i) xv[0][i] = rbf(xv[0][i] + 0.5f * acc[0][i]);
#pragma unroll
  for (int s = 0; s < 2; ++s) stg16((char*)(ws + WS_X1F + ((size_t)tb * 16 + 2 * w + s) * 1024 + lane * 16), pack8(xv[0], s));
  ln_to_frags<1>(xv, lds, w, lane);
  STAMP(0, 4);
  const int b = tb / NBLK, blk = tb - b * NBLK, bh = b * 4 + (w >> 1);
  char* kvdst = ws + WS_KVF + (size_t)(bh * NBLK + blk) * 8192 + (w & 1) * 2048 + lane * 16;
  {
    f32x16 a[1]; a[0] = f32x16{};
    gemm_k256<1, false, 8, NP1>(st, a, lds + L_X + lane * 16, NoWork{});
    a[0] = a[0] * QSCALE;
    char* dst = ws + WS_QF + (size_t)(bh * NBLK + blk) * 4096 + (w & 1) * 2048 + lane * 16;
    stg16((char*)(dst), pack8(a[0], 0)); stg16((char*)(dst + 1024), pack8(a[0], 1));
  }
  {
    f32x16 a[1]; a[0] = f32x16{};
    gemm_k256<1, false, 9, NP1>(st, a, lds + L_X + lane * 16, NoWork{});
    stg16((char*)(kvdst), pack8(a[0], 0)); stg16((char*)(kvdst + 1024), pack8(a[0], 1));
  }
  STAMP(0, 5);
  {
    f32x16 a[1]; a[0] = f32x16{};
    gemm_k256<1, true, 10, NP1>(st, a, lds + L_X + lane * 16, NoWork{});
    stg16((char*)(kvdst + 4096), pack8(a[0], 0)); stg16((char*)(kvdst + 5120), pack8(a[0], 1));
  }
  STAMP(0, 29);
}

constexpr int A_KG = 2, A_SBLK = A_KG * 2, A_HST = A_SBLK * 4096, A_NST = (NBLK + A_SBLK - 1) / A_SBLK, A_NFULL = NBLK / A_SBLK;
constexpr int A_VOFF = 3 * A_HST, A_LDS = 6 * A_HST;
DI void attn_body(char* __restrict__ ws, LAS char* lds) {
  const int lane = threadIdx.x & 63, w = __builtin_amdgcn_readfirstlane(threadIdx.x >> 6);
  const int bh = blockIdx.x & 7, qg = blockIdx.x >> 3, qi = w & 3, kg = w >> 2;
  const int qb_raw = qg * 4 + qi, qb = qb_raw < NBLK ? qb_raw : NBLK - 1;
  STAMP(1, 0);
  const char* kv = ws + WS_KVF + (size_t)bh * NBLK * 8192;
  const unsigned lds0 = (unsigned)(uintptr_t)lds;
  const unsigned voff = (w >> 1) * 8192 + (w & 1) * 2048 + lane * 16;
  auto dma2 = [&](int st, int isv, unsigned ldsoff) {
    asm volatile("s_mov_b32 m0, %2\n\ts_nop 0\n\tglobal_load_lds_dwordx4 %0, %1\n\tglobal_load_lds_dwordx4 %0, %1 offset:1024"
                 :: "v"(voff), "s"(kv + (size_t)st * (2 * A_HST) + isv * 4096), "s"(lds0 + ldsoff + w * 2048));
  };
  dma2(0, 0, 0); dma2(1, 0, A_HST); dma2(0, 1, A_VOFF); dma2(2, 0, 2 * A_HST); dma2(1, 1, A_VOFF + A_HST);
  bf16x8 qf[4];
#pragma unroll
  for (int f = 0; f < 4; ++f) qf[f] = *(const bf16x8*)(ws + WS_QF + ((size_t)(bh * NBLK + qb) * 4 + f) * 1024 + lane * 16);
#pragma unroll
  for (int f = 0; f < 4; ++f) asm volatile("" : "+v"(qf[f]));
  u32x4 wt = {0u, 0u, 0u, 0u};
  const int rem = NBLK - A_NFULL * A_SBLK - kg * 2;
  const int ntail = rem < 0 ? 0 : (rem > 2 ? 2 : rem);
  LAS const char* base = lds + kg * 8192 + lane * 16;
  f32x16 o0 = f32x16{}, o1 = f32x16{}, negr, sa, sb;
  f32x4 lacc = {0.f, 0.f, 0.f, 0.f};
  bf16x8 sel;
  { const short one = (((lane >> 4) ^ lane) & 1) ? (short)0 : (short)0x3f80;
#pragma unroll
    for (int j = 0; j < 8; ++j) sel[j] = one; }
  bf16x8 kf[4], vf[4], kg2[4], vg2[4]; unsigned pa[8], pb[8];
#pragma unroll
  for (int i = 0; i < 8; ++i) pa[i] = 0u;
  float ref;
  asm volatile("s_waitcnt vmcnt(0)\n\ts_barrier" : "+v"(wt) :: "memory");
  {
#pragma unroll
    for (int f = 0; f < 4; ++f) kf[f] = ldsfrag(base + f * 1024);
    f32x16 z = f32x16{};
    sa = MFMA(kf[0], qf[0], z); sa = MFMA(kf[1], qf[1], sa); sa = MFMA(kf[2], qf[2], sa); sa = MFMA(kf[3], qf[3], sa);
    float mx = sa[0];
#pragma unroll
    for (int i = 1; i < 16; ++i) mx = fmaxf(mx, sa[i]);
    ref = halfmax(mx) + 7.0f;
#pragma unroll
    for (int i = 0; i < 16; ++i) { sa[i] -= ref; negr[i] = -ref; }
#pragma unroll
    for (int f = 0; f < 4; ++f) { kf[f] = ldsfrag(base + 4096 + f * 1024); vf[f] = bf16x8{}; }
  }
#define SLOT3(MF, RF, i) do { MF; RF; e[2*(i)] = __builtin_amdgcn_exp2f(SC_[2*(i)]); e[2*(i)+1] = __builtin_amdgcn_exp2f(SC_[2*(i)+1]); \
    if ((i) > 0) PN_[(i)-1] = pk2(e[2*(i)-2], e[2*(i)-1]); SB(); } while (0)
  auto iter = [&](f32x16& SC_, f32x16& SN_, unsigned (&PP_)[8], unsigned (&PN_)[8], bf16x8 (&KC)[4], bf16x8 (&VC)[4], bf16x8 (&KN)[4], bf16x8 (&VN)[4], LAS const char* kn, LAS const char* vc) {
    float e[16];
    const bf16x8 p0 = __builtin_bit_cast(bf16x8, (u32x4){PP_[0], PP_[1], PP_[2], PP_[3]}), p1 = __builtin_bit_cast(bf16x8, (u32x4){PP_[4], PP_[5], PP_[6], PP_[7]});
    SB();
    SLOT3(SN_ = MFMA(KC[0], qf[0], negr), KN[0] = ldsfrag(kn), 0);
    SLOT3(o0 = MFMA(VC[0], p0, o0), VN[0] = ldsfrag(vc), 1);
    SLOT3(SN_ = MFMA(KC[1], qf[1], SN_), KN[1] = ldsfrag(kn + 1024), 2);
    SLOT3(o1 = MFMA(VC[2], p0, o1); lacc = MFMA16(sel, p0, lacc), VN[2] = ldsfrag(vc + 2048), 3);
    SLOT3(SN_ = MFMA(KC[2], qf[2], SN_), KN[2] = ldsfrag(kn + 2048), 4);
    SLOT3(o0 = MFMA(VC[1], p1, o0), VN[1] = ldsfrag(vc + 1024), 5);
    SLOT3(SN_ = MFMA(KC[3], qf[3], SN_), KN[3] = ldsfrag(kn + 3072), 6);
    SLOT3(o1 = MFMA(VC[3], p1, o1); lacc = MFMA16(sel, p1, lacc), VN[3] = ldsfrag(vc + 3072), 7);
    PN_[7] = pk2(e[14], e[15]);
    const unsigned orall = (PN_[0] | PN_[1] | PN_[2]) | (PN_[3] | PN_[4] | PN_[5]) | (PN_[6] | PN_[7]);
    if (__builtin_expect(__any((orall & 0x40004000u) != 0u), 0)) {
      float mx = SC_[0];
#pragma unroll
      for (int i = 1; i < 16; ++i) mx = fmaxf(mx, SC_[i]);
      mx = halfmax(mx);
      const float delta = (mx >= 0.5f) ? mx + 7.0f : 0.0f;
      const float alpha = __builtin_amdgcn_exp2f(-delta);
      const float alo = __shfl_xor(alpha, 16);
      const float aA = (lane & 16) ? alo : alpha, aB = (lane & 16) ? alpha : alo;
      lacc = lacc * (f32x4){aA, aB, aA, aB};
      o0 = o0 * alpha; o1 = o1 * alpha; negr = negr - delta; SN_ = SN_ - delta;
      asm volatile("" : "+v"(o0), "+v"(o1), "+v"(negr), "+v"(SN_), "+v"(lacc));
      ref = -negr[0];
#pragma unroll
      for (int i = 0; i < 8; ++i) PN_[i] = pk2(__builtin_amdgcn_exp2f(SC_[2*i] - delta), __builtin_amdgcn_exp2f(SC_[2*i+1] - delta));
    }
  };
  STAMP(1, 1);
  int r0 = 0, r1 = A_HST, r2 = 2 * A_HST;
  for (int st = 0; st < A_NFULL; ++st) {
    asm volatile("s_waitcnt vmcnt(4) lgkmcnt(0)\n\ts_barrier" ::: "memory");
    dma2(st + 3 < A_NST ? st + 3 : A_NST - 1, 0, r0); dma2(st + 2 < A_NST ? st + 2 : A_NST - 1, 1, A_VOFF + r2);
    iter(sa, sb, pa, pb, kf, vf, kg2, vg2, base + r1, base + A_VOFF + r0);
    iter(sb, sa, pb, pa, kg2, vg2, kf, vf, base + r1 + 4096, base + A_VOFF + r0 + 4096);
    const int t = r0; r0 = r1; r1 = r2; r2 = t;
  }
  asm volatile("s_waitcnt vmcnt(0) lgkmcnt(0)\n\ts_barrier" ::: "memory");
  if (ntail > 0) {
    iter(sa, sb, pa, pb, kf, vf, kg2, vg2, base + r1, base + A_VOFF + r0);
    if (ntail > 1) iter(sb, sa, pb, pa, kg2, vg2, kf, vf, base + r1 + 4096, base + A_VOFF + r0 + 4096);
    else {
#pragma unroll
      for (int i = 0; i < 8; ++i) pa[i] = pb[i];
#pragma unroll
      for (int i = 0; i < 4; ++i) vf[i] = vg2[i];
    }
  }
  {
    const bf16x8 p0 = __builtin_bit_cast(bf16x8, (u32x4){pa[0], pa[1], pa[2], pa[3]}), p1 = __builtin_bit_cast(bf16x8, (u32x4){pa[4], pa[5], pa[6], pa[7]});
    o0 = MFMA(vf[0], p0, o0); o1 = MFMA(vf[2], p0, o1); o0 = MFMA(vf[1], p1, o0); o1 = MFMA(vf[3], p1, o1);
    lacc = MFMA16(sel, p0, lacc); lacc = MFMA16(sel, p1, lacc);
  }
  float lsum = (lane & 16) ? lacc[1] : lacc[0];
  STAMP(1, 2);
  lds_barrier();
  if (kg > 0) {
    LAS char* cb = lds + ((kg - 1) * 4 + qi) * 9216 + lane * 16;
#pragma unroll
    for (int g = 0; g < 4; ++g) {
      *(LAS f32x4*)(cb + g * 1024) = (f32x4){o0[4 * g], o0[4 * g + 1], o0[4 * g + 2], o0[4 * g + 3]};
      *(LAS f32x4*)(cb + 4096 + g * 1024) = (f32x4){o1[4 * g], o1[4 * g + 1], o1[4 * g + 2], o1[4 * g + 3]};
    }
    *(LAS f32x2*)(lds + ((kg - 1) * 4 + qi) * 9216 + 8192 + lane * 8) = (f32x2){ref, lsum};
  }
  lds_barrier();
  if (kg == 0 && qb_raw < NBLK) {
#pragma unroll
    for (int k = 1; k < A_KG; ++k) {
      LAS char* cb = lds + ((k - 1) * 4 + qi) * 9216 + lane * 16;
      const f32x2 rl = *(LAS f32x2*)(lds + ((k - 1) * 4 + qi) * 9216 + 8192 + lane * 8);
      const float m = fmaxf(ref, rl[0]);
      const float c0 = __builtin_amdgcn_exp2f(ref - m), c1 = __builtin_amdgcn_exp2f(rl[0] - m);
      lsum = lsum * c0 + rl[1] * c1; ref = m;
#pragma unroll
      for (int g = 0; g < 4; ++g) {
        const f32x4 u0 = *(LAS f32x4*)(cb + g * 1024), u1 = *(LAS f32x4*)(cb + 4096 + g * 1024);
#pragma unroll
        for (int j = 0; j < 4; ++j) { o0[4 * g + j] = o0[4 * g + j] * c0 + u0[j] * c1; o1[4 * g + j] = o1[4 * g + j] * c0 + u1[j] * c1; }
      }
    }
    const float inv = 1.0f / lsum;
#pragma unroll
    for (int i = 0; i < 16; ++i) { o0[i] *= inv; o1[i] *= inv; }
    char* dst = ws + WS_ATTF + ((size_t)((bh >> 2) * NBLK + qb) * 16 + (bh & 3) * 4) * 1024 + lane * 16;
    const bf16x8 f0 = pack8(o0, 0), f1 = pack8(o0, 1), f2 = pack8(o1, 0), f3 = pack8(o1, 1);
    asm volatile("global_store_dwordx4 %0, %1, off sc1\n\tglobal_store_dwordx4 %0, %2, off offset:1024 sc1\n\t"
                 "global_store_dwordx4 %0, %3, off offset:2048 sc1\n\tglobal_store_dwordx4 %0, %4, off offset:3072 sc1\n\ts_nop 1"
                 :: "v"(dst), "v"(f0), "v"(f1), "v"(f2), "v"(f3) : "memory");
  }
  asm volatile("s_waitcnt vmcnt(0) lgkmcnt(0)\n\ts_barrier" ::: "memory");
  if (threadIdx.x == 0) __hip_atomic_store((unsigned*)(ws + WS_FLAG + blockIdx.x * 64), 1u, __ATOMIC_RELAXED, __HIP_MEMORY_SCOPE_AGENT);
  STAMP(1, 29);
}

__global__ __launch_bounds__(512, 2) void k23_kernel(const char* __restrict__ dw, char* __restrict__ ws, char* __restrict__ out) {
  extern __shared__ __attribute__((aligned(16))) char smem[];
  LAS char* lds = (LAS char*)smem;
  attn_body(ws, lds);
  if (blockIdx.x >= NTB) return;
  const int lane = threadIdx.x & 63, w = __builtin_amdgcn_readfirstlane(threadIdx.x >> 6), r = lane & 31, h = lane >> 5;
  const int tb = blockIdx.x, b = tb / NBLK, t0 = (tb - b * NBLK) * 32;
  STAMP(2, 0);
  Stream st;
  const int hc = r & 3, ht = (hc < 2) ? t0 - 2 + hc : t0 + 30 + hc;
  const bool hvalid = ht >= 0 && ht < TSEQ;
  const int hg = b * TSEQ + (ht < 0 ? 0 : (ht >= TSEQ ? TSEQ - 1 : ht));
  const unsigned hoff = (unsigned)(hg >> 5) * 16384 + ((hg & 31) + 32 * h) * 16 + w * 2048;
  const unsigned moff = (unsigned)tb * 16384 + lane * 16 + w * 2048;
  u32x4 xr[2][2];
#pragma unroll
  for (int f = 0; f < 2; ++f) {
    xr[0][f] = *(const u32x4*)(ws + WS_X1F + moff + f * 1024);
    xr[1][f] = *(const u32x4*)(ws + WS_X1F + hoff + f * 1024);
  }
  const u32x4 dwv = *(const u32x4*)(dw + (threadIdx.x < 160 ? threadIdx.x : 159) * 16);
  SB();
  st.start(ws + WS_WT3 + (size_t)w * NP3 * 16384 + lane * 16);
  SB();
  if (threadIdx.x < 160) {
    *(LAS f32x4*)(lds + L_DW + threadIdx.x * 32) = (f32x4){bflo(dwv[0]), bfhi(dwv[0]), bflo(dwv[1]), bfhi(dwv[1])};
    *(LAS f32x4*)(lds + L_DW + threadIdx.x * 32 + 16) = (f32x4){bflo(dwv[2]), bfhi(dwv[2]), bflo(dwv[3]), bfhi(dwv[3])};
  }
  {
    const int blk = tb - b * NBLK;
    int q = blk + (lane & 3) - 1; q = q < 0 ? 0 : (q >= NBLK ? NBLK - 1 : q);
    const unsigned* fp = (const unsigned*)(ws + WS_FLAG + (size_t)((q >> 2) * 8 + b * 4 + (w >> 1)) * 64);
    for (int spin = 0; spin < (1 << 16); ++spin) {
      const unsigned v = (lane < 3) ? __hip_atomic_load(fp, __ATOMIC_RELAXED, __HIP_MEMORY_SCOPE_AGENT) : 1u;
      if (__all(v == 1u)) break;
      __builtin_amdgcn_s_sleep(1);
    }
    __builtin_amdgcn_fence(__ATOMIC_ACQUIRE, "wavefront");
  }
  {
    const unsigned ly = (unsigned)(uintptr_t)(lds + L_Y + w * 2048);
    asm volatile("s_mov_b32 m0, %3\n\ts_nop 0\n\tglobal_load_lds_dwordx4 %0, %2 sc1\n\tglobal_load_lds_dwordx4 %0, %2 offset:1024 sc1\n\t"
                 "s_mov_b32 m0, %4\n\ts_nop 0\n\tglobal_load_lds_dwordx4 %1, %2 sc1\n\tglobal_load_lds_dwordx4 %1, %2 offset:1024 sc1\n\t"
                 "s_waitcnt vmcnt(0)"
                 :: "v"(moff), "v"(hoff), "s"(ws + WS_ATTF), "s"(ly), "s"(ly + 16384) : "memory");
  }
  lds_barrier();
  STAMP(2, 1);
  f32x16 xv[2];
#pragma unroll
  for (int nb = 0; nb < 2; ++nb) { unpack8(xr[nb][0], xv[nb], 0); unpack8(xr[nb][1], xv[nb], 1); }
  gemm_k256<2, false, 0, NP3>(st, xv, lds + L_Y + lane * 16, NoWork{});
  STAMP(2, 2);
  ln_to_frags<2>(xv, lds, w, lane);
  STAMP(2, 3);
  {
    f32x16 a[2], g[2];
#pragma unroll
    for (int nb = 0; nb < 2; ++nb) { a[nb] = f32x16{}; g[nb] = f32x16{}; }
    gemm_k256<2, false, 1, NP3>(st, a, lds + L_X + lane * 16, NoWork{});
    gemm_k256<2, false, 2, NP3>(st, g, lds + L_X + lane * 16, NoWork{});
    STAMP(2, 4);
    LAS char* G = lds + L_Y + w * GSZ;
    lds_barrier();
#pragma unroll
    for (int nb = 0; nb < 2; ++nb) {
      const int row = nb == 0 ? r + 2 : (hc < 2 ? hc : 32 + hc);
      const bool wr = nb == 0 || r < 4;
      const float keep = (nb == 0 || hvalid) ? 1.0f : 0.0f;
#pragma unroll
      for (int q = 0; q < 4; ++q) {
        f32x4 e;
#pragma unroll
        for (int j = 0; j < 4; ++j) e[j] = keep * a[nb][4 * q + j] * sigmoidf_(g[nb][4 * q + j]);
        if (wr) *(LAS f32x4*)(G + row * GROW + (8 * q + 4 * h) * 4) = e;
      }
    }
    asm volatile("s_waitcnt lgkmcnt(0)" ::: "memory");
    f32x16 c;
#pragma unroll
    for (int q = 0; q < 4; ++q) {
      f32x4 e = {0.f, 0.f, 0.f, 0.f};
#pragma unroll
      for (int i = 0; i < 5; ++i)
        e += *(LAS f32x4*)(G + (r + i) * GROW + (8 * q + 4 * h) * 4) * *(LAS f32x4*)(lds + L_DW + i * 1024 + (w * 32 + 8 * q + 4 * h) * 4);
#pragma unroll
      for (int j = 0; j < 4; ++j) c[4 * q + j] = siluf_(e[j]);
    }
#pragma unroll
    for (int s = 0; s < 2; ++s) *(LAS bf16x8*)(lds + L_X + (2 * w + s) * 1024 + lane * 16) = pack8(c, s);
    lds_barrier();
  }
  STAMP(2, 5);
  f32x16 x3[1];
  x3[0] = xv[0];
  gemm_k256<1, false, 3, NP3>(st, x3, lds + L_X + lane * 16, NoWork{});
  STAMP(2, 6);
  ln_to_frags<1>(x3, lds, w, lane);
  STAMP(2, 7);
  f32x16 acc[1];
  ff_block<4, NP3>(st, lds, w, lane, acc);
  x3[0] += 0.5f * acc[0];
  STAMP(2, 8);
  float mean[1], rstd[1];
  ln_stats<1>(x3, lds, w, lane, mean, rstd);
  char* orow = out + (size_t)(tb * 32 + r) * 512 + (w * 32 + 4 * h) * 2;
  const f32x16 n = (x3[0] - mean[0]) * rstd[0];
#pragma unroll
  for (int q = 0; q < 4; ++q) *(u32x2*)(orow + q * 16) = (u32x2){pk2(n[4 * q], n[4 * q + 1]), pk2(n[4 * q + 2], n[4 * q + 3])};
  STAMP(2, 29);
}

extern "C" void kernel_launch(void* const* d_in, const int* in_sizes, int n_in, void* d_out, int out_size, void* d_ws, size_t ws_size,
                              hipStream_t stream) {
  const char* x = (const char*)d_in[0];
  const char* w1a = (const char*)d_in[1];
  const char* w1b = (const char*)d_in[2];
  const char* wqkv = (const char*)d_in[3];
  const char* wo = (const char*)d_in[4];
  const char* pw1 = (const char*)d_in[5];
  const char* dw = (const char*)d_in[6];
  const char* pw2 = (const char*)d_in[7];
  const char* w2a = (const char*)d_in[8];
  const char* w2b = (const char*)d_in[9];
  char* ws = (char*)d_ws;
  (void)hipFuncSetAttribute((const void*)prep_kernel, hipFuncAttributeMaxDynamicSharedMemorySize, L_TOTAL);
  (void)hipFuncSetAttribute((const void*)k1_kernel, hipFuncAttributeMaxDynamicSharedMemorySize, L_TOTAL);
  (void)hipFuncSetAttribute((const void*)k23_kernel, hipFuncAttributeMaxDynamicSharedMemorySize, A_LDS);
  WPtrs wp{w1a, w1b, wqkv, wo, pw1, pw2, w2a, w2b};
  prep_kernel<<<NPREP + NTB, 512, L_TOTAL, stream>>>(wp, x, ws);
  k1_kernel<<<NTB, 512, L_TOTAL, stream>>>(x, ws);
  k23_kernel<<<256, 512, A_LDS, stream>>>(dw, ws, (char*)d_out);
}
```

```cpp
#include <hip/hip_runtime.h>
#include <hip/hip_bf16.h>
#include <stdint.h>

typedef short bf16x8 __attribute__((ext_vector_type(8)));
typedef short s16x4 __attribute__((ext_vector_type(4)));
typedef float f32x16 __attribute__((ext_vector_type(16)));
typedef float f32x2 __attribute__((ext_vector_type(2)));
typedef __bf16 bf16x2_t __attribute__((ext_vector_type(2)));
typedef unsigned u32x4 __attribute__((ext_vector_type(4)));
typedef unsigned u32x2 __attribute__((ext_vector_type(2)));
#define LAS __attribute__((address_space(3)))
#define DI __device__ __forceinline__
#define MFMA(a, b, c) __builtin_amdgcn_mfma_f32_32x32x16_bf16((a), (b), (c), 0, 0, 0)
#define MFMA16(a, b, c) __builtin_amdgcn_mfma_f32_16x16x32_bf16((a), (b), (c), 0, 0, 0)
#define SB() __builtin_amdgcn_sched_barrier(0)
#define STAMP(k, i) do {} while (0)
typedef float f32x4 __attribute__((ext_vector_type(4)));

constexpr int TSEQ = 4000, NBLK = 125, NTB = 250;
constexpr size_t WS_X1F = 0;
constexpr size_t WS_QF = 4u << 20;
constexpr size_t WS_KVF = 8u << 20;
constexpr size_t WS_ATTF = 17u << 20;
constexpr size_t WS_FLAG = 22u << 20;
constexpr float LN_EPS = 1e-5f;
constexpr float LOG2E = 1.4426950408889634f;
constexpr float QSCALE = 0.125f * LOG2E;

DI unsigned pk2(float a, float b) { f32x2 v = {a, b}; return __builtin_bit_cast(unsigned, __builtin_convertvector(v, bf16x2_t)); }
DI float bflo(unsigned u) { return __uint_as_float(u << 16); }
DI float bfhi(unsigned u) { return __uint_as_float(u & 0xffff0000u); }
DI float rbf(float a) { return bflo(pk2(a, a)); }
DI bf16x8 pack8(const f32x16& v, int s) {
  u32x4 p = {pk2(v[8 * s], v[8 * s + 1]), pk2(v[8 * s + 2], v[8 * s + 3]), pk2(v[8 * s + 4], v[8 * s + 5]), pk2(v[8 * s + 6], v[8 * s + 7])};
  return __builtin_bit_cast(bf16x8, p);
}
DI void unpack8(u32x4 p, f32x16& v, int s) {
#pragma unroll
  for (int i = 0; i < 4; ++i) { v[8 * s + 2 * i] = bflo(p[i]); v[8 * s + 2 * i + 1] = bfhi(p[i]); }
}
DI float halfsum(float x) {
  auto r = __builtin_amdgcn_permlane32_swap(__float_as_uint(x), __float_as_uint(x), false, false);
  return __uint_as_float(r[0]) + __uint_as_float(r[1]);
}
DI float halfmax(float x) {
  auto r = __builtin_amdgcn_permlane32_swap(__float_as_uint(x), __float_as_uint(x), false, false);
  return fmaxf(__uint_as_float(r[0]), __uint_as_float(r[1]));
}
DI float sigmoidf_(float x) { return __builtin_amdgcn_rcpf(1.0f + __builtin_amdgcn_exp2f(-LOG2E * x)); }
DI float siluf_(float x) { return x * sigmoidf_(x); }
DI void lds_barrier() { asm volatile("s_waitcnt lgkmcnt(0)\n\ts_barrier" ::: "memory"); }
typedef short v4i16_t __attribute__((ext_vector_type(4)));
DI s16x4 trread(LAS const char* p) { return __builtin_bit_cast(s16x4, __builtin_amdgcn_ds_read_tr16_b64_v4i16((LAS v4i16_t*)p)); }
DI void stg16(char* p, bf16x8 v) { __builtin_nontemporal_store(v, (bf16x8*)p); }
DI bf16x8 ldsfrag(LAS const char* p) { return *(LAS const bf16x8*)p; }

constexpr size_t WS_WT1 = 24u << 20, WS_WT3 = 26u << 20;
constexpr int NP1 = 11, NP3 = 12;
struct WPtrs { const char *w1a, *w1b, *wqkv, *wo, *pw1, *pw2, *w2a, *w2b; };
DI void ff_panel(const char* wa, const char* wb, int p, int w2, const char*& base, unsigned& ld) {
  const bool isb = (p == 2 || p == 4 || p >= 6);
  const int c = p < 2 ? p : (p == 3 ? 2 : (p == 5 ? 3 : (p == 2 ? 0 : (p == 4 ? 1 : p - 4))));
  if (isb) { base = wb + (size_t)c * 256 * 512 + w2 * 128; ld = 512; } else { base = wa + (size_t)(w2 + 4 * c) * 128; ld = 2048; }
}
__global__ __launch_bounds__(256) void prep_kernel(WPtrs wp, char* __restrict__ ws) {
  __shared__ __attribute__((aligned(16))) char sm[4 * 4096];
  const int lane = threadIdx.x & 63, wv = __builtin_amdgcn_readfirstlane(threadIdx.x >> 6);
  const int g = blockIdx.x * 4 + wv;
  const bool k3 = g >= 4 * NP1 * 8;
  const int gg = k3 ? g - 4 * NP1 * 8 : g, np = k3 ? NP3 : NP1;
  const int w2 = gg / (np * 8), p = (gg >> 3) % np, sl = gg & 7;
  const char* base; unsigned ld;
  if (!k3) {
    if (p < 8) ff_panel(wp.w1a, wp.w1b, p, w2, base, ld);
    else { base = wp.wqkv + (p - 8) * 512 + w2 * 128; ld = 1536; }
  } else {
    if (p == 0) { base = wp.wo + w2 * 128; ld = 512; }
    else if (p < 3) { base = wp.pw1 + (p - 1) * 512 + w2 * 128; ld = 1024; }
    else if (p == 3) { base = wp.pw2 + w2 * 128; ld = 512; }
    else ff_panel(wp.w2a, wp.w2b, p - 4, w2, base, ld);
  }
  LAS char* my = (LAS char*)sm + wv * 4096;
  const char* src = base + (size_t)(sl * 32 + (lane >> 3)) * ld + (((lane & 7) ^ (((lane >> 4) & 1) << 2)) << 4);
  u32x4 t[4];
#pragma unroll
  for (int i = 0; i < 4; ++i) t[i] = *(const u32x4*)(src + (size_t)i * 8 * ld);
#pragma unroll
  for (int i = 0; i < 4; ++i) *(LAS u32x4*)(my + i * 1024 + lane * 16) = t[i];
  asm volatile("s_waitcnt lgkmcnt(0)" ::: "memory");
  const int h = lane >> 5, cb = (lane >> 4) & 1, q = (lane & 15) >> 2, pp = lane & 3;
  char* dst = ws + (k3 ? WS_WT3 : WS_WT1) + (size_t)((2 * w2) * np + p) * 16384 + sl * 2048 + lane * 16;
#pragma unroll
  for (int i = 0; i < 4; ++i) {
    LAS const char* rp = my + (4 * h + q) * 128 + (((i & 1) ^ (q >> 1)) * 64) + cb * 32 + pp * 8 + (i >> 1) * 2048;
    s16x4 l = trread(rp), hh = trread(rp + 1024);
    *(bf16x8*)(dst + (size_t)(i & 1) * np * 16384 + (i >> 1) * 1024) = __builtin_shufflevector(l, hh, 0, 1, 2, 3, 4, 5, 6, 7);
  }
}

struct Stream {
  const char* p;
  bf16x8 wf[2][16];
  DI void start(const char* base) {
    p = base;
#pragma unroll
    for (int i = 0; i < 16; ++i) wf[0][i] = *(const bf16x8*)(p + i * 1024);
  }
};
struct NoWork { DI void init() {} DI void step(int) {} };
struct SiluWork {
  LAS char* p;
  u32x2 raw; float x[4], e[4];
  DI void init() { raw = *(LAS u32x2*)p; }
  DI void step(int ks) {
    const int g = ks >> 2, i = ks & 3;
    if (i == 0) {
      x[0] = bflo(raw[0]); x[1] = bfhi(raw[0]); x[2] = bflo(raw[1]); x[3] = bfhi(raw[1]);
#pragma unroll
      for (int j = 0; j < 4; ++j) e[j] = -LOG2E * x[j];
      asm volatile("" : "+v"(e[0]), "+v"(e[1]), "+v"(e[2]), "+v"(e[3]), "+v"(x[0]), "+v"(x[1]), "+v"(x[2]), "+v"(x[3]));
      if (g < 3) raw = *(LAS u32x2*)(p + ((g + 1) >> 1) * 1024 + ((g + 1) & 1) * 8);
    } else if (i == 1) {
#pragma unroll
      for (int j = 0; j < 4; ++j) e[j] = __builtin_amdgcn_exp2f(e[j]);
      asm volatile("" : "+v"(e[0]), "+v"(e[1]), "+v"(e[2]), "+v"(e[3]));
    } else if (i == 2) {
#pragma unroll
      for (int j = 0; j < 4; ++j) e[j] = __builtin_amdgcn_rcpf(1.0f + e[j]);
      asm volatile("" : "+v"(e[0]), "+v"(e[1]), "+v"(e[2]), "+v"(e[3]));
    } else {
      *(LAS u32x2*)(p + (g >> 1) * 1024 + (g & 1) * 8) = (u32x2){pk2(x[0] * e[0], x[1] * e[1]), pk2(x[2] * e[2], x[3] * e[3])};
    }
  }
};
template <int NB, bool SWAP, int P, int NP, class W>
DI void gemm_k256(Stream& st, f32x16 (&acc)[NB], LAS const char* bl, W work) {
  constexpr bool REFILL = P + 2 < NP;
  bf16x8 bq[3][NB];
#pragma unroll
  for (int nb = 0; nb < NB; ++nb) { bq[0][nb] = ldsfrag(bl + nb * 16384); bq[1][nb] = ldsfrag(bl + nb * 16384 + 1024); }
  work.init();
#pragma unroll
  for (int ks = 0; ks < 16; ++ks) {
    SB();
#pragma unroll
    for (int nb = 0; nb < NB; ++nb)
      acc[nb] = SWAP ? MFMA(bq[ks % 3][nb], st.wf[P & 1][ks], acc[nb]) : MFMA(st.wf[P & 1][ks], bq[ks % 3][nb], acc[nb]);
    if (ks < 14) {
#pragma unroll
      for (int nb = 0; nb < NB; ++nb) bq[(ks + 2) % 3][nb] = ldsfrag(bl + nb * 16384 + (ks + 2) * 1024);
    }
    if (P == 0) st.wf[1][ks] = *(const bf16x8*)(st.p + 16384 + ks * 1024);
    if (REFILL && ks > 0) st.wf[P & 1][ks - 1] = *(const bf16x8*)(st.p + (P + 2) * 16384 + (ks - 1) * 1024);
    work.step(ks);
  }
#pragma unroll
  for (int nb = 0; nb < NB; ++nb) asm volatile("" : "+v"(acc[nb]));
  SB();
  if (REFILL) st.wf[P & 1][15] = *(const bf16x8*)(st.p + (P + 2) * 16384 + 15 * 1024);
}

constexpr int L_X = 0, L_Y = L_X + 16384, L_Z = L_Y + 16384;
constexpr int L_STAT = L_Z + 16384 + 9216;
constexpr int GROW = 144, GSZ = 36 * GROW;
constexpr int L_DW = L_STAT + 4096;
constexpr int L_TOTAL = L_DW + 5120;

DI float sum16(const f32x16& t) {
  return (((t[0] + t[1]) + (t[2] + t[3])) + ((t[4] + t[5]) + (t[6] + t[7]))) + (((t[8] + t[9]) + (t[10] + t[11])) + ((t[12] + t[13]) + (t[14] + t[15])));
}
template <int NB>
DI void ln_stats(const f32x16 (&v)[NB], LAS char* lds, int w, int lane, float (&mean)[NB], float (&rstd)[NB]) {
  const int r = lane & 31;
#pragma unroll
  for (int nb = 0; nb < NB; ++nb) {
    const float mw = halfsum(sum16(v[nb])) * (1.0f / 32.0f);
    const f32x16 d = v[nb] - mw;
    const float m2 = halfsum(sum16(d * d));
    if (lane < 32) *(LAS f32x2*)(lds + L_STAT + ((nb * 32 + r) * 8 + w) * 8) = (f32x2){mw, m2};
  }
  lds_barrier();
#pragma unroll
  for (int nb = 0; nb < NB; ++nb) {
    f32x4 s[4];
#pragma unroll
    for (int i = 0; i < 4; ++i) s[i] = *(LAS f32x4*)(lds + L_STAT + (nb * 32 + r) * 64 + i * 16);
    const float m = 0.125f * (((s[0][0] + s[0][2]) + (s[1][0] + s[1][2])) + ((s[2][0] + s[2][2]) + (s[3][0] + s[3][2])));
    float M2 = 0.f;
#pragma unroll
    for (int i = 0; i < 4; ++i) { const float e0 = s[i][0] - m, e1 = s[i][2] - m; M2 += s[i][1] + s[i][3] + 32.0f * (e0 * e0 + e1 * e1); }
    mean[nb] = m; rstd[nb] = __builtin_amdgcn_rsqf(M2 * (1.0f / 256.0f) + LN_EPS);
  }
}
template <int NB>
DI void ln_to_frags(const f32x16 (&v)[NB], LAS char* lds, int w, int lane) {
  float mean[NB], rstd[NB];
  ln_stats<NB>(v, lds, w, lane, mean, rstd);
  LAS char* fb = lds + L_X + w * 2048 + lane * 16;
#pragma unroll
  for (int nb = 0; nb < NB; ++nb) {
    const f32x16 n = (v[nb] - mean[nb]) * rstd[nb];
#pragma unroll
    for (int s = 0; s < 2; ++s) *(LAS bf16x8*)(fb + nb * 16384 + s * 1024) = pack8(n, s);
  }
  lds_barrier();
}

template <int P0, int NP>
DI void ff_block(Stream& st, LAS char* lds, int w, int lane, f32x16 (&out)[1]) {
  out[0] = f32x16{};
  LAS char* xb = lds + L_X + lane * 16;
  LAS char* yb = lds + L_Y + lane * 16;
  LAS char* zb = lds + L_Z + lane * 16;
  auto put = [&](LAS char* dst, const f32x16& hv) { *(LAS bf16x8*)(dst + w * 2048) = pack8(hv, 0); *(LAS bf16x8*)(dst + w * 2048 + 1024) = pack8(hv, 1); };
  f32x16 hn[1];
  hn[0] = f32x16{}; gemm_k256<1, false, P0 + 0, NP>(st, hn, xb, NoWork{});
  put(yb, hn[0]);
  hn[0] = f32x16{}; gemm_k256<1, false, P0 + 1, NP>(st, hn, xb, SiluWork{yb + w * 2048});
  lds_barrier(); put(zb, hn[0]);
  gemm_k256<1, false, P0 + 2, NP>(st, out, yb, NoWork{});
  hn[0] = f32x16{}; gemm_k256<1, false, P0 + 3, NP>(st, hn, xb, SiluWork{zb + w * 2048});
  lds_barrier(); put(yb, hn[0]);
  gemm_k256<1, false, P0 + 4, NP>(st, out, zb, NoWork{});
  hn[0] = f32x16{}; gemm_k256<1, false, P0 + 5, NP>(st, hn, xb, SiluWork{yb + w * 2048});
  lds_barrier(); put(zb, hn[0]);
  gemm_k256<1, false, P0 + 6, NP>(st, out, yb, SiluWork{zb + w * 2048});
  lds_barrier();
  gemm_k256<1, false, P0 + 7, NP>(st, out, zb, NoWork{});
}

DI void l2_warm(const char* p, int kb_total, int idx, int lane, u32x4& t) {
  const int per = (kb_total + 247) / 248;
  int b = idx * per, e = b + per; if (e > kb_total) e = kb_total;
  for (int i = b; i < e; ++i)
    asm volatile("global_load_dwordx4 %0, %1, %2" : "+v"(t) : "v"(lane * 16 + i * 1024), "s"(p));
}

__global__ __launch_bounds__(512, 2) void k1_kernel(const char* __restrict__ x, char* __restrict__ ws) {
  extern __shared__ __attribute__((aligned(16))) char smem[];
  LAS char* lds = (LAS char*)smem;
  const int lane = threadIdx.x & 63, w = __builtin_amdgcn_readfirstlane(threadIdx.x >> 6), r = lane & 31, h = lane >> 5;
  const int tb = blockIdx.x;
  if (tb == 0 && threadIdx.x < 256) *(unsigned*)(ws + WS_FLAG + threadIdx.x * 64) = 0u;
  STAMP(0, 0);
  f32x16 xv[1];
  Stream st;
  u32x4 wt = {0u, 0u, 0u, 0u};
  {
    const char* xrow = x + (size_t)(tb * 32 + r) * 512 + (w * 32 + 4 * h) * 2;
    u32x2 u[4];
#pragma unroll
    for (int g = 0; g < 4; ++g) u[g] = *(const u32x2*)(xrow + g * 16);
    asm volatile("s_barrier" ::: "memory");
    SB();
    st.start(ws + WS_WT1 + (size_t)w * NP1 * 16384 + lane * 16);
    SB();
#pragma unroll
    for (int g = 0; g < 4; ++g) {
      xv[0][4 * g] = bflo(u[g][0]); xv[0][4 * g + 1] = bfhi(u[g][0]); xv[0][4 * g + 2] = bflo(u[g][1]); xv[0][4 * g + 3] = bfhi(u[g][1]);
    }
  }
  STAMP(0, 1);
  ln_to_frags<1>(xv, lds, w, lane);
  STAMP(0, 2);
  f32x16 acc[1];
  ff_block<0, NP1>(st, lds, w, lane, acc);
  asm volatile("" :: "v"(wt));
  STAMP(0, 3);
#pragma unroll
  for (int i = 0; i < 16; ++i) xv[0][i] = rbf(xv[0][i] + 0.5f * acc[0][i]);
#pragma unroll
  for (int s = 0; s < 2; ++s) stg16((char*)(ws + WS_X1F + ((size_t)tb * 16 + 2 * w + s) * 1024 + lane * 16), pack8(xv[0], s));
  ln_to_frags<1>(xv, lds, w, lane);
  STAMP(0, 4);
  const int b = tb / NBLK, blk = tb - b * NBLK, bh = b * 4 + (w >> 1);
  char* kvdst = ws + WS_KVF + (size_t)(bh * NBLK + blk) * 8192 + (w & 1) * 2048 + lane * 16;
  {
    f32x16 a[1]; a[0] = f32x16{};
    gemm_k256<1, false, 8, NP1>(st, a, lds + L_X + lane * 16, NoWork{});
    a[0] = a[0] * QSCALE;
    char* dst = ws + WS_QF + (size_t)(bh * NBLK + blk) * 4096 + (w & 1) * 2048 + lane * 16;
    stg16((char*)(dst), pack8(a[0], 0)); stg16((char*)(dst + 1024), pack8(a[0], 1));
  }
  {
    f32x16 a[1]; a[0] = f32x16{};
    gemm_k256<1, false, 9, NP1>(st, a, lds + L_X + lane * 16, NoWork{});
    stg16((char*)(kvdst), pack8(a[0], 0)); stg16((char*)(kvdst + 1024), pack8(a[0], 1));
  }
  STAMP(0, 5);
  {
    f32x16 a[1]; a[0] = f32x16{};
    gemm_k256<1, true, 10, NP1>(st, a, lds + L_X + lane * 16, NoWork{});
    stg16((char*)(kvdst + 4096), pack8(a[0], 0)); stg16((char*)(kvdst + 5120), pack8(a[0], 1));
  }
  STAMP(0, 29);
}

constexpr int A_KG = 2, A_SBLK = A_KG * 2, A_HST = A_SBLK * 4096, A_NST = (NBLK + A_SBLK - 1) / A_SBLK, A_NFULL = NBLK / A_SBLK;
constexpr int A_VOFF = 3 * A_HST, A_LDS = 6 * A_HST;
DI void attn_body(char* __restrict__ ws, LAS char* lds) {
  const int lane = threadIdx.x & 63, w = __builtin_amdgcn_readfirstlane(threadIdx.x >> 6);
  const int bh = blockIdx.x & 7, qg = blockIdx.x >> 3, qi = w & 3, kg = w >> 2;
  const int qb_raw = qg * 4 + qi, qb = qb_raw < NBLK ? qb_raw : NBLK - 1;
  STAMP(1, 0);
  const char* kv = ws + WS_KVF + (size_t)bh * NBLK * 8192;
  const unsigned lds0 = (unsigned)(uintptr_t)lds;
  const unsigned voff = (w >> 1) * 8192 + (w & 1) * 2048 + lane * 16;
  auto dma2 = [&](int st, int isv, unsigned ldsoff) {
    asm volatile("s_mov_b32 m0, %2\n\ts_nop 0\n\tglobal_load_lds_dwordx4 %0, %1\n\tglobal_load_lds_dwordx4 %0, %1 offset:1024"
                 :: "v"(voff), "s"(kv + (size_t)st * (2 * A_HST) + isv * 4096), "s"(lds0 + ldsoff + w * 2048));
  };
  dma2(0, 0, 0); dma2(1, 0, A_HST); dma2(0, 1, A_VOFF); dma2(2, 0, 2 * A_HST); dma2(1, 1, A_VOFF + A_HST);
  bf16x8 qf[4];
#pragma unroll
  for (int f = 0; f < 4; ++f) qf[f] = *(const bf16x8*)(ws + WS_QF + ((size_t)(bh * NBLK + qb) * 4 + f) * 1024 + lane * 16);
#pragma unroll
  for (int f = 0; f < 4; ++f) asm volatile("" : "+v"(qf[f]));
  u32x4 wt = {0u, 0u, 0u, 0u};
  const int rem = NBLK - A_NFULL * A_SBLK - kg * 2;
  const int ntail = rem < 0 ? 0 : (rem > 2 ? 2 : rem);
  LAS const char* base = lds + kg * 8192 + lane * 16;
  f32x16 o0 = f32x16{}, o1 = f32x16{}, negr, sa, sb;
  f32x4 lacc = {0.f, 0.f, 0.f, 0.f};
  bf16x8 sel;
  { const short one = (((lane >> 4) ^ lane) & 1) ? (short)0 : (short)0x3f80;
#pragma unroll
    for (int j = 0; j < 8; ++j) sel[j] = one; }
  bf16x8 kf[4], vf[4], kg2[4], vg2[4]; unsigned pa[8], pb[8];
#pragma unroll
  for (int i = 0; i < 8; ++i) pa[i] = 0u;
  float ref;
  asm volatile("s_waitcnt vmcnt(0)\n\ts_barrier" : "+v"(wt) :: "memory");
  {
#pragma unroll
    for (int f = 0; f < 4; ++f) kf[f] = ldsfrag(base + f * 1024);
    f32x16 z = f32x16{};
    sa = MFMA(kf[0], qf[0], z); sa = MFMA(kf[1], qf[1], sa); sa = MFMA(kf[2], qf[2], sa); sa = MFMA(kf[3], qf[3], sa);
    float mx = sa[0];
#pragma unroll
    for (int i = 1; i < 16; ++i) mx = fmaxf(mx, sa[i]);
    ref = halfmax(mx) + 7.0f;
#pragma unroll
    for (int i = 0; i < 16; ++i) { sa[i] -= ref; negr[i] = -ref; }
#pragma unroll
    for (int f = 0; f < 4; ++f) { kf[f] = ldsfrag(base + 4096 + f * 1024); vf[f] = bf16x8{}; }
  }
#define SLOT3(MF, RF, i) do { MF; RF; e[2*(i)] = __builtin_amdgcn_exp2f(SC_[2*(i)]); e[2*(i)+1] = __builtin_amdgcn_exp2f(SC_[2*(i)+1]); \
    if ((i) > 0) PN_[(i)-1] = pk2(e[2*(i)-2], e[2*(i)-1]); SB(); } while (0)
  auto iter = [&](f32x16& SC_, f32x16& SN_, unsigned (&PP_)[8], unsigned (&PN_)[8], bf16x8 (&KC)[4], bf16x8 (&VC)[4], bf16x8 (&KN)[4], bf16x8 (&VN)[4], LAS const char* kn, LAS const char* vc) {
    float e[16];
    const bf16x8 p0 = __builtin_bit_cast(bf16x8, (u32x4){PP_[0], PP_[1], PP_[2], PP_[3]}), p1 = __builtin_bit_cast(bf16x8, (u32x4){PP_[4], PP_[5], PP_[6], PP_[7]});
    SB();
    SLOT3(SN_ = MFMA(KC[0], qf[0], negr), KN[0] = ldsfrag(kn), 0);
    SLOT3(o0 = MFMA(VC[0], p0, o0), VN[0] = ldsfrag(vc), 1);
    SLOT3(SN_ = MFMA(KC[1], qf[1], SN_), KN[1] = ldsfrag(kn + 1024), 2);
    SLOT3(o1 = MFMA(VC[2], p0, o1); lacc = MFMA16(sel, p0, lacc), VN[2] = ldsfrag(vc + 2048), 3);
    SLOT3(SN_ = MFMA(KC[2], qf[2], SN_), KN[2] = ldsfrag(kn + 2048), 4);
    SLOT3(o0 = MFMA(VC[1], p1, o0), VN[1] = ldsfrag(vc + 1024), 5);
    SLOT3(SN_ = MFMA(KC[3], qf[3], SN_), KN[3] = ldsfrag(kn + 3072), 6);
    SLOT3(o1 = MFMA(VC[3], p1, o1); lacc = MFMA16(sel, p1, lacc), VN[3] = ldsfrag(vc + 3072), 7);
    PN_[7] = pk2(e[14], e[15]);
    const unsigned orall = (PN_[0] | PN_[1] | PN_[2]) | (PN_[3] | PN_[4] | PN_[5]) | (PN_[6] | PN_[7]);
    if (__builtin_expect(__any((orall & 0x40004000u) != 0u), 0)) {
      float mx = SC_[0];
#pragma unroll
      for (int i = 1; i < 16; ++i) mx = fmaxf(mx, SC_[i]);
      mx = halfmax(mx);
      const float delta = (mx >= 0.5f) ? mx + 7.0f : 0.0f;
      const float alpha = __builtin_amdgcn_exp2f(-delta);
      const float alo = __shfl_xor(alpha, 16);
      const float aA = (lane & 16) ? alo : alpha, aB = (lane & 16) ? alpha : alo;
      lacc = lacc * (f32x4){aA, aB, aA, aB};
      o0 = o0 * alpha; o1 = o1 * alpha; negr = negr - delta; SN_ = SN_ - delta;
      asm volatile("" : "+v"(o0), "+v"(o1), "+v"(negr), "+v"(SN_), "+v"(lacc));
      ref = -negr[0];
#pragma unroll
      for (int i = 0; i < 8; ++i) PN_[i] = pk2(__builtin_amdgcn_exp2f(SC_[2*i] - delta), __builtin_amdgcn_exp2f(SC_[2*i+1] - delta));
    }
  };
  STAMP(1, 1);
  if (w >= 4) __builtin_amdgcn_s_setprio(1);
  int r0 = 0, r1 = A_HST, r2 = 2 * A_HST;
  for (int st = 0; st < A_NFULL; ++st) {
    asm volatile("s_waitcnt vmcnt(4) lgkmcnt(0)\n\ts_barrier" ::: "memory");
    iter(sa, sb, pa, pb, kf, vf, kg2, vg2, base + r1, base + A_VOFF + r0);
    dma2(st + 3 < A_NST ? st + 3 : A_NST - 1, 0, r0); dma2(st + 2 < A_NST ? st + 2 : A_NST - 1, 1, A_VOFF + r2);
    iter(sb, sa, pb, pa, kg2, vg2, kf, vf, base + r1 + 4096, base + A_VOFF + r0 + 4096);
    const int t = r0; r0 = r1; r1 = r2; r2 = t;
  }
  asm volatile("s_waitcnt vmcnt(0) lgkmcnt(0)\n\ts_barrier" ::: "memory");
  if (ntail > 0) {
    iter(sa, sb, pa, pb, kf, vf, kg2, vg2, base + r1, base + A_VOFF + r0);
    if (ntail > 1) iter(sb, sa, pb, pa, kg2, vg2, kf, vf, base + r1 + 4096, base + A_VOFF + r0 + 4096);
    else {
#pragma unroll
      for (int i = 0; i < 8; ++i) pa[i] = pb[i];
#pragma unroll
      for (int i = 0; i < 4; ++i) vf[i] = vg2[i];
    }
  }
  {
    const bf16x8 p0 = __builtin_bit_cast(bf16x8, (u32x4){pa[0], pa[1], pa[2], pa[3]}), p1 = __builtin_bit_cast(bf16x8, (u32x4){pa[4], pa[5], pa[6], pa[7]});
    o0 = MFMA(vf[0], p0, o0); o1 = MFMA(vf[2], p0, o1); o0 = MFMA(vf[1], p1, o0); o1 = MFMA(vf[3], p1, o1);
    lacc = MFMA16(sel, p0, lacc); lacc = MFMA16(sel, p1, lacc);
  }
  float lsum = (lane & 16) ? lacc[1] : lacc[0];
  __builtin_amdgcn_s_setprio(0);
  STAMP(1, 2);
  lds_barrier();
  if (kg > 0) {
    LAS char* cb = lds + ((kg - 1) * 4 + qi) * 9216 + lane * 16;
#pragma unroll
    for (int g = 0; g < 4; ++g) {
      *(LAS f32x4*)(cb + g * 1024) = (f32x4){o0[4 * g], o0[4 * g + 1], o0[4 * g + 2], o0[4 * g + 3]};
      *(LAS f32x4*)(cb + 4096 + g * 1024) = (f32x4){o1[4 * g], o1[4 * g + 1], o1[4 * g + 2], o1[4 * g + 3]};
    }
    *(LAS f32x2*)(lds + ((kg - 1) * 4 + qi) * 9216 + 8192 + lane * 8) = (f32x2){ref, lsum};
  }
  lds_barrier();
  if (kg == 0 && qb_raw < NBLK) {
#pragma unroll
    for (int k = 1; k < A_KG; ++k) {
      LAS char* cb = lds + ((k - 1) * 4 + qi) * 9216 + lane * 16;
      const f32x2 rl = *(LAS f32x2*)(lds + ((k - 1) * 4 + qi) * 9216 + 8192 + lane * 8);
      const float m = fmaxf(ref, rl[0]);
      const float c0 = __builtin_amdgcn_exp2f(ref - m), c1 = __builtin_amdgcn_exp2f(rl[0] - m);
      lsum = lsum * c0 + rl[1] * c1; ref = m;
#pragma unroll
      for (int g = 0; g < 4; ++g) {
        const f32x4 u0 = *(LAS f32x4*)(cb + g * 1024), u1 = *(LAS f32x4*)(cb + 4096 + g * 1024);
#pragma unroll
        for (int j = 0; j < 4; ++j) { o0[4 * g + j] = o0[4 * g + j] * c0 + u0[j] * c1; o1[4 * g + j] = o1[4 * g + j] * c0 + u1[j] * c1; }
      }
    }
    const float inv = 1.0f / lsum;
#pragma unroll
    for (int i = 0; i < 16; ++i) { o0[i] *= inv; o1[i] *= inv; }
    char* dst = ws + WS_ATTF + ((size_t)((bh >> 2) * NBLK + qb) * 16 + (bh & 3) * 4) * 1024 + lane * 16;
    const bf16x8 f0 = pack8(o0, 0), f1 = pack8(o0, 1), f2 = pack8(o1, 0), f3 = pack8(o1, 1);
    asm volatile("global_store_dwordx4 %0, %1, off sc1\n\tglobal_store_dwordx4 %0, %2, off offset:1024 sc1\n\t"
                 "global_store_dwordx4 %0, %3, off offset:2048 sc1\n\tglobal_store_dwordx4 %0, %4, off offset:3072 sc1\n\ts_nop 1"
                 :: "v"(dst), "v"(f0), "v"(f1), "v"(f2), "v"(f3) : "memory");
  }
  asm volatile("s_waitcnt vmcnt(0) lgkmcnt(0)\n\ts_barrier" ::: "memory");
  if (threadIdx.x == 0) __hip_atomic_store((unsigned*)(ws + WS_FLAG + blockIdx.x * 64), 1u, __ATOMIC_RELAXED, __HIP_MEMORY_SCOPE_AGENT);
  STAMP(1, 29);
}

__global__ __launch_bounds__(512, 2) void k23_kernel(const char* __restrict__ dw, char* __restrict__ ws, char* __restrict__ out) {
  extern __shared__ __attribute__((aligned(16))) char smem[];
  LAS char* lds = (LAS char*)smem;
  attn_body(ws, lds);
  if (blockIdx.x >= NTB) return;
  const int lane = threadIdx.x & 63, w = __builtin_amdgcn_readfirstlane(threadIdx.x >> 6), r = lane & 31, h = lane >> 5;
  const int tb = blockIdx.x, b = tb / NBLK, t0 = (tb - b * NBLK) * 32;
  STAMP(2, 0);
  Stream st;
  const int hc = r & 3, ht = (hc < 2) ? t0 - 2 + hc : t0 + 30 + hc;
  const bool hvalid = ht >= 0 && ht < TSEQ;
  const int hg = b * TSEQ + (ht < 0 ? 0 : (ht >= TSEQ ? TSEQ - 1 : ht));
  const unsigned hoff = (unsigned)(hg >> 5) * 16384 + ((hg & 31) + 32 * h) * 16 + w * 2048;
  const unsigned moff = (unsigned)tb * 16384 + lane * 16 + w * 2048;
  u32x4 xr[2][2];
#pragma unroll
  for (int f = 0; f < 2; ++f) {
    xr[0][f] = *(const u32x4*)(ws + WS_X1F + moff + f * 1024);
    xr[1][f] = *(const u32x4*)(ws + WS_X1F + hoff + f * 1024);
  }
  const u32x4 dwv = *(const u32x4*)(dw + (threadIdx.x < 160 ? threadIdx.x : 159) * 16);
  SB();
  st.start(ws + WS_WT3 + (size_t)w * NP3 * 16384 + lane * 16);
  SB();
  if (threadIdx.x < 160) {
    *(LAS f32x4*)(lds + L_DW + threadIdx.x * 32) = (f32x4){bflo(dwv[0]), bfhi(dwv[0]), bflo(dwv[1]), bfhi(dwv[1])};
    *(LAS f32x4*)(lds + L_DW + threadIdx.x * 32 + 16) = (f32x4){bflo(dwv[2]), bfhi(dwv[2]), bflo(dwv[3]), bfhi(dwv[3])};
  }
  {
    const int blk = tb - b * NBLK;
    int q = blk + (lane & 3) - 1; q = q < 0 ? 0 : (q >= NBLK ? NBLK - 1 : q);
    const unsigned* fp = (const unsigned*)(ws + WS_FLAG + (size_t)((q >> 2) * 8 + b * 4 + (w >> 1)) * 64);
    for (int spin = 0; spin < (1 << 16); ++spin) {
      const unsigned v = (lane < 3) ? __hip_atomic_load(fp, __ATOMIC_RELAXED, __HIP_MEMORY_SCOPE_AGENT) : 1u;
      if (__all(v == 1u)) break;
      __builtin_amdgcn_s_sleep(1);
    }
    __builtin_amdgcn_fence(__ATOMIC_ACQUIRE, "wavefront");
  }
  {
    const unsigned ly = (unsigned)(uintptr_t)(lds + L_Y + w * 2048);
    asm volatile("s_mov_b32 m0, %3\n\ts_nop 0\n\tglobal_load_lds_dwordx4 %0, %2 sc1\n\tglobal_load_lds_dwordx4 %0, %2 offset:1024 sc1\n\t"
                 "s_mov_b32 m0, %4\n\ts_nop 0\n\tglobal_load_lds_dwordx4 %1, %2 sc1\n\tglobal_load_lds_dwordx4 %1, %2 offset:1024 sc1\n\t"
                 "s_waitcnt vmcnt(0)"
                 :: "v"(moff), "v"(hoff), "s"(ws + WS_ATTF), "s"(ly), "s"(ly + 16384) : "memory");
  }
  lds_barrier();
  STAMP(2, 1);
  f32x16 xv[2];
#pragma unroll
  for (int nb = 0; nb < 2; ++nb) { unpack8(xr[nb][0], xv[nb], 0); unpack8(xr[nb][1], xv[nb], 1); }
  gemm_k256<2, false, 0, NP3>(st, xv, lds + L_Y + lane * 16, NoWork{});
  STAMP(2, 2);
  ln_to_frags<2>(xv, lds, w, lane);
  STAMP(2, 3);
  {
    f32x16 a[2], g[2];
#pragma unroll
    for (int nb = 0; nb < 2; ++nb) { a[nb] = f32x16{}; g[nb] = f32x16{}; }
    gemm_k256<2, false, 1, NP3>(st, a, lds + L_X + lane * 16, NoWork{});
    gemm_k256<2, false, 2, NP3>(st, g, lds + L_X + lane * 16, NoWork{});
    STAMP(2, 4);
    LAS char* G = lds + L_Y + w * GSZ;
    lds_barrier();
#pragma unroll
    for (int nb = 0; nb < 2; ++nb) {
      const int row = nb == 0 ? r + 2 : (hc < 2 ? hc : 32 + hc);
      const bool wr = nb == 0 || r < 4;
      const float keep = (nb == 0 || hvalid) ? 1.0f : 0.0f;
#pragma unroll
      for (int q = 0; q < 4; ++q) {
        f32x4 e;
#pragma unroll
        for (int j = 0; j < 4; ++j) e[j] = keep * a[nb][4 * q + j] * sigmoidf_(g[nb][4 * q + j]);
        if (wr) *(LAS f32x4*)(G + row * GROW + (8 * q + 4 * h) * 4) = e;
      }
    }
    asm volatile("s_waitcnt lgkmcnt(0)" ::: "memory");
    f32x16 c;
#pragma unroll
    for (int q = 0; q < 4; ++q) {
      f32x4 e = {0.f, 0.f, 0.f, 0.f};
#pragma unroll
      for (int i = 0; i < 5; ++i)
        e += *(LAS f32x4*)(G + (r + i) * GROW + (8 * q + 4 * h) * 4) * *(LAS f32x4*)(lds + L_DW + i * 1024 + (w * 32 + 8 * q + 4 * h) * 4);
#pragma unroll
      for (int j = 0; j < 4; ++j) c[4 * q + j] = siluf_(e[j]);
    }
#pragma unroll
    for (int s = 0; s < 2; ++s) *(LAS bf16x8*)(lds + L_X + (2 * w + s) * 1024 + lane * 16) = pack8(c, s);
    lds_barrier();
  }
  STAMP(2, 5);
  f32x16 x3[1];
  x3[0] = xv[0];
  gemm_k256<1, false, 3, NP3>(st, x3, lds + L_X + lane * 16, NoWork{});
  STAMP(2, 6);
  ln_to_frags<1>(x3, lds, w, lane);
  STAMP(2, 7);
  f32x16 acc[1];
  ff_block<4, NP3>(st, lds, w, lane, acc);
  x3[0] += 0.5f * acc[0];
  STAMP(2, 8);
  float mean[1], rstd[1];
  ln_stats<1>(x3, lds, w, lane, mean, rstd);
  char* orow = out + (size_t)(tb * 32 + r) * 512 + (w * 32 + 4 * h) * 2;
  const f32x16 n = (x3[0] - mean[0]) * rstd[0];
#pragma unroll
  for (int q = 0; q < 4; ++q) *(u32x2*)(orow + q * 16) = (u32x2){pk2(n[4 * q], n[4 * q + 1]), pk2(n[4 * q + 2], n[4 * q + 3])};
  STAMP(2, 29);
}

extern "C" void kernel_launch(void* const* d_in, const int* in_sizes, int n_in, void* d_out, int out_size, void* d_ws, size_t ws_size,
                              hipStream_t stream) {
  const char* x = (const char*)d_in[0];
  const char* w1a = (const char*)d_in[1];
  const char* w1b = (const char*)d_in[2];
  const char* wqkv = (const char*)d_in[3];
  const char* wo = (const char*)d_in[4];
  const char* pw1 = (const char*)d_in[5];
  const char* dw = (const char*)d_in[6];
  const char* pw2 = (const char*)d_in[7];
  const char* w2a = (const char*)d_in[8];
  const char* w2b = (const char*)d_in[9];
  char* ws = (char*)d_ws;
  (void)hipFuncSetAttribute((const void*)k1_kernel, hipFuncAttributeMaxDynamicSharedMemorySize, L_TOTAL);
  (void)hipFuncSetAttribute((const void*)k23_kernel, hipFuncAttributeMaxDynamicSharedMemorySize, A_LDS);
  WPtrs wp{w1a, w1b, wqkv, wo, pw1, pw2, w2a, w2b};
  prep_kernel<<<(NP1 + NP3) * 8, 256, 0, stream>>>(wp, ws);
  k1_kernel<<<NTB, 512, L_TOTAL, stream>>>(x, ws);
  k23_kernel<<<256, 512, A_LDS, stream>>>(dw, ws, (char*)d_out);
}
```

```cpp
#include <hip/hip_runtime.h>
#include <hip/hip_bf16.h>
#include <stdint.h>

typedef short bf16x8 __attribute__((ext_vector_type(8)));
typedef short s16x4 __attribute__((ext_vector_type(4)));
typedef float f32x16 __attribute__((ext_vector_type(16)));
typedef float f32x2 __attribute__((ext_vector_type(2)));
typedef __bf16 bf16x2_t __attribute__((ext_vector_type(2)));
typedef unsigned u32x4 __attribute__((ext_vector_type(4)));
typedef unsigned u32x2 __attribute__((ext_vector_type(2)));
#define LAS __attribute__((address_space(3)))
#define DI __device__ __forceinline__
#define MFMA(a, b, c) __builtin_amdgcn_mfma_f32_32x32x16_bf16((a), (b), (c), 0, 0, 0)
#define MFMA16(a, b, c) __builtin_amdgcn_mfma_f32_16x16x32_bf16((a), (b), (c), 0, 0, 0)
#define SB() __builtin_amdgcn_sched_barrier(0)
#define STAMP(k, i) do {} while (0)
typedef float f32x4 __attribute__((ext_vector_type(4)));

constexpr int TSEQ = 4000, NBLK = 125, NTB = 250;
constexpr size_t WS_X1F = 0;
constexpr size_t WS_QF = 4u << 20;
constexpr size_t WS_KVF = 8u << 20;
constexpr size_t WS_ATTF = 17u << 20;
constexpr size_t WS_FLAG = 22u << 20;
constexpr float LN_EPS = 1e-5f;
constexpr float LOG2E = 1.4426950408889634f;
constexpr float QSCALE = 0.125f * LOG2E;

DI unsigned pk2(float a, float b) { f32x2 v = {a, b}; return __builtin_bit_cast(unsigned, __builtin_convertvector(v, bf16x2_t)); }
DI float bflo(unsigned u) { return __uint_as_float(u << 16); }
DI float bfhi(unsigned u) { return __uint_as_float(u & 0xffff0000u); }
DI float rbf(float a) { return bflo(pk2(a, a)); }
DI bf16x8 pack8(const f32x16& v, int s) {
  u32x4 p = {pk2(v[8 * s], v[8 * s + 1]), pk2(v[8 * s + 2], v[8 * s + 3]), pk2(v[8 * s + 4], v[8 * s + 5]), pk2(v[8 * s + 6], v[8 * s + 7])};
  return __builtin_bit_cast(bf16x8, p);
}
DI void unpack8(u32x4 p, f32x16& v, int s) {
#pragma unroll
  for (int i = 0; i < 4; ++i) { v[8 * s + 2 * i] = bflo(p[i]); v[8 * s + 2 * i + 1] = bfhi(p[i]); }
}
DI float halfsum(float x) {
  auto r = __builtin_amdgcn_permlane32_swap(__float_as_uint(x), __float_as_uint(x), false, false);
  return __uint_as_float(r[0]) + __uint_as_float(r[1]);
}
DI float halfmax(float x) {
  auto r = __builtin_amdgcn_permlane32_swap(__float_as_uint(x), __float_as_uint(x), false, false);
  return fmaxf(__uint_as_float(r[0]), __uint_as_float(r[1]));
}
DI float sigmoidf_(float x) { return __builtin_amdgcn_rcpf(1.0f + __builtin_amdgcn_exp2f(-LOG2E * x)); }
DI float siluf_(float x) { return x * sigmoidf_(x); }
DI void lds_barrier() { asm volatile("s_waitcnt lgkmcnt(0)\n\ts_barrier" ::: "memory"); }
typedef short v4i16_t __attribute__((ext_vector_type(4)));
DI s16x4 trread(LAS const char* p) { return __builtin_bit_cast(s16x4, __builtin_amdgcn_ds_read_tr16_b64_v4i16((LAS v4i16_t*)p)); }
DI void stg16(char* p, bf16x8 v) { __builtin_nontemporal_store(v, (bf16x8*)p); }
DI bf16x8 ldsfrag(LAS const char* p) { return *(LAS const bf16x8*)p; }

constexpr size_t WS_WT1 = 24u << 20, WS_WT3 = 26u << 20;
constexpr int NP1 = 11, NP3 = 12;
struct WPtrs { const char *w1a, *w1b, *wqkv, *wo, *pw1, *pw2, *w2a, *w2b; };
DI void ff_panel(const char* wa, const char* wb, int p, int w2, const char*& base, unsigned& ld) {
  const bool isb = (p == 2 || p == 4 || p >= 6);
  const int c = p < 2 ? p : (p == 3 ? 2 : (p == 5 ? 3 : (p == 2 ? 0 : (p == 4 ? 1 : p - 4))));
  if (isb) { base = wb + (size_t)c * 256 * 512 + w2 * 128; ld = 512; } else { base = wa + (size_t)(w2 + 4 * c) * 128; ld = 2048; }
}
__global__ __launch_bounds__(256) void prep_kernel(WPtrs wp, char* __restrict__ ws) {
  __shared__ __attribute__((aligned(16))) char sm[4 * 4096];
  const int lane = threadIdx.x & 63, wv = __builtin_amdgcn_readfirstlane(threadIdx.x >> 6);
  const int g = blockIdx.x * 4 + wv;
  const bool k3 = g >= 4 * NP1 * 8;
  const int gg = k3 ? g - 4 * NP1 * 8 : g, np = k3 ? NP3 : NP1;
  const int w2 = gg / (np * 8), p = (gg >> 3) % np, sl = gg & 7;
  const char* base; unsigned ld;
  if (!k3) {
    if (p < 8) ff_panel(wp.w1a, wp.w1b, p, w2, base, ld);
    else { base = wp.wqkv + (p - 8) * 512 + w2 * 128; ld = 1536; }
  } else {
    if (p == 0) { base = wp.wo + w2 * 128; ld = 512; }
    else if (p < 3) { base = wp.pw1 + (p - 1) * 512 + w2 * 128; ld = 1024; }
    else if (p == 3) { base = wp.pw2 + w2 * 128; ld = 512; }
    else ff_panel(wp.w2a, wp.w2b, p - 4, w2, base, ld);
  }
  LAS char* my = (LAS char*)sm + wv * 4096;
  const char* src = base + (size_t)(sl * 32 + (lane >> 3)) * ld + (((lane & 7) ^ (((lane >> 4) & 1) << 2)) << 4);
  u32x4 t[4];
#pragma unroll
  for (int i = 0; i < 4; ++i) t[i] = *(const u32x4*)(src + (size_t)i * 8 * ld);
#pragma unroll
  for (int i = 0; i < 4; ++i) *(LAS u32x4*)(my + i * 1024 + lane * 16) = t[i];
  asm volatile("s_waitcnt lgkmcnt(0)" ::: "memory");
  const int h = lane >> 5, cb = (lane >> 4) & 1, q = (lane & 15) >> 2, pp = lane & 3;
  char* dst = ws + (k3 ? WS_WT3 : WS_WT1) + (size_t)((2 * w2) * np + p) * 16384 + sl * 2048 + lane * 16;
#pragma unroll
  for (int i = 0; i < 4; ++i) {
    LAS const char* rp = my + (4 * h + q) * 128 + (((i & 1) ^ (q >> 1)) * 64) + cb * 32 + pp * 8 + (i >> 1) * 2048;
    s16x4 l = trread(rp), hh = trread(rp + 1024);
    *(bf16x8*)(dst + (size_t)(i & 1) * np * 16384 + (i >> 1) * 1024) = __builtin_shufflevector(l, hh, 0, 1, 2, 3, 4, 5, 6, 7);
  }
}

struct Stream {
  const char* p;
  bf16x8 wf[2][16];
  DI void start(const char* base) {
    p = base;
#pragma unroll
    for (int i = 0; i < 16; ++i) wf[0][i] = *(const bf16x8*)(p + i * 1024);
  }
};
struct NoWork { DI void init() {} DI void step(int) {} };
struct SiluWork {
  LAS char* p;
  u32x2 raw; float x[4], e[4];
  DI void init() { raw = *(LAS u32x2*)p; }
  DI void step(int ks) {
    const int g = ks >> 2, i = ks & 3;
    if (i == 0) {
      x[0] = bflo(raw[0]); x[1] = bfhi(raw[0]); x[2] = bflo(raw[1]); x[3] = bfhi(raw[1]);
#pragma unroll
      for (int j = 0; j < 4; ++j) e[j] = -LOG2E * x[j];
      asm volatile("" : "+v"(e[0]), "+v"(e[1]), "+v"(e[2]), "+v"(e[3]), "+v"(x[0]), "+v"(x[1]), "+v"(x[2]), "+v"(x[3]));
      if (g < 3) raw = *(LAS u32x2*)(p + ((g + 1) >> 1) * 1024 + ((g + 1) & 1) * 8);
    } else if (i == 1) {
#pragma unroll
      for (int j = 0; j < 4; ++j) e[j] = __builtin_amdgcn_exp2f(e[j]);
      asm volatile("" : "+v"(e[0]), "+v"(e[1]), "+v"(e[2]), "+v"(e[3]));
    } else if (i == 2) {
#pragma unroll
      for (int j = 0; j < 4; ++j) e[j] = __builtin_amdgcn_rcpf(1.0f + e[j]);
      asm volatile("" : "+v"(e[0]), "+v"(e[1]), "+v"(e[2]), "+v"(e[3]));
    } else {
      *(LAS u32x2*)(p + (g >> 1) * 1024 + (g & 1) * 8) = (u32x2){pk2(x[0] * e[0], x[1] * e[1]), pk2(x[2] * e[2], x[3] * e[3])};
    }
  }
};
template <int NB, bool SWAP, int P, int NP, class W>
DI void gemm_k256(Stream& st, f32x16 (&acc)[NB], LAS const char* bl, W work) {
  constexpr bool REFILL = P + 2 < NP;
  bf16x8 bq[3][NB];
#pragma unroll
  for (int nb = 0; nb < NB; ++nb) { bq[0][nb] = ldsfrag(bl + nb * 16384); bq[1][nb] = ldsfrag(bl + nb * 16384 + 1024); }
  work.init();
#pragma unroll
  for (int ks = 0; ks < 16; ++ks) {
    SB();
#pragma unroll
    for (int nb = 0; nb < NB; ++nb)
      acc[nb] = SWAP ? MFMA(bq[ks % 3][nb], st.wf[P & 1][ks], acc[nb]) : MFMA(st.wf[P & 1][ks], bq[ks % 3][nb], acc[nb]);
    if (ks < 14) {
#pragma unroll
      for (int nb = 0; nb < NB; ++nb) bq[(ks + 2) % 3][nb] = ldsfrag(bl + nb * 16384 + (ks + 2) * 1024);
    }
    if (P == 0) st.wf[1][ks] = *(const bf16x8*)(st.p + 16384 + ks * 1024);
    if (REFILL && ks > 0) st.wf[P & 1][ks - 1] = *(const bf16x8*)(st.p + (P + 2) * 16384 + (ks - 1) * 1024);
    work.step(ks);
  }
#pragma unroll
  for (int nb = 0; nb < NB; ++nb) asm volatile("" : "+v"(acc[nb]));
  SB();
  if (REFILL) st.wf[P & 1][15] = *(const bf16x8*)(st.p + (P + 2) * 16384 + 15 * 1024);
}

constexpr int L_X = 0, L_Y = L_X + 16384, L_Z = L_Y + 16384;
constexpr int L_STAT = L_Z + 16384 + 9216;
constexpr int GROW = 144, GSZ = 36 * GROW;
constexpr int L_DW = L_STAT + 4096;
constexpr int L_TOTAL = L_DW + 5120;

DI float sum16(const f32x16& t) {
  return (((t[0] + t[1]) + (t[2] + t[3])) + ((t[4] + t[5]) + (t[6] + t[7]))) + (((t[8] + t[9]) + (t[10] + t[11])) + ((t[12] + t[13]) + (t[14] + t[15])));
}
template <int NB>
DI void ln_stats(const f32x16 (&v)[NB], LAS char* lds, int w, int lane, float (&mean)[NB], float (&rstd)[NB]) {
  const int r = lane & 31;
#pragma unroll
  for (int nb = 0; nb < NB; ++nb) {
    const float mw = halfsum(sum16(v[nb])) * (1.0f / 32.0f);
    const f32x16 d = v[nb] - mw;
    const float m2 = halfsum(sum16(d * d));
    if (lane < 32) *(LAS f32x2*)(lds + L_STAT + ((nb * 32 + r) * 8 + w) * 8) = (f32x2){mw, m2};
  }
  lds_barrier();
#pragma unroll
  for (int nb = 0; nb < NB; ++nb) {
    f32x4 s[4];
#pragma unroll
    for (int i = 0; i < 4; ++i) s[i] = *(LAS f32x4*)(lds + L_STAT + (nb * 32 + r) * 64 + i * 16);
    const float m = 0.125f * (((s[0][0] + s[0][2]) + (s[1][0] + s[1][2])) + ((s[2][0] + s[2][2]) + (s[3][0] + s[3][2])));
    float M2 = 0.f;
#pragma unroll
    for (int i = 0; i < 4; ++i) { const float e0 = s[i][0] - m, e1 = s[i][2] - m; M2 += s[i][1] + s[i][3] + 32.0f * (e0 * e0 + e1 * e1); }
    mean[nb] = m; rstd[nb] = __builtin_amdgcn_rsqf(M2 * (1.0f / 256.0f) + LN_EPS);
  }
}
template <int NB>
DI void ln_to_frags(const f32x16 (&v)[NB], LAS char* lds, int w, int lane) {
  float mean[NB], rstd[NB];
  ln_stats<NB>(v, lds, w, lane, mean, rstd);
  LAS char* fb = lds + L_X + w * 2048 + lane * 16;
#pragma unroll
  for (int nb = 0; nb < NB; ++nb) {
    const f32x16 n = (v[nb] - mean[nb]) * rstd[nb];
#pragma unroll
    for (int s = 0; s < 2; ++s) *(LAS bf16x8*)(fb + nb * 16384 + s * 1024) = pack8(n, s);
  }
  lds_barrier();
}

template <int P0, int NP>
DI void ff_block(Stream& st, LAS char* lds, int w, int lane, f32x16 (&out)[1]) {
  out[0] = f32x16{};
  LAS char* xb = lds + L_X + lane * 16;
  LAS char* yb = lds + L_Y + lane * 16;
  LAS char* zb = lds + L_Z + lane * 16;
  auto put = [&](LAS char* dst, const f32x16& hv) { *(LAS bf16x8*)(dst + w * 2048) = pack8(hv, 0); *(LAS bf16x8*)(dst + w * 2048 + 1024) = pack8(hv, 1); };
  f32x16 hn[1];
  hn[0] = f32x16{}; gemm_k256<1, false, P0 + 0, NP>(st, hn, xb, NoWork{});
  put(yb, hn[0]);
  hn[0] = f32x16{}; gemm_k256<1, false, P0 + 1, NP>(st, hn, xb, SiluWork{yb + w * 2048});
  lds_barrier(); put(zb, hn[0]);
  gemm_k256<1, false, P0 + 2, NP>(st, out, yb, NoWork{});
  hn[0] = f32x16{}; gemm_k256<1, false, P0 + 3, NP>(st, hn, xb, SiluWork{zb + w * 2048});
  lds_barrier(); put(yb, hn[0]);
  gemm_k256<1, false, P0 + 4, NP>(st, out, zb, NoWork{});
  hn[0] = f32x16{}; gemm_k256<1, false, P0 + 5, NP>(st, hn, xb, SiluWork{yb + w * 2048});
  lds_barrier(); put(zb, hn[0]);
  gemm_k256<1, false, P0 + 6, NP>(st, out, yb, SiluWork{zb + w * 2048});
  lds_barrier();
  gemm_k256<1, false, P0 + 7, NP>(st, out, zb, NoWork{});
}

DI void l2_warm(const char* p, int kb_total, int idx, int lane, u32x4& t) {
  const int per = (kb_total + 247) / 248;
  int b = idx * per, e = b + per; if (e > kb_total) e = kb_total;
  for (int i = b; i < e; ++i)
    asm volatile("global_load_dwordx4 %0, %1, %2" : "+v"(t) : "v"(lane * 16 + i * 1024), "s"(p));
}

__global__ __launch_bounds__(512, 2) void k1_kernel(const char* __restrict__ x, char* __restrict__ ws) {
  extern __shared__ __attribute__((aligned(16))) char smem[];
  LAS char* lds = (LAS char*)smem;
  const int lane = threadIdx.x & 63, w = __builtin_amdgcn_readfirstlane(threadIdx.x >> 6), r = lane & 31, h = lane >> 5;
  const int tb = blockIdx.x;
  if (tb == 0 && threadIdx.x < 256) *(unsigned*)(ws + WS_FLAG + threadIdx.x * 64) = 0u;
  STAMP(0, 0);
  f32x16 xv[1];
  Stream st;
  u32x4 wt = {0u, 0u, 0u, 0u};
  {
    const char* xrow = x + (size_t)(tb * 32 + r) * 512 + (w * 32 + 4 * h) * 2;
    u32x2 u[4];
#pragma unroll
    for (int g = 0; g < 4; ++g) u[g] = *(const u32x2*)(xrow + g * 16);
    asm volatile("s_barrier" ::: "memory");
    SB();
    st.start(ws + WS_WT1 + (size_t)w * NP1 * 16384 + lane * 16);
    SB();
#pragma unroll
    for (int g = 0; g < 4; ++g) {
      xv[0][4 * g] = bflo(u[g][0]); xv[0][4 * g + 1] = bfhi(u[g][0]); xv[0][4 * g + 2] = bflo(u[g][1]); xv[0][4 * g + 3] = bfhi(u[g][1]);
    }
  }
  STAMP(0, 1);
  ln_to_frags<1>(xv, lds, w, lane);
  STAMP(0, 2);
  f32x16 acc[1];
  ff_block<0, NP1>(st, lds, w, lane, acc);
  asm volatile("" :: "v"(wt));
  STAMP(0, 3);
#pragma unroll
  for (int i = 0; i < 16; ++i) xv[0][i] = rbf(xv[0][i] + 0.5f * acc[0][i]);
#pragma unroll
  for (int s = 0; s < 2; ++s) stg16((char*)(ws + WS_X1F + ((size_t)tb * 16 + 2 * w + s) * 1024 + lane * 16), pack8(xv[0], s));
  ln_to_frags<1>(xv, lds, w, lane);
  STAMP(0, 4);
  const int b = tb / NBLK, blk = tb - b * NBLK, bh = b * 4 + (w >> 1);
  char* kvdst = ws + WS_KVF + (size_t)(bh * NBLK + blk) * 8192 + (w & 1) * 2048 + lane * 16;
  {
    f32x16 a[1]; a[0] = f32x16{};
    gemm_k256<1, false, 8, NP1>(st, a, lds + L_X + lane * 16, NoWork{});
    a[0] = a[0] * QSCALE;
    char* dst = ws + WS_QF + (size_t)(bh * NBLK + blk) * 4096 + (w & 1) * 2048 + lane * 16;
    stg16((char*)(dst), pack8(a[0], 0)); stg16((char*)(dst + 1024), pack8(a[0], 1));
  }
  {
    f32x16 a[1]; a[0] = f32x16{};
    gemm_k256<1, false, 9, NP1>(st, a, lds + L_X + lane * 16, NoWork{});
    stg16((char*)(kvdst), pack8(a[0], 0)); stg16((char*)(kvdst + 1024), pack8(a[0], 1));
  }
  STAMP(0, 5);
  {
    f32x16 a[1]; a[0] = f32x16{};
    gemm_k256<1, true, 10, NP1>(st, a, lds + L_X + lane * 16, NoWork{});
    stg16((char*)(kvdst + 4096), pack8(a[0], 0)); stg16((char*)(kvdst + 5120), pack8(a[0], 1));
  }
  STAMP(0, 29);
}

constexpr int A_KG = 2, A_SBLK = A_KG * 2, A_HST = A_SBLK * 4096, A_NST = (NBLK + A_SBLK - 1) / A_SBLK, A_NFULL = NBLK / A_SBLK;
constexpr int A_VOFF = 3 * A_HST, A_LDS = 6 * A_HST;
DI void attn_body(char* __restrict__ ws, LAS char* lds) {
  const int lane = threadIdx.x & 63, w = __builtin_amdgcn_readfirstlane(threadIdx.x >> 6);
  const int bh = blockIdx.x & 7, qg = blockIdx.x >> 3, qi = w & 3, kg = w >> 2;
  const int qb_raw = qg * 4 + qi, qb = qb_raw < NBLK ? qb_raw : NBLK - 1;
  STAMP(1, 0);
  const char* kv = ws + WS_KVF + (size_t)bh * NBLK * 8192;
  const unsigned lds0 = (unsigned)(uintptr_t)lds;
  const unsigned voff = (w >> 1) * 8192 + (w & 1) * 2048 + lane * 16;
  auto dma2 = [&](int st, int isv, unsigned ldsoff) {
    asm volatile("s_mov_b32 m0, %2\n\ts_nop 0\n\tglobal_load_lds_dwordx4 %0, %1\n\tglobal_load_lds_dwordx4 %0, %1 offset:1024"
                 :: "v"(voff), "s"(kv + (size_t)st * (2 * A_HST) + isv * 4096), "s"(lds0 + ldsoff + w * 2048));
  };
  dma2(0, 0, 0); dma2(1, 0, A_HST); dma2(0, 1, A_VOFF); dma2(2, 0, 2 * A_HST); dma2(1, 1, A_VOFF + A_HST);
  bf16x8 qf[4];
#pragma unroll
  for (int f = 0; f < 4; ++f) qf[f] = *(const bf16x8*)(ws + WS_QF + ((size_t)(bh * NBLK + qb) * 4 + f) * 1024 + lane * 16);
#pragma unroll
  for (int f = 0; f < 4; ++f) asm volatile("" : "+v"(qf[f]));
  u32x4 wt = {0u, 0u, 0u, 0u};
  const int rem = NBLK - A_NFULL * A_SBLK - kg * 2;
  const int ntail = rem < 0 ? 0 : (rem > 2 ? 2 : rem);
  LAS const char* base = lds + kg * 8192 + lane * 16;
  f32x16 o0 = f32x16{}, o1 = f32x16{}, negr, sa, sb;
  f32x4 lacc = {0.f, 0.f, 0.f, 0.f};
  bf16x8 sel;
  { const short one = (((lane >> 4) ^ lane) & 1) ? (short)0 : (short)0x3f80;
#pragma unroll
    for (int j = 0; j < 8; ++j) sel[j] = one; }
  bf16x8 kf[4], vf[4], kg2[4], vg2[4]; unsigned pa[8], pb[8];
#pragma unroll
  for (int i = 0; i < 8; ++i) pa[i] = 0u;
  float ref;
  asm volatile("s_waitcnt vmcnt(0)\n\ts_barrier" : "+v"(wt) :: "memory");
  {
#pragma unroll
    for (int f = 0; f < 4; ++f) kf[f] = ldsfrag(base + f * 1024);
    f32x16 z = f32x16{};
    sa = MFMA(kf[0], qf[0], z); sa = MFMA(kf[1], qf[1], sa); sa = MFMA(kf[2], qf[2], sa); sa = MFMA(kf[3], qf[3], sa);
    float mx = sa[0];
#pragma unroll
    for (int i = 1; i < 16; ++i) mx = fmaxf(mx, sa[i]);
    ref = halfmax(mx) + 7.0f;
#pragma unroll
    for (int i = 0; i < 16; ++i) { sa[i] -= ref; negr[i] = -ref; }
#pragma unroll
    for (int f = 0; f < 4; ++f) { kf[f] = ldsfrag(base + 4096 + f * 1024); vf[f] = bf16x8{}; }
  }
#define SLOT3(MF, RF, i) do { MF; RF; e[2*(i)] = __builtin_amdgcn_exp2f(SC_[2*(i)]); e[2*(i)+1] = __builtin_amdgcn_exp2f(SC_[2*(i)+1]); \
    if ((i) > 0) PN_[(i)-1] = pk2(e[2*(i)-2], e[2*(i)-1]); SB(); } while (0)
  auto iter = [&](f32x16& SC_, f32x16& SN_, unsigned (&PP_)[8], unsigned (&PN_)[8], bf16x8 (&KC)[4], bf16x8 (&VC)[4], bf16x8 (&KN)[4], bf16x8 (&VN)[4], LAS const char* kn, LAS const char* vc) {
    float e[16];
    const bf16x8 p0 = __builtin_bit_cast(bf16x8, (u32x4){PP_[0], PP_[1], PP_[2], PP_[3]}), p1 = __builtin_bit_cast(bf16x8, (u32x4){PP_[4], PP_[5], PP_[6], PP_[7]});
    SB();
    SLOT3(SN_ = MFMA(KC[0], qf[0], negr), KN[0] = ldsfrag(kn), 0);
    SLOT3(o0 = MFMA(VC[0], p0, o0), VN[0] = ldsfrag(vc), 1);
    SLOT3(SN_ = MFMA(KC[1], qf[1], SN_), KN[1] = ldsfrag(kn + 1024), 2);
    SLOT3(o1 = MFMA(VC[2], p0, o1); lacc = MFMA16(sel, p0, lacc), VN[2] = ldsfrag(vc + 2048), 3);
    SLOT3(SN_ = MFMA(KC[2], qf[2], SN_), KN[2] = ldsfrag(kn + 2048), 4);
    SLOT3(o0 = MFMA(VC[1], p1, o0), VN[1] = ldsfrag(vc + 1024), 5);
    SLOT3(SN_ = MFMA(KC[3], qf[3], SN_), KN[3] = ldsfrag(kn + 3072), 6);
    SLOT3(o1 = MFMA(VC[3], p1, o1); lacc = MFMA16(sel, p1, lacc), VN[3] = ldsfrag(vc + 3072), 7);
    PN_[7] = pk2(e[14], e[15]);
    const unsigned orall = (PN_[0] | PN_[1] | PN_[2]) | (PN_[3] | PN_[4] | PN_[5]) | (PN_[6] | PN_[7]);
    if (__builtin_expect(__any((orall & 0x40004000u) != 0u), 0)) {
      float mx = SC_[0];
#pragma unroll
      for (int i = 1; i < 16; ++i) mx = fmaxf(mx, SC_[i]);
      mx = halfmax(mx);
      const float delta = (mx >= 0.5f) ? mx + 7.0f : 0.0f;
      const float alpha = __builtin_amdgcn_exp2f(-delta);
      const float alo = __shfl_xor(alpha, 16);
      const float aA = (lane & 16) ? alo : alpha, aB = (lane & 16) ? alpha : alo;
      lacc = lacc * (f32x4){aA, aB, aA, aB};
      o0 = o0 * alpha; o1 = o1 * alpha; negr = negr - delta; SN_ = SN_ - delta;
      asm volatile("" : "+v"(o0), "+v"(o1), "+v"(negr), "+v"(SN_), "+v"(lacc));
      ref = -negr[0];
#pragma unroll
      for (int i = 0; i < 8; ++i) PN_[i] = pk2(__builtin_amdgcn_exp2f(SC_[2*i] - delta), __builtin_amdgcn_exp2f(SC_[2*i+1] - delta));
    }
  };
  STAMP(1, 1);
  if (w >= 4) __builtin_amdgcn_s_setprio(1);
  int r0 = 0, r1 = A_HST, r2 = 2 * A_HST;
  if (qb_raw < NBLK) {
    for (int st = 0; st < A_NFULL; ++st) {
      asm volatile("s_waitcnt vmcnt(4) lgkmcnt(0)\n\ts_barrier" ::: "memory");
      iter(sa, sb, pa, pb, kf, vf, kg2, vg2, base + r1, base + A_VOFF + r0);
      dma2(st + 3 < A_NST ? st + 3 : A_NST - 1, 0, r0); dma2(st + 2 < A_NST ? st + 2 : A_NST - 1, 1, A_VOFF + r2);
      iter(sb, sa, pb, pa, kg2, vg2, kf, vf, base + r1 + 4096, base + A_VOFF + r0 + 4096);
      const int t = r0; r0 = r1; r1 = r2; r2 = t;
    }
  } else {
    for (int st = 0; st < A_NFULL; ++st) {
      asm volatile("s_waitcnt vmcnt(4) lgkmcnt(0)\n\ts_barrier" ::: "memory");
      dma2(st + 3 < A_NST ? st + 3 : A_NST - 1, 0, r0); dma2(st + 2 < A_NST ? st + 2 : A_NST - 1, 1, A_VOFF + r2);
      const int t = r0; r0 = r1; r1 = r2; r2 = t;
    }
  }
  asm volatile("s_waitcnt vmcnt(0) lgkmcnt(0)\n\ts_barrier" ::: "memory");
  if (ntail > 0) {
    iter(sa, sb, pa, pb, kf, vf, kg2, vg2, base + r1, base + A_VOFF + r0);
    if (ntail > 1) iter(sb, sa, pb, pa, kg2, vg2, kf, vf, base + r1 + 4096, base + A_VOFF + r0 + 4096);
    else {
#pragma unroll
      for (int i = 0; i < 8; ++i) pa[i] = pb[i];
#pragma unroll
      for (int i = 0; i < 4; ++i) vf[i] = vg2[i];
    }
  }
  {
    const bf16x8 p0 = __builtin_bit_cast(bf16x8, (u32x4){pa[0], pa[1], pa[2], pa[3]}), p1 = __builtin_bit_cast(bf16x8, (u32x4){pa[4], pa[5], pa[6], pa[7]});
    o0 = MFMA(vf[0], p0, o0); o1 = MFMA(vf[2], p0, o1); o0 = MFMA(vf[1], p1, o0); o1 = MFMA(vf[3], p1, o1);
    lacc = MFMA16(sel, p0, lacc); lacc = MFMA16(sel, p1, lacc);
  }
  float lsum = (lane & 16) ? lacc[1] : lacc[0];
  __builtin_amdgcn_s_setprio(0);
  STAMP(1, 2);
  lds_barrier();
  if (kg > 0) {
    LAS char* cb = lds + ((kg - 1) * 4 + qi) * 9216 + lane * 16;
#pragma unroll
    for (int g = 0; g < 4; ++g) {
      *(LAS f32x4*)(cb + g * 1024) = (f32x4){o0[4 * g], o0[4 * g + 1], o0[4 * g + 2], o0[4 * g + 3]};
      *(LAS f32x4*)(cb + 4096 + g * 1024) = (f32x4){o1[4 * g], o1[4 * g + 1], o1[4 * g + 2], o1[4 * g + 3]};
    }
    *(LAS f32x2*)(lds + ((kg - 1) * 4 + qi) * 9216 + 8192 + lane * 8) = (f32x2){ref, lsum};
  }
  lds_barrier();
  if (kg == 0 && qb_raw < NBLK) {
#pragma unroll
    for (int k = 1; k < A_KG; ++k) {
      LAS char* cb = lds + ((k - 1) * 4 + qi) * 9216 + lane * 16;
      const f32x2 rl = *(LAS f32x2*)(lds + ((k - 1) * 4 + qi) * 9216 + 8192 + lane * 8);
      const float m = fmaxf(ref, rl[0]);
      const float c0 = __builtin_amdgcn_exp2f(ref - m), c1 = __builtin_amdgcn_exp2f(rl[0] - m);
      lsum = lsum * c0 + rl[1] * c1; ref = m;
#pragma unroll
      for (int g = 0; g < 4; ++g) {
        const f32x4 u0 = *(LAS f32x4*)(cb + g * 1024), u1 = *(LAS f32x4*)(cb + 4096 + g * 1024);
#pragma unroll
        for (int j = 0; j < 4; ++j) { o0[4 * g + j] = o0[4 * g + j] * c0 + u0[j] * c1; o1[4 * g + j] = o1[4 * g + j] * c0 + u1[j] * c1; }
      }
    }
    const float inv = 1.0f / lsum;
#pragma unroll
    for (int i = 0; i < 16; ++i) { o0[i] *= inv; o1[i] *= inv; }
    char* dst = ws + WS_ATTF + ((size_t)((bh >> 2) * NBLK + qb) * 16 + (bh & 3) * 4) * 1024 + lane * 16;
    const bf16x8 f0 = pack8(o0, 0), f1 = pack8(o0, 1), f2 = pack8(o1, 0), f3 = pack8(o1, 1);
    asm volatile("global_store_dwordx4 %0, %1, off sc1\n\tglobal_store_dwordx4 %0, %2, off offset:1024 sc1\n\t"
                 "global_store_dwordx4 %0, %3, off offset:2048 sc1\n\tglobal_store_dwordx4 %0, %4, off offset:3072 sc1\n\ts_nop 1"
                 :: "v"(dst), "v"(f0), "v"(f1), "v"(f2), "v"(f3) : "memory");
  }
  asm volatile("s_waitcnt vmcnt(0) lgkmcnt(0)\n\ts_barrier" ::: "memory");
  if (threadIdx.x == 0) __hip_atomic_store((unsigned*)(ws + WS_FLAG + blockIdx.x * 64), 1u, __ATOMIC_RELAXED, __HIP_MEMORY_SCOPE_AGENT);
  STAMP(1, 29);
}

__global__ __launch_bounds__(512, 2) void k23_kernel(const char* __restrict__ dw, char* __restrict__ ws, char* __restrict__ out) {
  extern __shared__ __attribute__((aligned(16))) char smem[];
  LAS char* lds = (LAS char*)smem;
  attn_body(ws, lds);
  if (blockIdx.x >= NTB) return;
  const int lane = threadIdx.x & 63, w = __builtin_amdgcn_readfirstlane(threadIdx.x >> 6), r = lane & 31, h = lane >> 5;
  const int tb = blockIdx.x, b = tb / NBLK, t0 = (tb - b * NBLK) * 32;
  STAMP(2, 0);
  Stream st;
  const int hc = r & 3, ht = (hc < 2) ? t0 - 2 + hc : t0 + 30 + hc;
  const bool hvalid = ht >= 0 && ht < TSEQ;
  const int hg = b * TSEQ + (ht < 0 ? 0 : (ht >= TSEQ ? TSEQ - 1 : ht));
  const unsigned hoff = (unsigned)(hg >> 5) * 16384 + ((hg & 31) + 32 * h) * 16 + w * 2048;
  const unsigned moff = (unsigned)tb * 16384 + lane * 16 + w * 2048;
  u32x4 xr[2][2];
#pragma unroll
  for (int f = 0; f < 2; ++f) {
    xr[0][f] = *(const u32x4*)(ws + WS_X1F + moff + f * 1024);
    xr[1][f] = *(const u32x4*)(ws + WS_X1F + hoff + f * 1024);
  }
  const u32x4 dwv = *(const u32x4*)(dw + (threadIdx.x < 160 ? threadIdx.x : 159) * 16);
  SB();
  st.start(ws + WS_WT3 + (size_t)w * NP3 * 16384 + lane * 16);
  SB();
  if (threadIdx.x < 160) {
    *(LAS f32x4*)(lds + L_DW + threadIdx.x * 32) = (f32x4){bflo(dwv[0]), bfhi(dwv[0]), bflo(dwv[1]), bfhi(dwv[1])};
    *(LAS f32x4*)(lds + L_DW + threadIdx.x * 32 + 16) = (f32x4){bflo(dwv[2]), bfhi(dwv[2]), bflo(dwv[3]), bfhi(dwv[3])};
  }
  {
    const int blk = tb - b * NBLK;
    int q = blk + (lane & 3) - 1; q = q < 0 ? 0 : (q >= NBLK ? NBLK - 1 : q);
    const unsigned* fp = (const unsigned*)(ws + WS_FLAG + (size_t)((q >> 2) * 8 + b * 4 + (w >> 1)) * 64);
    for (int spin = 0; spin < (1 << 16); ++spin) {
      const unsigned v = (lane < 3) ? __hip_atomic_load(fp, __ATOMIC_RELAXED, __HIP_MEMORY_SCOPE_AGENT) : 1u;
      if (__all(v == 1u)) break;
      __builtin_amdgcn_s_sleep(1);
    }
    __builtin_amdgcn_fence(__ATOMIC_ACQUIRE, "wavefront");
  }
  {
    const unsigned ly = (unsigned)(uintptr_t)(lds + L_Y + w * 2048);
    asm volatile("s_mov_b32 m0, %3\n\ts_nop 0\n\tglobal_load_lds_dwordx4 %0, %2 sc1\n\tglobal_load_lds_dwordx4 %0, %2 offset:1024 sc1\n\t"
                 "s_mov_b32 m0, %4\n\ts_nop 0\n\tglobal_load_lds_dwordx4 %1, %2 sc1\n\tglobal_load_lds_dwordx4 %1, %2 offset:1024 sc1\n\t"
                 "s_waitcnt vmcnt(0)"
                 :: "v"(moff), "v"(hoff), "s"(ws + WS_ATTF), "s"(ly), "s"(ly + 16384) : "memory");
  }
  lds_barrier();
  STAMP(2, 1);
  f32x16 xv[2];
#pragma unroll
  for (int nb = 0; nb < 2; ++nb) { unpack8(xr[nb][0], xv[nb], 0); unpack8(xr[nb][1], xv[nb], 1); }
  gemm_k256<2, false, 0, NP3>(st, xv, lds + L_Y + lane * 16, NoWork{});
  STAMP(2, 2);
  ln_to_frags<2>(xv, lds, w, lane);
  STAMP(2, 3);
  {
    f32x16 a[2], g[2];
#pragma unroll
    for (int nb = 0; nb < 2; ++nb) { a[nb] = f32x16{}; g[nb] = f32x16{}; }
    gemm_k256<2, false, 1, NP3>(st, a, lds + L_X + lane * 16, NoWork{});
    gemm_k256<2, false, 2, NP3>(st, g, lds + L_X + lane * 16, NoWork{});
    STAMP(2, 4);
    LAS char* G = lds + L_Y + w * GSZ;
    lds_barrier();
#pragma unroll
    for (int nb = 0; nb < 2; ++nb) {
      const int row = nb == 0 ? r + 2 : (hc < 2 ? hc : 32 + hc);
      const bool wr = nb == 0 || r < 4;
      const float keep = (nb == 0 || hvalid) ? 1.0f : 0.0f;
#pragma unroll
      for (int q = 0; q < 4; ++q) {
        f32x4 e;
#pragma unroll
        for (int j = 0; j < 4; ++j) e[j] = keep * a[nb][4 * q + j] * sigmoidf_(g[nb][4 * q + j]);
        if (wr) *(LAS f32x4*)(G + row * GROW + (8 * q + 4 * h) * 4) = e;
      }
    }
    asm volatile("s_waitcnt lgkmcnt(0)" ::: "memory");
    f32x16 c;
#pragma unroll
    for (int q = 0; q < 4; ++q) {
      f32x4 e = {0.f, 0.f, 0.f, 0.f};
#pragma unroll
      for (int i = 0; i < 5; ++i)
        e += *(LAS f32x4*)(G + (r + i) * GROW + (8 * q + 4 * h) * 4) * *(LAS f32x4*)(lds + L_DW + i * 1024 + (w * 32 + 8 * q + 4 * h) * 4);
#pragma unroll
      for (int j = 0; j < 4; ++j) c[4 * q + j] = siluf_(e[j]);
    }
#pragma unroll
    for (int s = 0; s < 2; ++s) *(LAS bf16x8*)(lds + L_X + (2 * w + s) * 1024 + lane * 16) = pack8(c, s);
    lds_barrier();
  }
  STAMP(2, 5);
  f32x16 x3[1];
  x3[0] = xv[0];
  gemm_k256<1, false, 3, NP3>(st, x3, lds + L_X + lane * 16, NoWork{});
  STAMP(2, 6);
  ln_to_frags<1>(x3, lds, w, lane);
  STAMP(2, 7);
  f32x16 acc[1];
  ff_block<4, NP3>(st, lds, w, lane, acc);
  x3[0] += 0.5f * acc[0];
  STAMP(2, 8);
  float mean[1], rstd[1];
  ln_stats<1>(x3, lds, w, lane, mean, rstd);
  char* orow = out + (size_t)(tb * 32 + r) * 512 + (w * 32 + 4 * h) * 2;
  const f32x16 n = (x3[0] - mean[0]) * rstd[0];
#pragma unroll
  for (int q = 0; q < 4; ++q) *(u32x2*)(orow + q * 16) = (u32x2){pk2(n[4 * q], n[4 * q + 1]), pk2(n[4 * q + 2], n[4 * q + 3])};
  STAMP(2, 29);
}

extern "C" void kernel_launch(void* const* d_in, const int* in_sizes, int n_in, void* d_out, int out_size, void* d_ws, size_t ws_size,
                              hipStream_t stream) {
  const char* x = (const char*)d_in[0];
  const char* w1a = (const char*)d_in[1];
  const char* w1b = (const char*)d_in[2];
  const char* wqkv = (const char*)d_in[3];
  const char* wo = (const char*)d_in[4];
  const char* pw1 = (const char*)d_in[5];
  const char* dw = (const char*)d_in[6];
  const char* pw2 = (const char*)d_in[7];
  const char* w2a = (const char*)d_in[8];
  const char* w2b = (const char*)d_in[9];
  char* ws = (char*)d_ws;
  (void)hipFuncSetAttribute((const void*)k1_kernel, hipFuncAttributeMaxDynamicSharedMemorySize, L_TOTAL);
  (void)hipFuncSetAttribute((const void*)k23_kernel, hipFuncAttributeMaxDynamicSharedMemorySize, A_LDS);
  WPtrs wp{w1a, w1b, wqkv, wo, pw1, pw2, w2a, w2b};
  prep_kernel<<<(NP1 + NP3) * 8, 256, 0, stream>>>(wp, ws);
  k1_kernel<<<NTB, 512, L_TOTAL, stream>>>(x, ws);
  k23_kernel<<<256, 512, A_LDS, stream>>>(dw, ws, (char*)d_out);
}
```

```cpp
#include <hip/hip_runtime.h>
#include <hip/hip_bf16.h>
#include <stdint.h>

typedef short bf16x8 __attribute__((ext_vector_type(8)));
typedef short s16x4 __attribute__((ext_vector_type(4)));
typedef float f32x16 __attribute__((ext_vector_type(16)));
typedef float f32x2 __attribute__((ext_vector_type(2)));
typedef __bf16 bf16x2_t __attribute__((ext_vector_type(2)));
typedef unsigned u32x4 __attribute__((ext_vector_type(4)));
typedef unsigned u32x2 __attribute__((ext_vector_type(2)));
#define LAS __attribute__((address_space(3)))
#define DI __device__ __forceinline__
#define MFMA(a, b, c) __builtin_amdgcn_mfma_f32_32x32x16_bf16((a), (b), (c), 0, 0, 0)
#define MFMA16(a, b, c) __builtin_amdgcn_mfma_f32_16x16x32_bf16((a), (b), (c), 0, 0, 0)
#define SB() __builtin_amdgcn_sched_barrier(0)
typedef float f32x4 __attribute__((ext_vector_type(4)));

constexpr int TSEQ = 4000, NBLK = 125, NTB = 250;
constexpr size_t WS_X1F = 0;
constexpr size_t WS_QF = 4u << 20;
constexpr size_t WS_KVF = 8u << 20;
constexpr size_t WS_ATTF = 17u << 20;
constexpr size_t WS_FLAG = 22u << 20;
constexpr float LN_EPS = 1e-5f;
constexpr float LOG2E = 1.4426950408889634f;
constexpr float QSCALE = 0.125f * LOG2E;

DI unsigned pk2(float a, float b) { f32x2 v = {a, b}; return __builtin_bit_cast(unsigned, __builtin_convertvector(v, bf16x2_t)); }
DI float bflo(unsigned u) { return __uint_as_float(u << 16); }
DI float bfhi(unsigned u) { return __uint_as_float(u & 0xffff0000u); }
DI float rbf(float a) { return bflo(pk2(a, a)); }
DI bf16x8 pack8(const f32x16& v, int s) {
  u32x4 p = {pk2(v[8 * s], v[8 * s + 1]), pk2(v[8 * s + 2], v[8 * s + 3]), pk2(v[8 * s + 4], v[8 * s + 5]), pk2(v[8 * s + 6], v[8 * s + 7])};
  return __builtin_bit_cast(bf16x8, p);
}
DI void unpack8(u32x4 p, f32x16& v, int s) {
#pragma unroll
  for (int i = 0; i < 4; ++i) { v[8 * s + 2 * i] = bflo(p[i]); v[8 * s + 2 * i + 1] = bfhi(p[i]); }
}
DI float halfsum(float x) {
  auto r = __builtin_amdgcn_permlane32_swap(__float_as_uint(x), __float_as_uint(x), false, false);
  return __uint_as_float(r[0]) + __uint_as_float(r[1]);
}
DI float halfmax(float x) {
  auto r = __builtin_amdgcn_permlane32_swap(__float_as_uint(x), __float_as_uint(x), false, false);
  return fmaxf(__uint_as_float(r[0]), __uint_as_float(r[1]));
}
DI float sigmoidf_(float x) { return __builtin_amdgcn_rcpf(1.0f + __builtin_amdgcn_exp2f(-LOG2E * x)); }
DI float siluf_(float x) { return x * sigmoidf_(x); }
DI void lds_barrier() { asm volatile("s_waitcnt lgkmcnt(0)\n\ts_barrier" ::: "memory"); }
typedef short v4i16_t __attribute__((ext_vector_type(4)));
DI s16x4 trread(LAS const char* p) { return __builtin_bit_cast(s16x4, __builtin_amdgcn_ds_read_tr16_b64_v4i16((LAS v4i16_t*)p)); }
DI void stg16(char* p, bf16x8 v) { __builtin_nontemporal_store(v, (bf16x8*)p); }
DI bf16x8 ldsfrag(LAS const char* p) { return *(LAS const bf16x8*)p; }

constexpr size_t WS_WT1 = 24u << 20, WS_WT3 = 26u << 20;
constexpr int NP1 = 11, NP3 = 12;
struct WPtrs { const char *w1a, *w1b, *wqkv, *wo, *pw1, *pw2, *w2a, *w2b; };
DI void ff_panel(const char* wa, const char* wb, int p, int w2, const char*& base, unsigned& ld) {
  const bool isb = (p == 2 || p == 4 || p >= 6);
  const int c = p < 2 ? p : (p == 3 ? 2 : (p == 5 ? 3 : (p == 2 ? 0 : (p == 4 ? 1 : p - 4))));
  if (isb) { base = wb + (size_t)c * 256 * 512 + w2 * 128; ld = 512; } else { base = wa + (size_t)(w2 + 4 * c) * 128; ld = 2048; }
}
__global__ __launch_bounds__(256) void prep_kernel(WPtrs wp, char* __restrict__ ws) {
  __shared__ __attribute__((aligned(16))) char sm[4 * 4096];
  const int lane = threadIdx.x & 63, wv = __builtin_amdgcn_readfirstlane(threadIdx.x >> 6);
  const int g = blockIdx.x * 4 + wv;
  const bool k3 = g >= 4 * NP1 * 8;
  const int gg = k3 ? g - 4 * NP1 * 8 : g, np = k3 ? NP3 : NP1;
  const int w2 = gg / (np * 8), p = (gg >> 3) % np, sl = gg & 7;
  const char* base; unsigned ld;
  if (!k3) {
    if (p < 8) ff_panel(wp.w1a, wp.w1b, p, w2, base, ld);
    else { base = wp.wqkv + (p - 8) * 512 + w2 * 128; ld = 1536; }
  } else {
    if (p == 0) { base = wp.wo + w2 * 128; ld = 512; }
    else if (p < 3) { base = wp.pw1 + (p - 1) * 512 + w2 * 128; ld = 1024; }
    else if (p == 3) { base = wp.pw2 + w2 * 128; ld = 512; }
    else ff_panel(wp.w2a, wp.w2b, p - 4, w2, base, ld);
  }
  LAS char* my = (LAS char*)sm + wv * 4096;
  const char* src = base + (size_t)(sl * 32 + (lane >> 3)) * ld + (((lane & 7) ^ (((lane >> 4) & 1) << 2)) << 4);
  u32x4 t[4];
#pragma unroll
  for (int i = 0; i < 4; ++i) t[i] = *(const u32x4*)(src + (size_t)i * 8 * ld);
#pragma unroll
  for (int i = 0; i < 4; ++i) *(LAS u32x4*)(my + i * 1024 + lane * 16) = t[i];
  asm volatile("s_waitcnt lgkmcnt(0)" ::: "memory");
  const int h = lane >> 5, cb = (lane >> 4) & 1, q = (lane & 15) >> 2, pp = lane & 3;
  char* dst = ws + (k3 ? WS_WT3 : WS_WT1) + (size_t)((2 * w2) * np + p) * 16384 + sl * 2048 + lane * 16;
#pragma unroll
  for (int i = 0; i < 4; ++i) {
    LAS const char* rp = my + (4 * h + q) * 128 + (((i & 1) ^ (q >> 1)) * 64) + cb * 32 + pp * 8 + (i >> 1) * 2048;
    s16x4 l = trread(rp), hh = trread(rp + 1024);
    *(bf16x8*)(dst + (size_t)(i & 1) * np * 16384 + (i >> 1) * 1024) = __builtin_shufflevector(l, hh, 0, 1, 2, 3, 4, 5, 6, 7);
  }
}

struct Stream {
  const char* p;
  bf16x8 wf[2][16];
  DI void start(const char* base) {
    p = base;
#pragma unroll
    for (int i = 0; i < 16; ++i) wf[0][i] = *(const bf16x8*)(p + i * 1024);
  }
};
struct NoWork { DI void init() {} DI void step(int) {} };
struct SiluWork {
  LAS char* p;
  u32x2 raw; float x[4], e[4];
  DI void init() { raw = *(LAS u32x2*)p; }
  DI void step(int ks) {
    const int g = ks >> 2, i = ks & 3;
    if (i == 0) {
      x[0] = bflo(raw[0]); x[1] = bfhi(raw[0]); x[2] = bflo(raw[1]); x[3] = bfhi(raw[1]);
#pragma unroll
      for (int j = 0; j < 4; ++j) e[j] = -LOG2E * x[j];
      asm volatile("" : "+v"(e[0]), "+v"(e[1]), "+v"(e[2]), "+v"(e[3]), "+v"(x[0]), "+v"(x[1]), "+v"(x[2]), "+v"(x[3]));
      if (g < 3) raw = *(LAS u32x2*)(p + ((g + 1) >> 1) * 1024 + ((g + 1) & 1) * 8);
    } else if (i == 1) {
#pragma unroll
      for (int j = 0; j < 4; ++j) e[j] = __builtin_amdgcn_exp2f(e[j]);
      asm volatile("" : "+v"(e[0]), "+v"(e[1]), "+v"(e[2]), "+v"(e[3]));
    } else if (i == 2) {
#pragma unroll
      for (int j = 0; j < 4; ++j) e[j] = __builtin_amdgcn_rcpf(1.0f + e[j]);
      asm volatile("" : "+v"(e[0]), "+v"(e[1]), "+v"(e[2]), "+v"(e[3]));
    } else {
      *(LAS u32x2*)(p + (g >> 1) * 1024 + (g & 1) * 8) = (u32x2){pk2(x[0] * e[0], x[1] * e[1]), pk2(x[2] * e[2], x[3] * e[3])};
    }
  }
};
template <int NB, bool SWAP, int P, int NP, class W>
DI void gemm_k256(Stream& st, f32x16 (&acc)[NB], LAS const char* bl, W work) {
  constexpr bool REFILL = P + 2 < NP;
  bf16x8 bq[3][NB];
#pragma unroll
  for (int nb = 0; nb < NB; ++nb) { bq[0][nb] = ldsfrag(bl + nb * 16384); bq[1][nb] = ldsfrag(bl + nb * 16384 + 1024); }
  work.init();
#pragma unroll
  for (int ks = 0; ks < 16; ++ks) {
    SB();
#pragma unroll
    for (int nb = 0; nb < NB; ++nb)
      acc[nb] = SWAP ? MFMA(bq[ks % 3][nb], st.wf[P & 1][ks], acc[nb]) : MFMA(st.wf[P & 1][ks], bq[ks % 3][nb], acc[nb]);
    if (ks < 14) {
#pragma unroll
      for (int nb = 0; nb < NB; ++nb) bq[(ks + 2) % 3][nb] = ldsfrag(bl + nb * 16384 + (ks + 2) * 1024);
    }
    if (P == 0) st.wf[1][ks] = *(const bf16x8*)(st.p + 16384 + ks * 1024);
    if (REFILL && ks > 0) st.wf[P & 1][ks - 1] = *(const bf16x8*)(st.p + (P + 2) * 16384 + (ks - 1) * 1024);
    work.step(ks);
  }
#pragma unroll
  for (int nb = 0; nb < NB; ++nb) asm volatile("" : "+v"(acc[nb]));
  SB();
  if (REFILL) st.wf[P & 1][15] = *(const bf16x8*)(st.p + (P + 2) * 16384 + 15 * 1024);
}

constexpr int L_X = 0, L_Y = L_X + 16384, L_Z = L_Y + 16384;
constexpr int L_STAT = L_Z + 16384 + 9216;
constexpr int GROW = 144, GSZ = 36 * GROW;
constexpr int L_DW = L_STAT + 4096;
constexpr int L_TOTAL = L_DW + 5120;

DI float sum16(const f32x16& t) {
  return (((t[0] + t[1]) + (t[2] + t[3])) + ((t[4] + t[5]) + (t[6] + t[7]))) + (((t[8] + t[9]) + (t[10] + t[11])) + ((t[12] + t[13]) + (t[14] + t[15])));
}
template <int NB>
DI void ln_stats(const f32x16 (&v)[NB], LAS char* lds, int w, int lane, float (&mean)[NB], float (&rstd)[NB]) {
  const int r = lane & 31;
#pragma unroll
  for (int nb = 0; nb < NB; ++nb) {
    const float mw = halfsum(sum16(v[nb])) * (1.0f / 32.0f);
    const f32x16 d = v[nb] - mw;
    const float m2 = halfsum(sum16(d * d));
    if (lane < 32) *(LAS f32x2*)(lds + L_STAT + ((nb * 32 + r) * 8 + w) * 8) = (f32x2){mw, m2};
  }
  lds_barrier();
#pragma unroll
  for (int nb = 0; nb < NB; ++nb) {
    f32x4 s[4];
#pragma unroll
    for (int i = 0; i < 4; ++i) s[i] = *(LAS f32x4*)(lds + L_STAT + (nb * 32 + r) * 64 + i * 16);
    const float m = 0.125f * (((s[0][0] + s[0][2]) + (s[1][0] + s[1][2])) + ((s[2][0] + s[2][2]) + (s[3][0] + s[3][2])));
    float M2 = 0.f;
#pragma unroll
    for (int i = 0; i < 4; ++i) { const float e0 = s[i][0] - m, e1 = s[i][2] - m; M2 += s[i][1] + s[i][3] + 32.0f * (e0 * e0 + e1 * e1); }
    mean[nb] = m; rstd[nb] = __builtin_amdgcn_rsqf(M2 * (1.0f / 256.0f) + LN_EPS);
  }
}
template <int NB>
DI void ln_to_frags(const f32x16 (&v)[NB], LAS char* lds, int w, int lane) {
  float mean[NB], rstd[NB];
  ln_stats<NB>(v, lds, w, lane, mean, rstd);
  LAS char* fb = lds + L_X + w * 2048 + lane * 16;
#pragma unroll
  for (int nb = 0; nb < NB; ++nb) {
    const f32x16 n = (v[nb] - mean[nb]) * rstd[nb];
#pragma unroll
    for (int s = 0; s < 2; ++s) *(LAS bf16x8*)(fb + nb * 16384 + s * 1024) = pack8(n, s);
  }
  lds_barrier();
}

template <int P0, int NP>
DI void ff_block(Stream& st, LAS char* lds, int w, int lane, f32x16 (&out)[1]) {
  out[0] = f32x16{};
  LAS char* xb = lds + L_X + lane * 16;
  LAS char* yb = lds + L_Y + lane * 16;
  LAS char* zb = lds + L_Z + lane * 16;
  auto put = [&](LAS char* dst, const f32x16& hv) { *(LAS bf16x8*)(dst + w * 2048) = pack8(hv, 0); *(LAS bf16x8*)(dst + w * 2048 + 1024) = pack8(hv, 1); };
  f32x16 hn[1];
  hn[0] = f32x16{}; gemm_k256<1, false, P0 + 0, NP>(st, hn, xb, NoWork{});
  put(yb, hn[0]);
  hn[0] = f32x16{}; gemm_k256<1, false, P0 + 1, NP>(st, hn, xb, SiluWork{yb + w * 2048});
  lds_barrier(); put(zb, hn[0]);
  gemm_k256<1, false, P0 + 2, NP>(st, out, yb, NoWork{});
  hn[0] = f32x16{}; gemm_k256<1, false, P0 + 3, NP>(st, hn, xb, SiluWork{zb + w * 2048});
  lds_barrier(); put(yb, hn[0]);
  gemm_k256<1, false, P0 + 4, NP>(st, out, zb, NoWork{});
  hn[0] = f32x16{}; gemm_k256<1, false, P0 + 5, NP>(st, hn, xb, SiluWork{yb + w * 2048});
  lds_barrier(); put(zb, hn[0]);
  gemm_k256<1, false, P0 + 6, NP>(st, out, yb, SiluWork{zb + w * 2048});
  lds_barrier();
  gemm_k256<1, false, P0 + 7, NP>(st, out, zb, NoWork{});
}

__global__ __launch_bounds__(512, 2) void k1_kernel(const char* __restrict__ x, char* __restrict__ ws) {
  extern __shared__ __attribute__((aligned(16))) char smem[];
  LAS char* lds = (LAS char*)smem;
  const int lane = threadIdx.x & 63, w = __builtin_amdgcn_readfirstlane(threadIdx.x >> 6), r = lane & 31, h = lane >> 5;
  const int tb = blockIdx.x;
  if (tb == 0 && threadIdx.x < 256) *(unsigned*)(ws + WS_FLAG + threadIdx.x * 64) = 0u;
  f32x16 xv[1];
  Stream st;
  {
    const char* xrow = x + (size_t)(tb * 32 + r) * 512 + (w * 32 + 4 * h) * 2;
    u32x2 u[4];
#pragma unroll
    for (int g = 0; g < 4; ++g) u[g] = *(const u32x2*)(xrow + g * 16);
    asm volatile("s_barrier" ::: "memory");
    SB();
    st.start(ws + WS_WT1 + (size_t)w * NP1 * 16384 + lane * 16);
    SB();
#pragma unroll
    for (int g = 0; g < 4; ++g) {
      xv[0][4 * g] = bflo(u[g][0]); xv[0][4 * g + 1] = bfhi(u[g][0]); xv[0][4 * g + 2] = bflo(u[g][1]); xv[0][4 * g + 3] = bfhi(u[g][1]);
    }
  }
  ln_to_frags<1>(xv, lds, w, lane);
  f32x16 acc[1];
  ff_block<0, NP1>(st, lds, w, lane, acc);
#pragma unroll
  for (int i = 0; i < 16; ++i) xv[0][i] = rbf(xv[0][i] + 0.5f * acc[0][i]);
#pragma unroll
  for (int s = 0; s < 2; ++s) stg16((char*)(ws + WS_X1F + ((size_t)tb * 16 + 2 * w + s) * 1024 + lane * 16), pack8(xv[0], s));
  ln_to_frags<1>(xv, lds, w, lane);
  const int b = tb / NBLK, blk = tb - b * NBLK, bh = b * 4 + (w >> 1);
  char* kvdst = ws + WS_KVF + (size_t)(bh * NBLK + blk) * 8192 + (w & 1) * 2048 + lane * 16;
  {
    f32x16 a[1]; a[0] = f32x16{};
    gemm_k256<1, false, 8, NP1>(st, a, lds + L_X + lane * 16, NoWork{});
    a[0] = a[0] * QSCALE;
    char* dst = ws + WS_QF + (size_t)(bh * NBLK + blk) * 4096 + (w & 1) * 2048 + lane * 16;
    stg16((char*)(dst), pack8(a[0], 0)); stg16((char*)(dst + 1024), pack8(a[0], 1));
  }
  {
    f32x16 a[1]; a[0] = f32x16{};
    gemm_k256<1, false, 9, NP1>(st, a, lds + L_X + lane * 16, NoWork{});
    stg16((char*)(kvdst), pack8(a[0], 0)); stg16((char*)(kvdst + 1024), pack8(a[0], 1));
  }
  {
    f32x16 a[1]; a[0] = f32x16{};
    gemm_k256<1, true, 10, NP1>(st, a, lds + L_X + lane * 16, NoWork{});
    stg16((char*)(kvdst + 4096), pack8(a[0], 0)); stg16((char*)(kvdst + 5120), pack8(a[0], 1));
  }
}

constexpr int A_KG = 2, A_SBLK = A_KG * 2, A_HST = A_SBLK * 4096, A_NST = (NBLK + A_SBLK - 1) / A_SBLK, A_NFULL = NBLK / A_SBLK;
constexpr int A_VOFF = 3 * A_HST, A_LDS = 6 * A_HST;
DI void attn_body(char* __restrict__ ws, LAS char* lds) {
  const int lane = threadIdx.x & 63, w = __builtin_amdgcn_readfirstlane(threadIdx.x >> 6);
  const int bh = blockIdx.x & 7, qg = blockIdx.x >> 3, qi = w & 3, kg = w >> 2;
  const int qb_raw = qg * 4 + qi, qb = qb_raw < NBLK ? qb_raw : NBLK - 1;
  const char* kv = ws + WS_KVF + (size_t)bh * NBLK * 8192;
  const unsigned lds0 = (unsigned)(uintptr_t)lds;
  const unsigned voff = (w >> 1) * 8192 + (w & 1) * 2048 + lane * 16;
  auto dma2 = [&](int st, int isv, unsigned ldsoff) {
    asm volatile("s_mov_b32 m0, %2\n\ts_nop 0\n\tglobal_load_lds_dwordx4 %0, %1\n\tglobal_load_lds_dwordx4 %0, %1 offset:1024"
                 :: "v"(voff), "s"(kv + (size_t)st * (2 * A_HST) + isv * 4096), "s"(lds0 + ldsoff + w * 2048));
  };
  dma2(0, 0, 0); dma2(1, 0, A_HST); dma2(0, 1, A_VOFF); dma2(2, 0, 2 * A_HST); dma2(1, 1, A_VOFF + A_HST);
  bf16x8 qf[4];
#pragma unroll
  for (int f = 0; f < 4; ++f) qf[f] = *(const bf16x8*)(ws + WS_QF + ((size_t)(bh * NBLK + qb) * 4 + f) * 1024 + lane * 16);
#pragma unroll
  for (int f = 0; f < 4; ++f) asm volatile("" : "+v"(qf[f]));
  const int rem = NBLK - A_NFULL * A_SBLK - kg * 2;
  const int ntail = rem < 0 ? 0 : (rem > 2 ? 2 : rem);
  LAS const char* base = lds + kg * 8192 + lane * 16;
  f32x16 o0 = f32x16{}, o1 = f32x16{}, negr, sa, sb;
  f32x4 lacc = {0.f, 0.f, 0.f, 0.f};
  bf16x8 sel;
  { const short one = (((lane >> 4) ^ lane) & 1) ? (short)0 : (short)0x3f80;
#pragma unroll
    for (int j = 0; j < 8; ++j) sel[j] = one; }
  bf16x8 kf[4], vf[4], kg2[4], vg2[4]; unsigned pa[8], pb[8];
#pragma unroll
  for (int i = 0; i < 8; ++i) pa[i] = 0u;
  float ref;
  asm volatile("s_waitcnt vmcnt(0)\n\ts_barrier" ::: "memory");
  {
#pragma unroll
    for (int f = 0; f < 4; ++f) kf[f] = ldsfrag(base + f * 1024);
    f32x16 z = f32x16{};
    sa = MFMA(kf[0], qf[0], z); sa = MFMA(kf[1], qf[1], sa); sa = MFMA(kf[2], qf[2], sa); sa = MFMA(kf[3], qf[3], sa);
    float mx = sa[0];
#pragma unroll
    for (int i = 1; i < 16; ++i) mx = fmaxf(mx, sa[i]);
    ref = halfmax(mx) + 7.0f;
#pragma unroll
    for (int i = 0; i < 16; ++i) { sa[i] -= ref; negr[i] = -ref; }
#pragma unroll
    for (int f = 0; f < 4; ++f) { kf[f] = ldsfrag(base + 4096 + f * 1024); vf[f] = bf16x8{}; }
  }
#define SLOT3(MF, RF, i) do { MF; RF; e[2*(i)] = __builtin_amdgcn_exp2f(SC_[2*(i)]); e[2*(i)+1] = __builtin_amdgcn_exp2f(SC_[2*(i)+1]); \
    if ((i) > 0) PN_[(i)-1] = pk2(e[2*(i)-2], e[2*(i)-1]); SB(); } while (0)
  auto iter = [&](f32x16& SC_, f32x16& SN_, unsigned (&PP_)[8], unsigned (&PN_)[8], bf16x8 (&KC)[4], bf16x8 (&VC)[4], bf16x8 (&KN)[4], bf16x8 (&VN)[4], LAS const char* kn, LAS const char* vc) {
    float e[16];
    const bf16x8 p0 = __builtin_bit_cast(bf16x8, (u32x4){PP_[0], PP_[1], PP_[2], PP_[3]}), p1 = __builtin_bit_cast(bf16x8, (u32x4){PP_[4], PP_[5], PP_[6], PP_[7]});
    SB();
    SLOT3(SN_ = MFMA(KC[0], qf[0], negr), KN[0] = ldsfrag(kn), 0);
    SLOT3(o0 = MFMA(VC[0], p0, o0), VN[0] = ldsfrag(vc), 1);
    SLOT3(SN_ = MFMA(KC[1], qf[1], SN_), KN[1] = ldsfrag(kn + 1024), 2);
    SLOT3(o1 = MFMA(VC[2], p0, o1); lacc = MFMA16(sel, p0, lacc), VN[2] = ldsfrag(vc + 2048), 3);
    SLOT3(SN_ = MFMA(KC[2], qf[2], SN_), KN[2] = ldsfrag(kn + 2048), 4);
    SLOT3(o0 = MFMA(VC[1], p1, o0), VN[1] = ldsfrag(vc + 1024), 5);
    SLOT3(SN_ = MFMA(KC[3], qf[3], SN_), KN[3] = ldsfrag(kn + 3072), 6);
    SLOT3(o1 = MFMA(VC[3], p1, o1); lacc = MFMA16(sel, p1, lacc), VN[3] = ldsfrag(vc + 3072), 7);
    PN_[7] = pk2(e[14], e[15]);
    const unsigned orall = (PN_[0] | PN_[1] | PN_[2]) | (PN_[3] | PN_[4] | PN_[5]) | (PN_[6] | PN_[7]);
    if (__builtin_expect(__any((orall & 0x40004000u) != 0u), 0)) {
      float mx = SC_[0];
#pragma unroll
      for (int i = 1; i < 16; ++i) mx = fmaxf(mx, SC_[i]);
      mx = halfmax(mx);
      const float delta = (mx >= 0.5f) ? mx + 7.0f : 0.0f;
      const float alpha = __builtin_amdgcn_exp2f(-delta);
      const float alo = __shfl_xor(alpha, 16);
      const float aA = (lane & 16) ? alo : alpha, aB = (lane & 16) ? alpha : alo;
      lacc = lacc * (f32x4){aA, aB, aA, aB};
      o0 = o0 * alpha; o1 = o1 * alpha; negr = negr - delta; SN_ = SN_ - delta;
      asm volatile("" : "+v"(o0), "+v"(o1), "+v"(negr), "+v"(SN_), "+v"(lacc));
      ref = -negr[0];
#pragma unroll
      for (int i = 0; i < 8; ++i) PN_[i] = pk2(__builtin_amdgcn_exp2f(SC_[2*i] - delta), __builtin_amdgcn_exp2f(SC_[2*i+1] - delta));
    }
  };
  if (w >= 4) __builtin_amdgcn_s_setprio(1);
  int r0 = 0, r1 = A_HST, r2 = 2 * A_HST;
  if (qb_raw < NBLK) {
    for (int st = 0; st < A_NFULL; ++st) {
      asm volatile("s_waitcnt vmcnt(4) lgkmcnt(0)\n\ts_barrier" ::: "memory");
      iter(sa, sb, pa, pb, kf, vf, kg2, vg2, base + r1, base + A_VOFF + r0);
      dma2(st + 3 < A_NST ? st + 3 : A_NST - 1, 0, r0); dma2(st + 2 < A_NST ? st + 2 : A_NST - 1, 1, A_VOFF + r2);
      iter(sb, sa, pb, pa, kg2, vg2, kf, vf, base + r1 + 4096, base + A_VOFF + r0 + 4096);
      const int t = r0; r0 = r1; r1 = r2; r2 = t;
    }
  } else {
    for (int st = 0; st < A_NFULL; ++st) {
      asm volatile("s_waitcnt vmcnt(4) lgkmcnt(0)\n\ts_barrier" ::: "memory");
      dma2(st + 3 < A_NST ? st + 3 : A_NST - 1, 0, r0); dma2(st + 2 < A_NST ? st + 2 : A_NST - 1, 1, A_VOFF + r2);
      const int t = r0; r0 = r1; r1 = r2; r2 = t;
    }
  }
  asm volatile("s_waitcnt vmcnt(0) lgkmcnt(0)\n\ts_barrier" ::: "memory");
  if (ntail > 0) {
    iter(sa, sb, pa, pb, kf, vf, kg2, vg2, base + r1, base + A_VOFF + r0);
    if (ntail > 1) iter(sb, sa, pb, pa, kg2, vg2, kf, vf, base + r1 + 4096, base + A_VOFF + r0 + 4096);
    else {
#pragma unroll
      for (int i = 0; i < 8; ++i) pa[i] = pb[i];
#pragma unroll
      for (int i = 0; i < 4; ++i) vf[i] = vg2[i];
    }
  }
  {
    const bf16x8 p0 = __builtin_bit_cast(bf16x8, (u32x4){pa[0], pa[1], pa[2], pa[3]}), p1 = __builtin_bit_cast(bf16x8, (u32x4){pa[4], pa[5], pa[6], pa[7]});
    o0 = MFMA(vf[0], p0, o0); o1 = MFMA(vf[2], p0, o1); o0 = MFMA(vf[1], p1, o0); o1 = MFMA(vf[3], p1, o1);
    lacc = MFMA16(sel, p0, lacc); lacc = MFMA16(sel, p1, lacc);
  }
  float lsum = (lane & 16) ? lacc[1] : lacc[0];
  __builtin_amdgcn_s_setprio(0);
  lds_barrier();
  if (kg > 0) {
    LAS char* cb = lds + ((kg - 1) * 4 + qi) * 9216 + lane * 16;
#pragma unroll
    for (int g = 0; g < 4; ++g) {
      *(LAS f32x4*)(cb + g * 1024) = (f32x4){o0[4 * g], o0[4 * g + 1], o0[4 * g + 2], o0[4 * g + 3]};
      *(LAS f32x4*)(cb + 4096 + g * 1024) = (f32x4){o1[4 * g], o1[4 * g + 1], o1[4 * g + 2], o1[4 * g + 3]};
    }
    *(LAS f32x2*)(lds + ((kg - 1) * 4 + qi) * 9216 + 8192 + lane * 8) = (f32x2){ref, lsum};
  }
  lds_barrier();
  if (kg == 0 && qb_raw < NBLK) {
#pragma unroll
    for (int k = 1; k < A_KG; ++k) {
      LAS char* cb = lds + ((k - 1) * 4 + qi) * 9216 + lane * 16;
      const f32x2 rl = *(LAS f32x2*)(lds + ((k - 1) * 4 + qi) * 9216 + 8192 + lane * 8);
      const float m = fmaxf(ref, rl[0]);
      const float c0 = __builtin_amdgcn_exp2f(ref - m), c1 = __builtin_amdgcn_exp2f(rl[0] - m);
      lsum = lsum * c0 + rl[1] * c1; ref = m;
#pragma unroll
      for (int g = 0; g < 4; ++g) {
        const f32x4 u0 = *(LAS f32x4*)(cb + g * 1024), u1 = *(LAS f32x4*)(cb + 4096 + g * 1024);
#pragma unroll
        for (int j = 0; j < 4; ++j) { o0[4 * g + j] = o0[4 * g + j] * c0 + u0[j] * c1; o1[4 * g + j] = o1[4 * g + j] * c0 + u1[j] * c1; }
      }
    }
    const float inv = 1.0f / lsum;
#pragma unroll
    for (int i = 0; i < 16; ++i) { o0[i] *= inv; o1[i] *= inv; }
    char* dst = ws + WS_ATTF + ((size_t)((bh >> 2) * NBLK + qb) * 16 + (bh & 3) * 4) * 1024 + lane * 16;
    const bf16x8 f0 = pack8(o0, 0), f1 = pack8(o0, 1), f2 = pack8(o1, 0), f3 = pack8(o1, 1);
    asm volatile("global_store_dwordx4 %0, %1, off sc1\n\tglobal_store_dwordx4 %0, %2, off offset:1024 sc1\n\t"
                 "global_store_dwordx4 %0, %3, off offset:2048 sc1\n\tglobal_store_dwordx4 %0, %4, off offset:3072 sc1\n\ts_nop 1"
                 :: "v"(dst), "v"(f0), "v"(f1), "v"(f2), "v"(f3) : "memory");
  }
  asm volatile("s_waitcnt vmcnt(0) lgkmcnt(0)\n\ts_barrier" ::: "memory");
  if (threadIdx.x == 0) __hip_atomic_store((unsigned*)(ws + WS_FLAG + blockIdx.x * 64), 1u, __ATOMIC_RELAXED, __HIP_MEMORY_SCOPE_AGENT);
}

__global__ __launch_bounds__(512, 2) void k23_kernel(const char* __restrict__ dw, char* __restrict__ ws, char* __restrict__ out) {
  extern __shared__ __attribute__((aligned(16))) char smem[];
  LAS char* lds = (LAS char*)smem;
  attn_body(ws, lds);
  if (blockIdx.x >= NTB) return;
  const int lane = threadIdx.x & 63, w = __builtin_amdgcn_readfirstlane(threadIdx.x >> 6), r = lane & 31, h = lane >> 5;
  const int tb = blockIdx.x, b = tb / NBLK, t0 = (tb - b * NBLK) * 32;
  Stream st;
  const int hc = r & 3, ht = (hc < 2) ? t0 - 2 + hc : t0 + 30 + hc;
  const bool hvalid = ht >= 0 && ht < TSEQ;
  const int hg = b * TSEQ + (ht < 0 ? 0 : (ht >= TSEQ ? TSEQ - 1 : ht));
  const unsigned hoff = (unsigned)(hg >> 5) * 16384 + ((hg & 31) + 32 * h) * 16 + w * 2048;
  const unsigned moff = (unsigned)tb * 16384 + lane * 16 + w * 2048;
  u32x4 xr[2][2];
#pragma unroll
  for (int f = 0; f < 2; ++f) {
    xr[0][f] = *(const u32x4*)(ws + WS_X1F + moff + f * 1024);
    xr[1][f] = *(const u32x4*)(ws + WS_X1F + hoff + f * 1024);
  }
  const u32x4 dwv = *(const u32x4*)(dw + (threadIdx.x < 160 ? threadIdx.x : 159) * 16);
  SB();
  st.start(ws + WS_WT3 + (size_t)w * NP3 * 16384 + lane * 16);
  SB();
  if (threadIdx.x < 160) {
    *(LAS f32x4*)(lds + L_DW + threadIdx.x * 32) = (f32x4){bflo(dwv[0]), bfhi(dwv[0]), bflo(dwv[1]), bfhi(dwv[1])};
    *(LAS f32x4*)(lds + L_DW + threadIdx.x * 32 + 16) = (f32x4){bflo(dwv[2]), bfhi(dwv[2]), bflo(dwv[3]), bfhi(dwv[3])};
  }
  {
    const int blk = tb - b * NBLK;
    int q = blk + (lane & 3) - 1; q = q < 0 ? 0 : (q >= NBLK ? NBLK - 1 : q);
    const unsigned* fp = (const unsigned*)(ws + WS_FLAG + (size_t)((q >> 2) * 8 + b * 4 + (w >> 1)) * 64);
    for (int spin = 0; spin < (1 << 16); ++spin) {
      const unsigned v = (lane < 3) ? __hip_atomic_load(fp, __ATOMIC_RELAXED, __HIP_MEMORY_SCOPE_AGENT) : 1u;
      if (__all(v == 1u)) break;
      __builtin_amdgcn_s_sleep(1);
    }
    __builtin_amdgcn_fence(__ATOMIC_ACQUIRE, "wavefront");
  }
  {
    const unsigned ly = (unsigned)(uintptr_t)(lds + L_Y + w * 2048);
    asm volatile("s_mov_b32 m0, %3\n\ts_nop 0\n\tglobal_load_lds_dwordx4 %0, %2 sc1\n\tglobal_load_lds_dwordx4 %0, %2 offset:1024 sc1\n\t"
                 "s_mov_b32 m0, %4\n\ts_nop 0\n\tglobal_load_lds_dwordx4 %1, %2 sc1\n\tglobal_load_lds_dwordx4 %1, %2 offset:1024 sc1\n\t"
                 "s_waitcnt vmcnt(0)"
                 :: "v"(moff), "v"(hoff), "s"(ws + WS_ATTF), "s"(ly), "s"(ly + 16384) : "memory");
  }
  lds_barrier();
  f32x16 xv[2];
#pragma unroll
  for (int nb = 0; nb < 2; ++nb) { unpack8(xr[nb][0], xv[nb], 0); unpack8(xr[nb][1], xv[nb], 1); }
  gemm_k256<2, false, 0, NP3>(st, xv, lds + L_Y + lane * 16, NoWork{});
  ln_to_frags<2>(xv, lds, w, lane);
  {
    f32x16 a[2], g[2];
#pragma unroll
    for (int nb = 0; nb < 2; ++nb) { a[nb] = f32x16{}; g[nb] = f32x16{}; }
    gemm_k256<2, false, 1, NP3>(st, a, lds + L_X + lane * 16, NoWork{});
    gemm_k256<2, false, 2, NP3>(st, g, lds + L_X + lane * 16, NoWork{});
    LAS char* G = lds + L_Y + w * GSZ;
    lds_barrier();
#pragma unroll
    for (int nb = 0; nb < 2; ++nb) {
      const int row = nb == 0 ? r + 2 : (hc < 2 ? hc : 32 + hc);
      const bool wr = nb == 0 || r < 4;
      const float keep = (nb == 0 || hvalid) ? 1.0f : 0.0f;
#pragma unroll
      for (int q = 0; q < 4; ++q) {
        f32x4 e;
#pragma unroll
        for (int j = 0; j < 4; ++j) e[j] = keep * a[nb][4 * q + j] * sigmoidf_(g[nb][4 * q + j]);
        if (wr) *(LAS f32x4*)(G + row * GROW + (8 * q + 4 * h) * 4) = e;
      }
    }
    asm volatile("s_waitcnt lgkmcnt(0)" ::: "memory");
    f32x16 c;
#pragma unroll
    for (int q = 0; q < 4; ++q) {
      f32x4 e = {0.f, 0.f, 0.f, 0.f};
#pragma unroll
      for (int i = 0; i < 5; ++i)
        e += *(LAS f32x4*)(G + (r + i) * GROW + (8 * q + 4 * h) * 4) * *(LAS f32x4*)(lds + L_DW + i * 1024 + (w * 32 + 8 * q + 4 * h) * 4);
#pragma unroll
      for (int j = 0; j < 4; ++j) c[4 * q + j] = siluf_(e[j]);
    }
#pragma unroll
    for (int s = 0; s < 2; ++s) *(LAS bf16x8*)(lds + L_X + (2 * w + s) * 1024 + lane * 16) = pack8(c, s);
    lds_barrier();
  }
  f32x16 x3[1];
  x3[0] = xv[0];
  gemm_k256<1, false, 3, NP3>(st, x3, lds + L_X + lane * 16, NoWork{});
  ln_to_frags<1>(x3, lds, w, lane);
  f32x16 acc[1];
  ff_block<4, NP3>(st, lds, w, lane, acc);
  x3[0] += 0.5f * acc[0];
  float mean[1], rstd[1];
  ln_stats<1>(x3, lds, w, lane, mean, rstd);
  char* orow = out + (size_t)(tb * 32 + r) * 512 + (w * 32 + 4 * h) * 2;
  const f32x16 n = (x3[0] - mean[0]) * rstd[0];
#pragma unroll
  for (int q = 0; q < 4; ++q) *(u32x2*)(orow + q * 16) = (u32x2){pk2(n[4 * q], n[4 * q + 1]), pk2(n[4 * q + 2], n[4 * q + 3])};
}

extern "C" void kernel_launch(void* const* d_in, const int* in_sizes, int n_in, void* d_out, int out_size, void* d_ws, size_t ws_size,
                              hipStream_t stream) {
  const char* x = (const char*)d_in[0];
  const char* w1a = (const char*)d_in[1];
  const char* w1b = (const char*)d_in[2];
  const char* wqkv = (const char*)d_in[3];
  const char* wo = (const char*)d_in[4];
  const char* pw1 = (const char*)d_in[5];
  const char* dw = (const char*)d_in[6];
  const char* pw2 = (const char*)d_in[7];
  const char* w2a = (const char*)d_in[8];
  const char* w2b = (const char*)d_in[9];
  char* ws = (char*)d_ws;
  (void)hipFuncSetAttribute((const void*)k1_kernel, hipFuncAttributeMaxDynamicSharedMemorySize, L_TOTAL);
  (void)hipFuncSetAttribute((const void*)k23_kernel, hipFuncAttributeMaxDynamicSharedMemorySize, A_LDS);
  WPtrs wp{w1a, w1b, wqkv, wo, pw1, pw2, w2a, w2b};
  prep_kernel<<<(NP1 + NP3) * 8, 256, 0, stream>>>(wp, ws);
  k1_kernel<<<NTB, 512, L_TOTAL, stream>>>(x, ws);
  k23_kernel<<<256, 512, A_LDS, stream>>>(dw, ws, (char*)d_out);
}
```
